```python
import torch

def _softmax(x, axis=-1):
    return torch.softmax(x, dim=axis)
def _gelu(x):
    return torch.nn.functional.gelu(x, approximate="tanh")
def _silu(x):
    return torch.nn.functional.silu(x)
def _sigmoid(x):
    return torch.sigmoid(x)
def _layernorm(x, axis=-1, eps=1e-5):
    m = x.mean(dim=axis, keepdim=True)
    v = x.var(dim=axis, keepdim=True, unbiased=False)
    return (x - m) / torch.sqrt(v + eps)
def _scatter_add(tgt, idx, src, axis=0):
    return tgt.clone().index_add_(axis, idx, src)
def _segment_sum(src, seg, n):
    out = torch.zeros((n,) + src.shape[1:], dtype=src.dtype)
    return out.index_add_(0, seg, src)
def _pad(x, pw):
    flat = tuple(v for pair in reversed(pw) for v in pair)
    return torch.nn.functional.pad(x, flat)

kernel_name = 'interleave_layers__grouped_kv_mqa__sliding_block__prompt'

def setup_inputs():

    _w = (lambda a: (lambda t: t.to(torch.bfloat16) if t.is_floating_point() else t)(torch.as_tensor(a)))
    def _A_setup():
        B, S, D, H = 1, 2048, 448, 16
        Dh = D // H
        return {
            "x": _w((lambda *s: torch.randn(*s, dtype=torch.bfloat16) * 0.05)(B, S, D)),
            "wq": _w((lambda *s: torch.randn(*s, dtype=torch.bfloat16) * 0.05)(D, H * Dh)),
            "wk": _w((lambda *s: torch.randn(*s, dtype=torch.bfloat16) * 0.05)(D, Dh)),
            "wv": _w((lambda *s: torch.randn(*s, dtype=torch.bfloat16) * 0.05)(D, Dh)),
            "wo": _w((lambda *s: torch.randn(*s, dtype=torch.bfloat16) * 0.05)(H * Dh, D)),
        }
    _A_in = _A_setup()

    def _B_setup():
        B, S, D = 1, 2048, 448
        return {
            "x": _w((lambda *s: torch.randn(*s, dtype=torch.bfloat16) * 0.05)(B, S, D)),
            "wqkv": _w((lambda *s: torch.randn(*s, dtype=torch.bfloat16) * 0.05)(D, 3 * D)),
            "wo": _w((lambda *s: torch.randn(*s, dtype=torch.bfloat16) * 0.05)(D, D)),
        }
    _B_in = _B_setup()

    return {
        "A_x": _A_in["x"],
        "A_wq": _A_in["wq"],
        "A_wk": _A_in["wk"],
        "A_wv": _A_in["wv"],
        "A_wo": _A_in["wo"],
        "B_x": _B_in["x"],
        "B_wqkv": _B_in["wqkv"],
        "B_wo": _B_in["wo"],
    }

def reference(A_x, A_wq, A_wk, A_wv, A_wo, B_x, B_wqkv, B_wo):
    def _A_ref(x, wq, wk, wv, wo):
        B, S, D = x.shape
        H = 16
        Dh = D // H
        q = (lambda x, *d: x.permute(*d))((x @ wq).reshape(B, S, H, Dh), 0, 2, 1, 3)
        k = x @ wk
        v = x @ wv
        scores = torch.einsum("bhqd,bkd->bhqk", q, k) * (Dh ** -0.5)
        p = _softmax(scores, axis=-1)
        o = torch.einsum("bhqk,bkd->bhqd", p, v)
        return (lambda x, *d: x.permute(*d))(o, 0, 2, 1, 3).reshape(B, S, H * Dh) @ wo

    def _B_ref(x, wqkv, wo):
        B, S, D = x.shape
        H = 16; Dh = D // H; W = 256
        qkv = (lambda x, *d: x.permute(*d))((x @ wqkv).reshape(B, S, 3, H, Dh), 2, 0, 3, 1, 4)
        q, k, v = qkv[0], qkv[1], qkv[2]
        scores = torch.einsum("bhqd,bhkd->bhqk", q, k) * (Dh ** -0.5)
        idx = torch.arange(S)
        mask = (idx[:, None] - idx[None, :] >= 0) & (idx[:, None] - idx[None, :] < W)
        scores = torch.where(mask[None, None], scores, -1e9)
        p = _softmax(scores, axis=-1)
        o = torch.einsum("bhqk,bhkd->bhqd", p, v)
        return (lambda x, *d: x.permute(*d))(o, 0, 2, 1, 3).reshape(B, S, D) @ wo

    x = A_x
    x = x + _A_ref(_layernorm(x, axis=-1), A_wq, A_wk, A_wv, A_wo)
    x = x + _B_ref(_layernorm(x, axis=-1), B_wqkv, B_wo)
    x = x + _A_ref(_layernorm(x, axis=-1), A_wq, A_wk, A_wv, A_wo)
    x = x + _B_ref(_layernorm(x, axis=-1), B_wqkv, B_wo)
    return x
```

```cpp
#include <hip/hip_runtime.h>
#include <hip/hip_bf16.h>
#include <stdint.h>

typedef short bf16x8 __attribute__((ext_vector_type(8)));
typedef short s16x4 __attribute__((ext_vector_type(4)));
typedef float f32x16 __attribute__((ext_vector_type(16)));
typedef float f32x4 __attribute__((ext_vector_type(4)));
typedef float f32x2 __attribute__((ext_vector_type(2)));
typedef unsigned u32x4 __attribute__((ext_vector_type(4)));
typedef unsigned u32x2 __attribute__((ext_vector_type(2)));
typedef __bf16 bf16x2_t __attribute__((ext_vector_type(2)));
#define DI __device__ __forceinline__
#define LAS __attribute__((address_space(3)))
#define GAS __attribute__((address_space(1)))
typedef LAS char* ldsp;

#define MFMA(a, b, c) __builtin_amdgcn_mfma_f32_32x32x16_bf16(__builtin_bit_cast(bf16x8, (a)), __builtin_bit_cast(bf16x8, (b)), (c), 0, 0, 0)
#define BAR() asm volatile("s_waitcnt lgkmcnt(0)\n\ts_barrier" ::: "memory")
#define CBAR() asm volatile("" ::: "memory")
#define SBAR() __builtin_amdgcn_sched_barrier(0)

constexpr int S_ = 2048, D_ = 448;
constexpr float C2 = 0.18898223650461362f * 1.4426950408889634f;
constexpr float EPS = 1e-5f;
constexpr unsigned SENT = 0xFFFFFFFFu;
constexpr unsigned ONES2 = 0x3F803F80u;
constexpr int SPIN_CAP = 1 << 14;

constexpr int KB = 1024;
constexpr int OFF_WAT = 0;
constexpr int OFF_WBT = OFF_WAT + 448 * KB;
constexpr int OFF_WOA = OFF_WBT + 1344 * KB;
constexpr int OFF_WOB = OFF_WOA + 448 * KB;
constexpr int OFF_QA = OFF_WOB + 448 * KB;
constexpr int QA_SZ = 64 * 28 * KB;
constexpr int OFF_KVA = OFF_QA + 2 * QA_SZ;
constexpr int KVA_SZ = 256 * KB;
constexpr int OFF_OFA = OFF_KVA + 2 * KVA_SZ;
constexpr int OFF_XF1 = OFF_OFA + 2048 * KB;
constexpr int OFF_KVB = OFF_XF1 + QA_SZ;
constexpr int OFF_OFB = OFF_KVB + 4096 * KB;
constexpr int OFF_XF2 = OFF_OFB + 2048 * KB;
constexpr int OFF_FLG = OFF_XF2 + QA_SZ;
constexpr int OFF_DBG = OFF_FLG + 64 * KB;
constexpr int WS_BYTES = OFF_DBG + 512 * KB;

#define DLY_OA 0
#define DLY_X1 0
#define DLY_KV 0
#define DLY_OB 0
#define DLY_X2 0
#define DLY_WOB 16
#define DLY_WOB2 16
#define POST_OA 1
#define POST_OB 1
#define POST_KV 29
#define POST_GEMM 4

DI unsigned pk(float a, float b) { f32x2 v = {a, b}; bf16x2_t r = __builtin_convertvector(v, bf16x2_t); return __builtin_bit_cast(unsigned, r); }
DI float dot2(unsigned a, unsigned b, float c) { return __builtin_amdgcn_fdot2_f32_bf16(__builtin_bit_cast(bf16x2_t, a), __builtin_bit_cast(bf16x2_t, b), c, false); }
DI float bflo(unsigned u) { return __uint_as_float(u << 16); }
DI float bfhi(unsigned u) { return __uint_as_float(u & 0xFFFF0000u); }
DI float xhalf_add(float v) { auto r = __builtin_amdgcn_permlane32_swap(__float_as_uint(v), __float_as_uint(v), false, false); return __uint_as_float(r[0]) + __uint_as_float(r[1]); }
DI float xhalf_max(float v) { auto r = __builtin_amdgcn_permlane32_swap(__float_as_uint(v), __float_as_uint(v), false, false); return __builtin_fmaxf(__uint_as_float(r[0]), __uint_as_float(r[1])); }
DI float from_hi(float v) { auto r = __builtin_amdgcn_permlane32_swap(__float_as_uint(v), __float_as_uint(v), false, false); return __uint_as_float(r[1]); }
DI float max3f(float a, float b, float c) { return __builtin_fmaxf(__builtin_fmaxf(a, b), c); }
DI float max16(const f32x16& s, float m) {
#pragma unroll
  for (int i = 0; i < 16; i += 2) m = max3f(m, s[i], s[i + 1]);
  return m;
}
typedef __amdgpu_buffer_rsrc_t rsrc_t;
DI rsrc_t mkrs(const void* p, unsigned n) { return __builtin_amdgcn_make_buffer_rsrc((void*)p, 0, n, 0x00020000); }
template <int AUX> DI u32x4 ld16(rsrc_t rs, int voff, int soff) { return __builtin_amdgcn_raw_buffer_load_b128(rs, voff, soff, AUX); }
template <int AUX> DI u32x2 ld8(rsrc_t rs, int voff, int soff) { return __builtin_amdgcn_raw_buffer_load_b64(rs, voff, soff, AUX); }
template <int AUX> DI void st16(u32x4 v, rsrc_t rs, int voff, int soff) { __builtin_amdgcn_raw_buffer_store_b128(v, rs, voff, soff, AUX); }
template <int AUX> DI void st8(u32x2 v, rsrc_t rs, int voff, int soff) { __builtin_amdgcn_raw_buffer_store_b64(v, rs, voff, soff, AUX); }
DI u32x4 lds_r16(ldsp p) { return *(LAS u32x4*)p; }
DI f32x4 lds_rf4(ldsp p) { return *(LAS f32x4*)p; }
DI void lds_w16(ldsp p, u32x4 v) { *(LAS u32x4*)p = v; }
DI void lds_wf4(ldsp p, f32x4 v) { *(LAS f32x4*)p = v; }
DI unsigned umax3(unsigned a, unsigned b, unsigned c) { return max(max(a, b), c); }
DI unsigned smax(unsigned m, u32x4 v) { return umax3(umax3(m, v[0], v[1]), v[2], v[3]); }
DI void glds16(const void* g, unsigned lds_base) {
  unsigned sv;
  asm volatile("s_mov_b32 %0, m0\n\ts_mov_b32 m0, %2\n\ts_nop 0\n\tglobal_load_lds_dwordx4 %1, off\n\ts_mov_b32 m0, %0" : "=&s"(sv) : "v"(g), "s"(lds_base) : "memory");
}
DI u32x4 pack8(const f32x16& a, int s) {
  u32x4 r;
#pragma unroll
  for (int i = 0; i < 4; ++i) r[i] = pk(a[8 * s + 2 * i], a[8 * s + 2 * i + 1]);
  return r;
}
template <int N> DI void ld_handoff(u32x4 (&v)[N], rsrc_t rs, int voff, int soff) {
#pragma unroll
  for (int i = 0; i < N; ++i) v[i] = ld16<16>(rs, voff + (i & 3) * KB, soff + (i >> 2) * 4 * KB);
}
template <int N, int I0, int I1> DI void chk_handoff(u32x4 (&v)[N], rsrc_t rs, int voff, int soff) {
  for (int spin = 0; spin < SPIN_CAP; ++spin) {
    unsigned m = 0;
#pragma unroll
    for (int i = I0; i < I1; ++i) m = smax(m, v[i]);
    if (__builtin_expect(__builtin_amdgcn_ballot_w64(m == SENT) == 0ull, 1)) break;
    CBAR();
    __builtin_amdgcn_s_sleep(2);
#pragma unroll
    for (int i = I0; i < I1; ++i) v[i] = ld16<16>(rs, voff + (i & 3) * KB, soff + (i >> 2) * 4 * KB);
  }
}

#define STAMP(i) do { } while (0)

struct Params {
  const unsigned short *x, *wq, *wk, *wv, *woa, *wqkv, *wob;
  unsigned short* out;
  unsigned char* ws;
  int pair;
  int pad;
};

DI void ln_fix_tok(f32x16& a, ldsp cs, int h, float mu, float sc) {
#pragma unroll
  for (int g = 0; g < 4; ++g) {
    f32x4 c = lds_rf4(cs + (8 * g + 4 * h) * 4);
#pragma unroll
    for (int i = 0; i < 4; ++i) a[4 * g + i] = (a[4 * g + i] - mu * c[i]) * sc;
  }
}
DI void ln_fix_col(f32x16& a, ldsp mup, ldsp rsp, int h, float cs) {
#pragma unroll
  for (int g = 0; g < 4; ++g) {
    f32x4 m = lds_rf4(mup + (8 * g + 4 * h) * 4), r = lds_rf4(rsp + (8 * g + 4 * h) * 4);
#pragma unroll
    for (int i = 0; i < 4; ++i) a[4 * g + i] = (a[4 * g + i] - m[i] * cs) * r[i];
  }
}

constexpr int XI_KS = 1056;
constexpr int P_XI = 0, P_WI = 2 * 28 * XI_KS, P_ST = P_WI + 448 * 128, P_CS = P_ST + 512, P_BYTES = P_CS + 4 * 128;
__global__ void __launch_bounds__(512, 2) k_pre(Params p) {
  __shared__ __attribute__((aligned(16))) char smem[P_BYTES];
  const ldsp lds = (ldsp)smem;
  const int tid = threadIdx.x, lane = tid & 63, r = lane & 31, h = lane >> 5;
  const int w = __builtin_amdgcn_readfirstlane(tid >> 6);
  const int b = blockIdx.x;
  const rsrc_t ws = mkrs(p.ws, WS_BYTES);
  STAMP(0);
  if (w >= 4) {
    if (tid >= 256 && tid < 258) *(unsigned*)(p.ws + OFF_FLG + ((tid - 256) * 256 + b) * 128) = 0u;
    const int gw = b * 4 + (w - 4);
#pragma unroll
    for (int it = 0; it < 3; ++it) {
      const int f = gw + it * 1024;
      if (f >= 2688) break;
      const unsigned short* src; int pitch, k0, kmax = 1 << 20; bool colok = true; unsigned short fillv = 0;
      if (f < 448) {
        const int tile = f / 28, ks = f % 28;
        k0 = 16 * ks + 4 * h;
        if (tile < 14) { src = p.wq + 32 * tile + r; pitch = 448; }
        else { src = (tile == 14 ? p.wk : p.wv) + (r < 28 ? r : 0); pitch = 28; colok = r < 28; }
      } else if (f < 448 + 1344) {
        const int g = f - 448, head = g / 84, which = (g / 28) % 3, ks = g % 28;
        k0 = 16 * ks + 4 * h;
        src = p.wqkv + which * 448 + head * 28 + (r < 28 ? r : 0); pitch = 1344; colok = r < 28;
        fillv = (which == 0 && r == 28) ? (unsigned short)0x3F80 : (unsigned short)0;
      } else {
        int g = f - 448 - 1344; const unsigned short* wo = p.woa;
        if (g >= 448) { g -= 448; wo = p.wob; }
        const int ct = g >> 5, ks = g & 31, head = ks >> 1;
        k0 = head * 28 + 16 * (ks & 1) + 4 * h; kmax = head * 28 + 28;
        src = wo + 32 * ct + r; pitch = 448;
      }
      unsigned short e[8];
#pragma unroll
      for (int j = 0; j < 8; ++j) {
        const int k = k0 + 8 * (j >> 2) + (j & 3);
        const bool ok = colok && k < kmax;
        const unsigned short v = src[(ok ? k : 0) * pitch];
        e[j] = ok ? v : fillv;
      }
      u32x4 o;
#pragma unroll
      for (int i = 0; i < 4; ++i) o[i] = (unsigned)e[2 * i] | ((unsigned)e[2 * i + 1] << 16);
      st16<0>(o, ws, lane * 16, f * KB);
    }
    return;
  }
  const int xcd = b & 7, jj = b >> 3, rb64 = 4 * xcd + (jj >> 3), cp = jj & 7;
  const int q8 = tid & 7, row8 = tid >> 3;
  u32x4 xv[2][7], wv[14];
  {
    const unsigned short* xr = p.x + (size_t)(rb64 * 64 + row8) * D_ + q8 * 8;
#pragma unroll
    for (int ps = 0; ps < 2; ++ps)
#pragma unroll
      for (int i = 0; i < 7; ++i) xv[ps][i] = *(const u32x4*)(xr + ps * 32 * D_ + i * 64);
  }
  if (cp < 7) {
    const unsigned short* wr = p.wq + (size_t)row8 * 448 + cp * 64 + q8 * 8;
#pragma unroll
    for (int i = 0; i < 14; ++i) wv[i] = *(const u32x4*)(wr + i * 32 * 448);
  } else {
    const unsigned short* wr = ((q8 >> 2) ? p.wv : p.wk) + (size_t)row8 * 28 + (q8 & 3) * 8;
    const bool last = (q8 & 3) == 3;
#pragma unroll
    for (int i = 0; i < 14; ++i) {
      const u32x2 a = *(const u32x2*)(wr + i * 32 * 28);
      u32x2 c = *(const u32x2*)(wr + i * 32 * 28 + (last ? 0 : 4));
      if (last) { c[0] = 0; c[1] = 0; }
      wv[i] = u32x4{a[0], a[1], c[0], c[1]};
    }
  }
#pragma unroll
  for (int ps = 0; ps < 2; ++ps) {
    float sm = 0.f, sq = 0.f;
#pragma unroll
    for (int i = 0; i < 7; ++i) {
#pragma unroll
      for (int d = 0; d < 4; ++d) { sm = dot2(xv[ps][i][d], ONES2, sm); sq = dot2(xv[ps][i][d], xv[ps][i][d], sq); }
      const int ks = 4 * i + (q8 >> 1);
      const ldsp dst = lds + P_XI + (ps * 28 + ks) * XI_KS + row8 * 16 + (q8 & 1) * 8;
      *(LAS u32x2*)dst = u32x2{xv[ps][i][0], xv[ps][i][1]};
      *(LAS u32x2*)(dst + 512) = u32x2{xv[ps][i][2], xv[ps][i][3]};
    }
#pragma unroll
    for (int o = 1; o < 8; o <<= 1) { sm += __shfl_xor(sm, o); sq += __shfl_xor(sq, o); }
    const float mu = sm * (1.f / D_), var = __builtin_fmaxf(sq * (1.f / D_) - mu * mu, 0.f);
    if (q8 == 0) { *(LAS float*)(lds + P_ST + (ps * 32 + row8) * 4) = mu; *(LAS float*)(lds + P_ST + 256 + (ps * 32 + row8) * 4) = __builtin_amdgcn_rsqf(var + EPS); }
  }
#pragma unroll
  for (int i = 0; i < 14; ++i) {
    const int k = row8 + 32 * i;
    lds_w16(lds + P_WI + k * 128 + (((q8 >> 2) ^ ((k >> 1) & 1)) * 64) + (q8 & 3) * 16, wv[i]);
  }
  BAR();
  STAMP(1);
  const int rt2 = w >> 1, ct2 = w & 1, tile = 2 * cp + ct2, rt = rb64 * 2 + rt2;
  const bool isV = tile == 15;
  const int i16 = lane & 15, tq = i16 >> 2, tp = i16 & 3, blk = (lane >> 4) & 1;
  const ldsp wbase = lds + P_WI + (4 * h + tq) * 128 + ((ct2 ^ ((tq >> 1) & 1)) * 64) + blk * 32 + tp * 8;
  const ldsp xbase = lds + P_XI + rt2 * 28 * XI_KS + lane * 16;
  f32x16 acc = {};
  float cs = 0.f;
#define PRE_LOOP(MM) \
  _Pragma("unroll") for (int ks = 0; ks < 28; ++ks) { \
    const s16x4 lo = __builtin_amdgcn_ds_read_tr16_b64_v4i16((LAS s16x4*)(wbase + ks * 16 * 128)); \
    const s16x4 hi = __builtin_amdgcn_ds_read_tr16_b64_v4i16((LAS s16x4*)(wbase + ks * 16 * 128 + 8 * 128)); \
    const u32x4 wf = __builtin_bit_cast(u32x4, __builtin_shufflevector(lo, hi, 0, 1, 2, 3, 4, 5, 6, 7)); \
    const u32x4 xf = lds_r16(xbase + ks * XI_KS); \
    _Pragma("unroll") for (int d = 0; d < 4; ++d) cs = dot2(wf[d], ONES2, cs); \
    acc = MM; \
  }
  if (isV) { PRE_LOOP(MFMA(xf, wf, acc)) } else { PRE_LOOP(MFMA(wf, xf, acc)) }
  cs = xhalf_add(cs);
  u32x4 o0, o1;
  if (!isV) {
    if (h == 0) *(LAS float*)(lds + P_CS + w * 128 + r * 4) = cs;
    const float mu = *(LAS float*)(lds + P_ST + (rt2 * 32 + r) * 4), rs = *(LAS float*)(lds + P_ST + 256 + (rt2 * 32 + r) * 4);
    asm volatile("s_waitcnt lgkmcnt(0)" ::: "memory");
    ln_fix_tok(acc, lds + P_CS + w * 128, h, mu, tile < 14 ? rs * C2 : rs);
    o0 = pack8(acc, 0); o1 = pack8(acc, 1);
    if (tile == 14 && h == 1) { o1[2] = ONES2; o1[3] = 0x00003F80u; }
  } else {
    ln_fix_col(acc, lds + P_ST + rt2 * 128, lds + P_ST + 256 + rt2 * 128, h, cs);
    o0 = pack8(acc, 0); o1 = pack8(acc, 1);
    if (r >= 28) { const unsigned f = r == 28 ? ONES2 : 0u; o0 = u32x4{f, f, f, f}; o1 = o0; }
  }
  const int dst = tile < 14 ? OFF_QA + (rt * 14 + tile) * 2 * KB : OFF_KVA + rt * 4 * KB + (tile - 14) * 2 * KB;
  st16<0>(o0, ws, lane * 16, dst);
  st16<0>(o1, ws, lane * 16 + KB, dst);
  STAMP(2);
}

constexpr int L_WB = 0;
constexpr int L_SCR = 84 * KB;
constexpr int L_MISC = L_SCR + 64 * KB;
constexpr int M_BAD = L_MISC;
constexpr int M_CNT = L_MISC + 32;
constexpr int M_CS = L_MISC + 64;
constexpr int M_ST = M_CS + 1024;
constexpr int M_MX = M_ST + 2048;
constexpr int L_BYTES = M_MX + 2048;

DI void exp_pack(const f32x16& s, u32x4& p0, u32x4& p1) {
#pragma unroll
  for (int i = 0; i < 4; ++i) p0[i] = pk(__builtin_amdgcn_exp2f(s[2 * i]), __builtin_amdgcn_exp2f(s[2 * i + 1]));
#pragma unroll
  for (int i = 0; i < 4; ++i) p1[i] = pk(__builtin_amdgcn_exp2f(s[8 + 2 * i]), __builtin_amdgcn_exp2f(s[8 + 2 * i + 1]));
}
DI void set_shift(u32x4& q1, float m, int h, unsigned hi31) {
  const unsigned a = pk(m, m); const float r1 = m - bflo(a);
  const unsigned c = pk(r1, r1); const float r2 = r1 - bflo(c);
  const unsigned e = pk(r2, 0.f);
  const unsigned d2 = ((a & 0xFFFFu) | (c << 16)) ^ 0x80008000u, d3 = (e ^ 0x00008000u) | hi31;
  q1[2] = h ? d2 : q1[2]; q1[3] = h ? d3 : q1[3];
}
template <bool DOQK, bool DOPVP, class FA, class FB, class FC>
DI void unit(const f32x16& s, f32x16& sn, f32x16& o, f32x16& op, u32x4& pp, const u32x4& kn0, const u32x4& kn1, const u32x4& qn0, const u32x4& qn1,
             const u32x4& v0, const u32x4& v1p, FA ha, FB hb, FC hc) {
  float e[16]; u32x4 p0;
#define E_(i) e[i] = __builtin_amdgcn_exp2f(s[i]); asm volatile("" : "+v"(e[i])); SBAR()
#define C0_(i) p0[i] = pk(e[2 * i], e[2 * i + 1]); asm volatile("" : "+v"(p0[i])); SBAR()
#define C1_(i) pp[i] = pk(e[8 + 2 * i], e[8 + 2 * i + 1]); asm volatile("" : "+v"(pp[i])); SBAR()
  SBAR();
  if (DOQK) { sn = MFMA(kn0, qn0, f32x16{}); SBAR(); }
  E_(0); E_(1); E_(2); E_(3);
  if (DOPVP) { op = MFMA(v1p, pp, op); SBAR(); }
  ha(); SBAR();
  E_(4); C0_(0); E_(5); C0_(1); E_(6); E_(7);
  if (DOQK) { sn = MFMA(kn1, qn1, sn); SBAR(); }
  hb(); SBAR();
  E_(8); C0_(2); E_(9); C0_(3); E_(10); E_(11);
  o = MFMA(v0, p0, o); SBAR();
  hc(); SBAR();
  E_(12); C1_(0); E_(13); C1_(1); E_(14); E_(15); C1_(2); C1_(3);
}

struct Ctx { unsigned char* ws; ldsp lds; int lane; int flg; };
DI void post(const Ctx& c, int b, unsigned stage) {
  __hip_atomic_fetch_max((GAS unsigned*)(c.ws + c.flg + b * 128), stage, __ATOMIC_RELAXED, __HIP_MEMORY_SCOPE_AGENT);
}
DI void arrive_post(const Ctx& c, int b, unsigned stage, unsigned n) {
  if (c.lane == 0) {
    const unsigned k = __hip_atomic_fetch_add((LAS unsigned*)(c.lds + M_CNT + stage * 4), 1u, __ATOMIC_RELAXED, __HIP_MEMORY_SCOPE_WORKGROUP);
    if (k == n - 1) post(c, b, stage);
  }
}
DI unsigned poll_ld(const Ctx& c, int pb) { return __hip_atomic_load((GAS unsigned*)(c.ws + c.flg + pb * 128), __ATOMIC_RELAXED, __HIP_MEMORY_SCOPE_AGENT); }
DI void poll_wait(const Ctx& c, int pb, unsigned stage, unsigned f) {
  for (int spin = 0; spin < SPIN_CAP; ++spin) {
    if (__builtin_amdgcn_ballot_w64(f < stage) == 0ull) break;
    __builtin_amdgcn_s_sleep(1);
    f = poll_ld(c, pb);
  }
  CBAR();
}

template <class F>
DI void gemm_wo(const u32x4 (&wf)[16], ldsp lds, int lane, int w, float (&res)[8], F mid) {
  const int kh = w >> 2;
  f32x16 acc = {};
  const ldsp bb = lds + L_SCR + kh * 16 * KB + lane * 16;
  u32x4 bf[16];
#pragma unroll
  for (int i = 0; i < 16; ++i) bf[i] = lds_r16(bb + i * KB);
  SBAR();
#pragma unroll
  for (int i = 0; i < 16; ++i) { acc = MFMA(wf[i], bf[i], acc); if (i == POST_GEMM) { SBAR(); mid(); SBAR(); } }
  const ldsp ex = lds + L_SCR + 32 * KB;
  f32x4 s0, s1;
#pragma unroll
  for (int i = 0; i < 4; ++i) { s0[i] = kh ? acc[i] : acc[8 + i]; s1[i] = kh ? acc[4 + i] : acc[12 + i]; }
  lds_wf4(ex + w * 2 * KB + lane * 16, s0);
  lds_wf4(ex + w * 2 * KB + KB + lane * 16, s1);
  BAR();
  const f32x4 t0 = lds_rf4(ex + (w ^ 4) * 2 * KB + lane * 16), t1 = lds_rf4(ex + (w ^ 4) * 2 * KB + KB + lane * 16);
#pragma unroll
  for (int i = 0; i < 4; ++i) { res[i] = (kh ? acc[8 + i] : acc[i]) + t0[i]; res[4 + i] = (kh ? acc[12 + i] : acc[4 + i]) + t1[i]; }
}

__global__ void __launch_bounds__(512, 2) k_main(Params p) {
  __shared__ __attribute__((aligned(16))) char smem[L_BYTES];
  const ldsp lds = (ldsp)smem;
  const int tid = threadIdx.x, lane = tid & 63, r = lane & 31, h = lane >> 5;
  const int w = __builtin_amdgcn_readfirstlane(tid >> 6);
  const int b = blockIdx.x, xcd = b & 7, jj = b >> 3;
  const int rt = 8 * xcd + (jj >> 2), sub = jj & 3;
  const int rb = 2 * xcd + (jj >> 4), hd = jj & 15;
  const int pair = p.pair;
  const rsrc_t ws = mkrs(p.ws, WS_BYTES);
  const int l16 = lane * 16;
  const int ct0 = sub < 2 ? 4 * sub : 3 * sub + 2, nct = sub < 2 ? 4 : 3;
  const int ct4 = w & 3, kh = w >> 2;
  const bool ctok = ct4 < nct;
  const int ct = ct0 + (ctok ? ct4 : 0);
  const Ctx cx = {p.ws, lds, lane, OFF_FLG + pair * 256 * 128};
  const int pb4 = (((jj & ~3) | (lane & 3)) << 3) | xcd;
  const int pb16 = (((jj & ~15) | (lane & 15)) << 3) | xcd;
  auto nop = []() {};
  STAMP(0);

  const int qp = w & 1, kq = w >> 1;
  const int kva = OFF_KVA + pair * KVA_SZ;
  const int kvb = kva + kq * 64 * KB;
  u32x4 q[2][2], K[2][2], V[2][2];
  u32x4 wf[16];
  u32x2 xres[2];
  f32x16 o0 = {}, o1 = {};
  {
    const int qa = OFF_QA + pair * QA_SZ + rt * 28 * KB;
#pragma unroll
    for (int a = 0; a < 2; ++a)
#pragma unroll
      for (int s = 0; s < 2; ++s)
#pragma unroll
        for (int jh = 0; jh < 2; ++jh) {
          const int dg = 4 * s + 2 * jh + h, G = 7 * (4 * sub + 2 * qp + a) + (dg < 7 ? dg : 6);
          const int g8 = G & 7;
          u32x2 v = ld8<0>(ws, ((G >> 3) * 2 + (g8 >> 2)) * KB + (r + 32 * (g8 & 1)) * 16 + ((g8 >> 1) & 1) * 8, qa);
          if (dg == 7) { v[0] = 0; v[1] = 0; }
          q[a][s][2 * jh] = v[0]; q[a][s][2 * jh + 1] = v[1];
        }
    const u32x4 k0 = ld16<0>(ws, l16, kva), k1 = ld16<0>(ws, l16 + KB, kva);
    K[0][0] = ld16<0>(ws, l16, kvb); K[0][1] = ld16<0>(ws, l16 + KB, kvb);
    V[0][0] = ld16<0>(ws, l16 + 2 * KB, kvb); V[0][1] = ld16<0>(ws, l16 + 3 * KB, kvb);
    K[1][0] = ld16<0>(ws, l16, kvb + 4 * KB); K[1][1] = ld16<0>(ws, l16 + KB, kvb + 4 * KB);
    V[1][0] = ld16<0>(ws, l16 + 2 * KB, kvb + 4 * KB);
    if (tid < 8) *(LAS unsigned*)(lds + M_CNT + tid * 4) = 0u;
    BAR();
    f32x16 s0 = MFMA(k0, q[0][0], f32x16{}), s1 = MFMA(k0, q[1][0], f32x16{});
    s0 = MFMA(k1, q[0][1], s0); s1 = MFMA(k1, q[1][1], s1);
    set_shift(q[0][1], xhalf_max(max16(s0, s0[0])), h, 0u);
    set_shift(q[1][1], xhalf_max(max16(s1, s1[0])), h, 0u);
  }
  STAMP(12);
  {
    f32x16 sa = MFMA(K[0][0], q[0][0], f32x16{}), sb;
    sa = MFMA(K[0][1], q[0][1], sa);
    u32x4 pp;
    const u32x4 sv = {SENT, SENT, SENT, SENT};
    const unsigned char* wbsrc = p.ws + OFF_WBT + (hd * 84 + w) * KB + l16;
    const unsigned wbdst = (unsigned)(uintptr_t)(lds + L_WB) + w * KB;
#pragma unroll
    for (int t = 0; t < 16; ++t) {
      const int c = t & 1, n = c ^ 1;
      auto ldV1 = [&]() { if (t + 1 < 16) V[n][1] = ld16<0>(ws, l16 + 3 * KB, kvb + (t + 1) * 4 * KB); };
      auto ldK = [&]() { if (t + 2 < 16) { K[c][0] = ld16<0>(ws, l16, kvb + (t + 2) * 4 * KB); K[c][1] = ld16<0>(ws, l16 + KB, kvb + (t + 2) * 4 * KB); } };
      auto ldV0 = [&]() { if (t + 2 < 16) V[c][0] = ld16<0>(ws, l16 + 2 * KB, kvb + (t + 2) * 4 * KB); };
      auto side0 = [&]() {
        if (t == 0) st16<16>(sv, ws, l16, OFF_OFA + (rt * 32 + sub * 8 + w) * KB);
        if (t == 1 && ctok) st16<16>(sv, ws, l16, OFF_XF1 + (rt * 28 + ct * 2 + kh) * KB);
        if (t == 2) st16<16>(sv, ws, l16, OFF_KVB + ((hd * 64 + rb * 4) * 4 + 2 * w) * KB);
        if (t == 3) st16<16>(sv, ws, l16 + KB, OFF_KVB + ((hd * 64 + rb * 4) * 4 + 2 * w) * KB);
        if (t == 4) st16<16>(sv, ws, l16, OFF_OFB + (((rb * 4 + (w >> 1)) * 16 + hd) * 2 + (w & 1)) * KB);
        if (t == 5 && ctok && pair == 0) st16<16>(sv, ws, l16, OFF_XF2 + (rt * 28 + ct * 2 + kh) * KB);
        if (t == 12) {
          if (pair == 0) {
            const unsigned short* xr = p.x + (size_t)(rt * 32 + r) * D_ + ct * 32 + kh * 16 + 4 * h;
            xres[0] = *(const u32x2*)xr; xres[1] = *(const u32x2*)(xr + 8);
          } else {
            const u32x4 v = ld16<0>(ws, l16, OFF_XF2 + (rt * 28 + ct * 2 + kh) * KB);
            xres[0] = u32x2{v[0], v[1]}; xres[1] = u32x2{v[2], v[3]};
          }
        }
      };
      auto side1 = [&]() { wf[t] = ld16<0>(ws, l16, OFF_WOA + (ct * 32 + kh * 16 + t) * KB); };
      auto side2 = [&]() { if (t < 10 || (t == 10 && w < 4)) glds16(wbsrc + t * 8 * KB, wbdst + t * 8 * KB); };
      if (t == 0) unit<true, false>(sa, sb, o0, o1, pp, K[c][0], K[c][1], q[1][0], q[1][1], V[c][0], V[n][1], ldV1, ldK, side0);
      else unit<true, true>(sa, sb, o0, o1, pp, K[c][0], K[c][1], q[1][0], q[1][1], V[c][0], V[n][1], ldV1, ldK, side0);
      if (t == 15 && POST_OA == 0) { asm volatile("s_waitcnt vmcnt(20)" ::: "memory"); arrive_post(cx, b, 1u, 8u); }
      if (t < 15) unit<true, true>(sb, sa, o1, o0, pp, K[n][0], K[n][1], q[0][0], q[0][1], V[c][0], V[c][1], side1, side2, ldV0);
      else unit<false, true>(sb, sa, o1, o0, pp, K[n][0], K[n][1], q[0][0], q[0][1], V[c][0], V[c][1], side1, side2, ldV0);
    }
    o1 = MFMA(V[1][1], pp, o1);
  }
  STAMP(1);
  if (POST_OA == 1) { asm volatile("s_waitcnt vmcnt(20)" ::: "memory"); arrive_post(cx, b, 1u, 8u); }
  for (int attempt = 0;; ++attempt) {
    {
      const ldsp red = lds + L_SCR + ((2 * qp) * 4 + kq) * 4 * KB + l16;
#pragma unroll
      for (int g = 0; g < 4; ++g) lds_wf4(red + g * KB, f32x4{o0[4 * g], o0[4 * g + 1], o0[4 * g + 2], o0[4 * g + 3]});
#pragma unroll
      for (int g = 0; g < 4; ++g) lds_wf4(red + 16 * KB + g * KB, f32x4{o1[4 * g], o1[4 * g + 1], o1[4 * g + 2], o1[4 * g + 3]});
      const bool big = h == 1 && !(o0[12] < 1e29f && o1[12] < 1e29f);
      const unsigned bad = __builtin_amdgcn_ballot_w64(big) != 0ull;
      if (lane == 0) *(LAS unsigned*)(lds + M_BAD + w * 4) = bad;
    }
    asm volatile("s_waitcnt vmcnt(4)" ::: "memory");
    BAR();
    if (POST_OA == 2 && tid == 0) post(cx, b, 1u);
    const u32x4 b0 = lds_r16(lds + M_BAD), b1 = lds_r16(lds + M_BAD + 16);
    const unsigned anybad = __builtin_amdgcn_readfirstlane(b0[0] | b0[1] | b0[2] | b0[3] | b1[0] | b1[1] | b1[2] | b1[3]);
    if (__builtin_expect(anybad == 0u || attempt == 1, 1)) break;
    if (h == 1) { q[0][1][2] = 0; q[0][1][3] = 0; q[1][1][2] = 0; q[1][1][3] = 0; }
    float m0 = -INFINITY, m1 = -INFINITY;
#pragma unroll 1
    for (int t = 0; t < 16; ++t) {
      const u32x4 k0 = ld16<0>(ws, l16, kvb + t * 4 * KB), k1 = ld16<0>(ws, l16 + KB, kvb + t * 4 * KB);
      f32x16 s = MFMA(k0, q[0][0], f32x16{}); s = MFMA(k1, q[0][1], s); m0 = max16(s, m0);
      s = MFMA(k0, q[1][0], f32x16{}); s = MFMA(k1, q[1][1], s); m1 = max16(s, m1);
    }
    m0 = xhalf_max(m0); m1 = xhalf_max(m1);
    *(LAS float*)(lds + M_MX + (w * 64 + lane) * 4) = h ? m1 : m0;
    BAR();
#pragma unroll
    for (int k = 0; k < 4; ++k) {
      m0 = __builtin_fmaxf(m0, *(LAS float*)(lds + M_MX + ((2 * k + qp) * 64 + r) * 4));
      m1 = __builtin_fmaxf(m1, *(LAS float*)(lds + M_MX + ((2 * k + qp) * 64 + 32 + r) * 4));
    }
    set_shift(q[0][1], m0, h, 0u);
    set_shift(q[1][1], m1, h, 0u);
    o0 = f32x16{}; o1 = f32x16{};
#pragma unroll 1
    for (int t = 0; t < 16; ++t) {
      const u32x4 k0 = ld16<0>(ws, l16, kvb + t * 4 * KB), k1 = ld16<0>(ws, l16 + KB, kvb + t * 4 * KB);
      const u32x4 v0 = ld16<0>(ws, l16 + 2 * KB, kvb + t * 4 * KB), v1 = ld16<0>(ws, l16 + 3 * KB, kvb + t * 4 * KB);
      u32x4 p0, p1;
      f32x16 s = MFMA(k0, q[0][0], f32x16{}); s = MFMA(k1, q[0][1], s);
      exp_pack(s, p0, p1); o0 = MFMA(v0, p0, o0); o0 = MFMA(v1, p1, o0);
      s = MFMA(k0, q[1][0], f32x16{}); s = MFMA(k1, q[1][1], s);
      exp_pack(s, p0, p1); o1 = MFMA(v0, p0, o1); o1 = MFMA(v1, p1, o1);
    }
  }
  {
    unsigned f = 0;
    if (w == 7) f = poll_ld(cx, pb4);
    const int T = w >> 1, s = w & 1;
    const ldsp red = lds + L_SCR + T * 16 * KB;
    f32x4 a0 = {}, a1 = {}; float l = 0.f;
#pragma unroll
    for (int k = 0; k < 4; ++k) {
      a0 += lds_rf4(red + k * 4 * KB + (2 * s) * KB + l16);
      a1 += lds_rf4(red + k * 4 * KB + (2 * s + 1) * KB + l16);
      l += *(LAS float*)(red + k * 4 * KB + 3 * KB + (32 + r) * 16);
    }
    const float inv = __builtin_amdgcn_rcpf(l);
    u32x4 o = {pk(a0[0] * inv, a0[1] * inv), pk(a0[2] * inv, a0[3] * inv), pk(a1[0] * inv, a1[1] * inv), pk(a1[2] * inv, a1[3] * inv)};
    if (s == 1 && h == 1) { o[2] = 0; o[3] = 0; }
    st16<16>(o, ws, l16, OFF_OFA + (rt * 32 + sub * 8 + w) * KB);
    STAMP(2);
    if (w == 7) poll_wait(cx, pb4, 1u, f);
  }
  u32x4 xkeep;
  {
    BAR();
    if (DLY_OA) __builtin_amdgcn_s_sleep(DLY_OA);
    u32x4 of[4];
    ld_handoff<4>(of, ws, l16, OFF_OFA + (rt * 32 + 4 * w) * KB);
    chk_handoff<4, 0, 4>(of, ws, l16, OFF_OFA + (rt * 32 + 4 * w) * KB);
#pragma unroll
    for (int i = 0; i < 4; ++i) lds_w16(lds + L_SCR + (4 * w + i) * KB + l16, of[i]);
    BAR();
    STAMP(3);
    float res[8];
    gemm_wo(wf, lds, lane, w, res, [&]() { if (tid == 448) post(cx, b, 2u); });
    xkeep = u32x4{pk(res[0] + bflo(xres[0][0]), res[1] + bfhi(xres[0][0])), pk(res[2] + bflo(xres[0][1]), res[3] + bfhi(xres[0][1])),
                  pk(res[4] + bflo(xres[1][0]), res[5] + bfhi(xres[1][0])), pk(res[6] + bflo(xres[1][1]), res[7] + bfhi(xres[1][1]))};
    if (ctok) st16<16>(xkeep, ws, l16, OFF_XF1 + (rt * 28 + ct * 2 + kh) * KB);
  }
  STAMP(4);
  const ldsp KVL = lds + L_SCR, QL = lds + L_SCR + 48 * KB;
  if (w < 4) {
    if (w == 3) poll_wait(cx, pb16, 2u, poll_ld(cx, pb16));
    BAR();
    if (w < 3) {
      float cs = 0.f;
#pragma unroll 4
      for (int ks = 0; ks < 28; ++ks) {
        const u32x4 f = lds_r16(lds + L_WB + (w * 28 + ks) * KB + l16);
#pragma unroll
        for (int d = 0; d < 4; ++d) cs = dot2(f[d], ONES2, cs);
      }
      cs = xhalf_add(cs);
      if (h == 0) *(LAS float*)(lds + M_CS + w * 128 + r * 4) = cs;
    }
    __builtin_amdgcn_s_sleep(DLY_WOB);
#pragma unroll
    for (int t = 0; t < 16; ++t) wf[t] = ld16<0>(ws, l16, OFF_WOB + (ct * 32 + t) * KB);
    BAR();
    const int kt = 4 * rb - 8 + 2 * w;
    u32x4 hv[8];
    if (kt >= 0) {
      const int prb = kt >> 2, pb = ((((prb & 1) * 16 + hd) << 3) | (prb >> 1));
      poll_wait(cx, pb, 3u, poll_ld(cx, pb));
      if (DLY_KV) __builtin_amdgcn_s_sleep(DLY_KV);
      ld_handoff<8>(hv, ws, l16, OFF_KVB + (hd * 64 + kt) * 4 * KB);
      chk_handoff<8, 0, 8>(hv, ws, l16, OFF_KVB + (hd * 64 + kt) * 4 * KB);
    } else {
#pragma unroll
      for (int i = 0; i < 8; ++i) hv[i] = u32x4{0u, 0u, 0u, ((i & 3) == 1 && h == 1) ? 0x3F800000u : 0u};
    }
#pragma unroll
    for (int i = 0; i < 8; ++i) lds_w16(KVL + (8 * w + i) * KB + l16, hv[i]);
  } else {
    const int t = w - 4, xo = OFF_XF1 + (rb * 4 + t) * 28 * KB;
    BAR();
    if (DLY_X1) __builtin_amdgcn_s_sleep(DLY_X1);
    u32x4 xf[28];
    ld_handoff<28>(xf, ws, l16, xo);
    f32x16 aq = {}, ak = {}, av = {};
    float sq = 0.f;
    const ldsp wb = lds + L_WB + l16;
#define QKV_STEPS(I0, I1) \
    chk_handoff<28, I0, I1>(xf, ws, l16, xo); \
    _Pragma("unroll") for (int ks = I0; ks < I1; ++ks) { \
      aq = MFMA(lds_r16(wb + ks * KB), xf[ks], aq); \
      ak = MFMA(lds_r16(wb + (28 + ks) * KB), xf[ks], ak); \
      av = MFMA(xf[ks], lds_r16(wb + (56 + ks) * KB), av); \
      _Pragma("unroll") for (int d = 0; d < 4; ++d) sq = dot2(xf[ks][d], xf[ks][d], sq); \
    }
    QKV_STEPS(0, 4) QKV_STEPS(4, 12) QKV_STEPS(12, 20)
    STAMP(13);
    QKV_STEPS(20, 28)
    sq = xhalf_add(sq);
    const float mu = from_hi(aq[12]) * (1.f / D_);
    const float rs = __builtin_amdgcn_rsqf(__builtin_fmaxf(sq * (1.f / D_) - mu * mu, 0.f) + EPS);
    const ldsp st = lds + M_ST + w * 256;
    if (h == 0) { *(LAS float*)(st + r * 4) = mu; *(LAS float*)(st + 128 + r * 4) = rs; }
    BAR();
    if (POST_KV == 28 && tid == 448) post(cx, b, 3u);
    ln_fix_tok(ak, lds + M_CS + 128, h, mu, rs);
    ln_fix_col(av, st, st + 128, h, *(LAS float*)(lds + M_CS + 256 + r * 4));
    u32x4 k0 = pack8(ak, 0), k1 = pack8(ak, 1), v0 = pack8(av, 0), v1 = pack8(av, 1);
    if (h == 1) { k1[2] = ONES2; k1[3] = 0x00003F80u; }
    if (r >= 28) { const unsigned f = r == 28 ? ONES2 : 0u; v0 = u32x4{f, f, f, f}; v1 = v0; }
    const int ko = OFF_KVB + (hd * 64 + rb * 4 + t) * 4 * KB;
    st16<16>(k0, ws, l16, ko); st16<16>(k1, ws, l16 + KB, ko); st16<16>(v0, ws, l16 + 2 * KB, ko); st16<16>(v1, ws, l16 + 3 * KB, ko);
    if (POST_KV == 29) arrive_post(cx, b, 3u, 4u);
    STAMP(14);
    ln_fix_tok(aq, lds + M_CS, h, mu, rs * C2);
    u32x4 q0 = pack8(aq, 0), q1 = pack8(aq, 1);
    if (h == 1) { q1[2] = 0; q1[3] = 0xF1490000u; }
    lds_w16(KVL + (32 + 4 * t) * KB + l16, k0); lds_w16(KVL + (33 + 4 * t) * KB + l16, k1);
    lds_w16(KVL + (34 + 4 * t) * KB + l16, v0); lds_w16(KVL + (35 + 4 * t) * KB + l16, v1);
    lds_w16(QL + 2 * t * KB + l16, q0); lds_w16(QL + (2 * t + 1) * KB + l16, q1);
    __builtin_amdgcn_s_sleep(DLY_WOB2);
#pragma unroll
    for (int i = 0; i < 16; ++i) wf[i] = ld16<0>(ws, l16, OFF_WOB + (ct * 32 + 16 + i) * KB);
  }
  STAMP(5);
  BAR();
  STAMP(6);
  {
    const int t = w & 3, half = w >> 2;
    const u32x4 q0 = lds_r16(QL + 2 * t * KB + l16);
    u32x4 q1 = lds_r16(QL + (2 * t + 1) * KB + l16);
    const ldsp kp = KVL + (t + 8 * half) * 4 * KB + l16;
    const int stp = half ? -4 * KB : 4 * KB;
    u32x4 kf[5][2], vf[5][2];
#pragma unroll
    for (int u = 0; u < 5; ++u) { kf[u][0] = lds_r16(kp + u * stp); kf[u][1] = lds_r16(kp + u * stp + KB); }
    f32x16 cm;
#pragma unroll
    for (int i = 0; i < 16; ++i) {
      const int kk = (i & 3) + 8 * (i >> 2) + 4 * h;
      cm[i] = ((kk <= r) == (half == 1)) ? 0.f : -1e30f;
    }
    SBAR();
    f32x16 sa = MFMA(kf[0][0], q0, cm), sb = MFMA(kf[1][0], q0, f32x16{});
    sa = MFMA(kf[0][1], q1, sa); sb = MFMA(kf[1][1], q1, sb);
#pragma unroll
    for (int u = 0; u < 2; ++u) { vf[u][0] = lds_r16(kp + u * stp + 2 * KB); vf[u][1] = lds_r16(kp + u * stp + 3 * KB); }
    SBAR();
    float m = max16(sa, -1e30f); SBAR();
    sa = MFMA(kf[2][0], q0, f32x16{}); sa = MFMA(kf[2][1], q1, sa); SBAR();
    m = max16(sb, m); SBAR();
    sb = MFMA(kf[3][0], q0, f32x16{}); sb = MFMA(kf[3][1], q1, sb); SBAR();
    m = max16(sa, m); SBAR();
    if (half) { sa = MFMA(kf[4][0], q0, f32x16{}); sa = MFMA(kf[4][1], q1, sa); }
    m = max16(sb, m);
    if (half) m = max16(sa, m);
    m = xhalf_max(m);
    if (h == 0) *(LAS float*)(lds + M_MX + (w * 32 + r) * 4) = m;
    BAR();
    m = __builtin_fmaxf(m, *(LAS float*)(lds + M_MX + ((w ^ 4) * 32 + r) * 4));
    set_shift(q1, m, h, 0xF1490000u);
    f32x16 o = {};
    u32x4 pp;
    sa = MFMA(kf[0][0], q0, cm); sa = MFMA(kf[0][1], q1, sa);
#define LDV(u) [&]() { vf[u][0] = lds_r16(kp + u * stp + 2 * KB); vf[u][1] = lds_r16(kp + u * stp + 3 * KB); }
    unit<true, false>(sa, sb, o, o, pp, kf[1][0], kf[1][1], q0, q1, vf[0][0], vf[0][1], nop, nop, LDV(2));
    unit<true, true>(sb, sa, o, o, pp, kf[2][0], kf[2][1], q0, q1, vf[1][0], vf[0][1], nop, nop, LDV(3));
    unit<true, true>(sa, sb, o, o, pp, kf[3][0], kf[3][1], q0, q1, vf[2][0], vf[1][1], nop, nop, LDV(4));
    if (half) {
      unit<true, true>(sb, sa, o, o, pp, kf[4][0], kf[4][1], q0, q1, vf[3][0], vf[2][1], nop, nop, nop);
      if (POST_OB == 0) arrive_post(cx, b, 4u, 8u);
      unit<false, true>(sa, sb, o, o, pp, kf[4][0], kf[4][1], q0, q1, vf[4][0], vf[3][1], nop, nop, nop);
      o = MFMA(vf[4][1], pp, o);
    } else {
      if (POST_OB == 0) arrive_post(cx, b, 4u, 8u);
      unit<false, true>(sb, sa, o, o, pp, kf[4][0], kf[4][1], q0, q1, vf[3][0], vf[2][1], nop, nop, nop);
      o = MFMA(vf[3][1], pp, o);
    }
    if (POST_OB == 1) arrive_post(cx, b, 4u, 8u);
    const ldsp ex = lds + L_WB;
    f32x4 s0, s1;
#pragma unroll
    for (int i = 0; i < 4; ++i) { s0[i] = half ? o[i] : o[8 + i]; s1[i] = half ? o[4 + i] : o[12 + i]; }
    float l = from_hi(o[12]);
    lds_wf4(ex + w * 2 * KB + l16, s0);
    lds_wf4(ex + w * 2 * KB + KB + l16, s1);
    if (h == 0) *(LAS float*)(lds + M_MX + 1024 + (w * 32 + r) * 4) = l;
    BAR();
    unsigned f = 0;
    if (w == 0) f = poll_ld(cx, pb16);
    const f32x4 t0 = lds_rf4(ex + (w ^ 4) * 2 * KB + l16), t1 = lds_rf4(ex + (w ^ 4) * 2 * KB + KB + l16);
    l += *(LAS float*)(lds + M_MX + 1024 + ((w ^ 4) * 32 + r) * 4);
    const float inv = __builtin_amdgcn_rcpf(l);
    float v[8];
#pragma unroll
    for (int i = 0; i < 4; ++i) { v[i] = ((half ? o[8 + i] : o[i]) + t0[i]) * inv; v[4 + i] = ((half ? o[12 + i] : o[4 + i]) + t1[i]) * inv; }
    u32x4 ov = {pk(v[0], v[1]), pk(v[2], v[3]), pk(v[4], v[5]), pk(v[6], v[7])};
    if (half == 1 && h == 1) { ov[2] = 0; ov[3] = 0; }
    st16<16>(ov, ws, l16, OFF_OFB + (((rb * 4 + t) * 16 + hd) * 2 + half) * KB);
    STAMP(7);
    if (w == 0) poll_wait(cx, pb16, 4u, f);
  }
  u32x4 wa[14];
  {
    BAR();
    if (DLY_OB) __builtin_amdgcn_s_sleep(DLY_OB);
    u32x4 of[4];
    ld_handoff<4>(of, ws, l16, OFF_OFB + (rt * 32 + 4 * w) * KB);
    chk_handoff<4, 0, 4>(of, ws, l16, OFF_OFB + (rt * 32 + 4 * w) * KB);
    if (pair == 0) {
#pragma unroll
      for (int i = 0; i < 14; ++i) wa[i] = ld16<0>(ws, l16, OFF_WAT + ((4 * sub + ct4) * 28 + kh * 14 + i) * KB);
    }
#pragma unroll
    for (int i = 0; i < 4; ++i) lds_w16(lds + L_SCR + (4 * w + i) * KB + l16, of[i]);
    BAR();
    STAMP(8);
    float res[8];
    gemm_wo(wf, lds, lane, w, res, [&]() { if (tid == 448 && pair == 0) post(cx, b, 5u); });
    const u32x4 xn = {pk(res[0] + bflo(xkeep[0]), res[1] + bfhi(xkeep[0])), pk(res[2] + bflo(xkeep[1]), res[3] + bfhi(xkeep[1])),
                      pk(res[4] + bflo(xkeep[2]), res[5] + bfhi(xkeep[2])), pk(res[6] + bflo(xkeep[3]), res[7] + bfhi(xkeep[3]))};
    if (pair == 1) {
      if (ctok) {
        unsigned short* orow = p.out + (size_t)(rt * 32 + r) * D_ + ct * 32 + kh * 16 + 4 * h;
        *(u32x2*)orow = u32x2{xn[0], xn[1]}; *(u32x2*)(orow + 8) = u32x2{xn[2], xn[3]};
      }
      STAMP(9);
      return;
    }
    if (ctok) st16<16>(xn, ws, l16, OFF_XF2 + (rt * 28 + ct * 2 + kh) * KB);
  }
  STAMP(9);
  {
    const int tile = 4 * sub + ct4;
    const bool isV = tile == 15;
    if (w == 7) poll_wait(cx, pb4, 5u, poll_ld(cx, pb4));
    BAR();
    if (DLY_X2) __builtin_amdgcn_s_sleep(DLY_X2);
    u32x4 xf[4];
    if (w < 7) ld_handoff<4>(xf, ws, l16, OFF_XF2 + (rt * 28 + 4 * w) * KB);
    float cs = 0.f;
#pragma unroll
    for (int i = 0; i < 14; ++i)
#pragma unroll
      for (int d = 0; d < 4; ++d) cs = dot2(wa[i][d], ONES2, cs);
    cs = xhalf_add(cs);
    if (h == 0) *(LAS float*)(lds + M_CS + w * 128 + r * 4) = cs;
    if (w < 7) {
      chk_handoff<4, 0, 4>(xf, ws, l16, OFF_XF2 + (rt * 28 + 4 * w) * KB);
      float sm = 0.f, sq = 0.f;
#pragma unroll
      for (int i = 0; i < 4; ++i) lds_w16(lds + L_SCR + (4 * w + i) * KB + l16, xf[i]);
#pragma unroll
      for (int i = 0; i < 4; ++i)
#pragma unroll
        for (int d = 0; d < 4; ++d) { sm = dot2(xf[i][d], ONES2, sm); sq = dot2(xf[i][d], xf[i][d], sq); }
      sm = xhalf_add(sm); sq = xhalf_add(sq);
      *(LAS float*)(lds + M_ST + w * 256 + lane * 4) = h ? sq : sm;
    }
    BAR();
    STAMP(10);
    f32x16 acc = {};
    const ldsp bb = lds + L_SCR + kh * 14 * KB + l16;
    u32x4 bf[14];
#pragma unroll
    for (int i = 0; i < 14; ++i) bf[i] = lds_r16(bb + i * KB);
    float sm = 0.f, sq = 0.f;
#pragma unroll
    for (int k = 0; k < 7; ++k) { sm += *(LAS float*)(lds + M_ST + k * 256 + r * 4); sq += *(LAS float*)(lds + M_ST + k * 256 + 128 + r * 4); }
    if (isV) {
#pragma unroll
      for (int i = 0; i < 14; ++i) acc = MFMA(bf[i], wa[i], acc);
    } else {
#pragma unroll
      for (int i = 0; i < 14; ++i) acc = MFMA(wa[i], bf[i], acc);
    }
    const float mu = sm * (1.f / D_);
    const float rs = __builtin_amdgcn_rsqf(__builtin_fmaxf(sq * (1.f / D_) - mu * mu, 0.f) + EPS);
    const ldsp ex = lds + L_WB;
    f32x4 s0, s1;
#pragma unroll
    for (int i = 0; i < 4; ++i) { s0[i] = kh ? acc[i] : acc[8 + i]; s1[i] = kh ? acc[4 + i] : acc[12 + i]; }
    lds_wf4(ex + w * 2 * KB + l16, s0);
    lds_wf4(ex + w * 2 * KB + KB + l16, s1);
    if (w == 0) *(LAS float*)(lds + M_MX + lane * 4) = h ? rs : mu;
    BAR();
    const f32x4 t0 = lds_rf4(ex + (w ^ 4) * 2 * KB + l16), t1 = lds_rf4(ex + (w ^ 4) * 2 * KB + KB + l16);
    float v[8];
#pragma unroll
    for (int i = 0; i < 4; ++i) { v[i] = (kh ? acc[8 + i] : acc[i]) + t0[i]; v[4 + i] = (kh ? acc[12 + i] : acc[4 + i]) + t1[i]; }
    u32x4 ov;
    if (!isV) {
      const float sc = tile < 14 ? rs * C2 : rs;
#pragma unroll
      for (int g = 0; g < 2; ++g) {
        const f32x4 c = lds_rf4(lds + M_CS + ct4 * 128 + (16 * kh + 8 * g + 4 * h) * 4) + lds_rf4(lds + M_CS + (ct4 + 4) * 128 + (16 * kh + 8 * g + 4 * h) * 4);
#pragma unroll
        for (int i = 0; i < 4; ++i) v[4 * g + i] = (v[4 * g + i] - mu * c[i]) * sc;
      }
      ov = u32x4{pk(v[0], v[1]), pk(v[2], v[3]), pk(v[4], v[5]), pk(v[6], v[7])};
      if (tile == 14 && kh == 1 && h == 1) { ov[2] = ONES2; ov[3] = 0x00003F80u; }
    } else {
      const float c = *(LAS float*)(lds + M_CS + ct4 * 128 + r * 4) + *(LAS float*)(lds + M_CS + (ct4 + 4) * 128 + r * 4);
#pragma unroll
      for (int g = 0; g < 2; ++g) {
        const f32x4 m4 = lds_rf4(lds + M_MX + (16 * kh + 8 * g + 4 * h) * 4), r4 = lds_rf4(lds + M_MX + 128 + (16 * kh + 8 * g + 4 * h) * 4);
#pragma unroll
        for (int i = 0; i < 4; ++i) v[4 * g + i] = (v[4 * g + i] - m4[i] * c) * r4[i];
      }
      ov = u32x4{pk(v[0], v[1]), pk(v[2], v[3]), pk(v[4], v[5]), pk(v[6], v[7])};
      if (r >= 28) { const unsigned f = r == 28 ? ONES2 : 0u; ov = u32x4{f, f, f, f}; }
    }
    const int dst = tile < 14 ? OFF_QA + QA_SZ + (rt * 14 + tile) * 2 * KB : OFF_KVA + KVA_SZ + rt * 4 * KB + (tile - 14) * 2 * KB;
    st16<0>(ov, ws, l16, dst + kh * KB);
  }
  STAMP(11);
}

extern "C" void kernel_launch(void* const* d_in, const int* in_sizes, int n_in, void* d_out, int out_size, void* d_ws, size_t ws_size,
                              hipStream_t stream) {
  Params p;
  p.x = (const unsigned short*)d_in[0];
  p.wq = (const unsigned short*)d_in[1];
  p.wk = (const unsigned short*)d_in[2];
  p.wv = (const unsigned short*)d_in[3];
  p.woa = (const unsigned short*)d_in[4];
  p.wqkv = (const unsigned short*)d_in[6];
  p.wob = (const unsigned short*)d_in[7];
  p.out = (unsigned short*)d_out;
  p.ws = (unsigned char*)d_ws;
  p.pad = 0;
  p.pair = -1;
  k_pre<<<256, 512, 0, stream>>>(p);
  p.pair = 0;
  k_main<<<256, 512, 0, stream>>>(p);
  p.pair = 1;
  k_main<<<256, 512, 0, stream>>>(p);
}
```

```cpp
#include <hip/hip_runtime.h>
#include <hip/hip_bf16.h>
#include <stdint.h>

typedef short bf16x8 __attribute__((ext_vector_type(8)));
typedef short s16x4 __attribute__((ext_vector_type(4)));
typedef float f32x16 __attribute__((ext_vector_type(16)));
typedef float f32x4 __attribute__((ext_vector_type(4)));
typedef float f32x2 __attribute__((ext_vector_type(2)));
typedef unsigned u32x4 __attribute__((ext_vector_type(4)));
typedef unsigned u32x2 __attribute__((ext_vector_type(2)));
typedef __bf16 bf16x2_t __attribute__((ext_vector_type(2)));
#define DI __device__ __forceinline__
#define LAS __attribute__((address_space(3)))
#define GAS __attribute__((address_space(1)))
typedef LAS char* ldsp;

#define MFMA(a, b, c) __builtin_amdgcn_mfma_f32_32x32x16_bf16(__builtin_bit_cast(bf16x8, (a)), __builtin_bit_cast(bf16x8, (b)), (c), 0, 0, 0)
#define BAR() asm volatile("s_waitcnt lgkmcnt(0)\n\ts_barrier" ::: "memory")
#define CBAR() asm volatile("" ::: "memory")
#define SBAR() __builtin_amdgcn_sched_barrier(0)

constexpr int S_ = 2048, D_ = 448;
constexpr float C2 = 0.18898223650461362f * 1.4426950408889634f;
constexpr float EPS = 1e-5f;
constexpr unsigned SENT = 0xFFFFFFFFu;
constexpr unsigned ONES2 = 0x3F803F80u;
constexpr int SPIN_CAP = 1 << 14;

constexpr int KB = 1024;
constexpr int OFF_WAT = 0;
constexpr int OFF_WBT = OFF_WAT + 448 * KB;
constexpr int OFF_WOA = OFF_WBT + 1344 * KB;
constexpr int OFF_WOB = OFF_WOA + 448 * KB;
constexpr int OFF_CS = OFF_WOB + 448 * KB;
constexpr int OFF_PAIR = OFF_CS + 8 * KB;
constexpr int QA_SZ = 64 * 28 * KB;
constexpr int R_QA = 0;
constexpr int R_KVA = R_QA + QA_SZ;
constexpr int R_OFA = R_KVA + 256 * KB;
constexpr int R_XF1 = R_OFA + 2048 * KB;
constexpr int R_KVB = R_XF1 + QA_SZ;
constexpr int R_OFB = R_KVB + 4096 * KB;
constexpr int R_XF2 = R_OFB + 2048 * KB;
constexpr int R_ST1 = R_XF2 + QA_SZ;
constexpr int R_ST2 = R_ST1 + 64 * 14 * 256;
constexpr int R_FLG = R_ST2 + 64 * 14 * 256;
constexpr int R_FKV = R_FLG + 32 * KB;
constexpr int PAIR_SZ = R_FKV + 64 * KB;
constexpr int OFF_DBG = OFF_PAIR + 2 * PAIR_SZ;
constexpr int WS_BYTES = OFF_DBG + 2048 * KB;

#define NOSIDE 0
#define NOKV 0
#define HPRIO 0
#define NOH 0
#define SIG_X1 14
#define WOB1 0
#define DT_OA 3700
#define DT_X 1750
#define DT_KV 3200
#define DT_OB 900
#define DT_UNIT 240
#define X1_AHEAD 8
#define WA1 8
#define PRE_AUX 16
#define PRO_DLY 2
#define WARM 1
#define KV_DLY 8
#define Q_DLY 6
#define PACE3 4
#define DT_Q 1600
#define ETA_AT 26
#define DLY_OA 0
#define DLY_X1 0
#define DLY_KV 0
#define DLY_OB 0
#define DLY_X2 0
#define DLY_WOB 24
#define PACE 1
#define PACE2 0
#define POST_OA -1
#define POST_OB 6
#define POST_KV 16
#define POST_GEMM 8

DI unsigned pk(float a, float b) { f32x2 v = {a, b}; bf16x2_t r = __builtin_convertvector(v, bf16x2_t); return __builtin_bit_cast(unsigned, r); }
DI float dot2(unsigned a, unsigned b, float c) { return __builtin_amdgcn_fdot2_f32_bf16(__builtin_bit_cast(bf16x2_t, a), __builtin_bit_cast(bf16x2_t, b), c, false); }
DI float bflo(unsigned u) { return __uint_as_float(u << 16); }
DI float bfhi(unsigned u) { return __uint_as_float(u & 0xFFFF0000u); }
DI float xhalf_add(float v) { auto r = __builtin_amdgcn_permlane32_swap(__float_as_uint(v), __float_as_uint(v), false, false); return __uint_as_float(r[0]) + __uint_as_float(r[1]); }
DI float xhalf_max(float v) { auto r = __builtin_amdgcn_permlane32_swap(__float_as_uint(v), __float_as_uint(v), false, false); return __builtin_fmaxf(__uint_as_float(r[0]), __uint_as_float(r[1])); }
DI float from_hi(float v) { auto r = __builtin_amdgcn_permlane32_swap(__float_as_uint(v), __float_as_uint(v), false, false); return __uint_as_float(r[1]); }
DI float max3f(float a, float b, float c) { return __builtin_fmaxf(__builtin_fmaxf(a, b), c); }
DI float max16(const f32x16& s, float m) {
#pragma unroll
  for (int i = 0; i < 16; i += 2) m = max3f(m, s[i], s[i + 1]);
  return m;
}
typedef __amdgpu_buffer_rsrc_t rsrc_t;
DI rsrc_t mkrs(const void* p, unsigned n) { return __builtin_amdgcn_make_buffer_rsrc((void*)p, 0, n, 0x00020000); }
template <int AUX> DI u32x4 ld16(rsrc_t rs, int voff, int soff) { return __builtin_amdgcn_raw_buffer_load_b128(rs, voff, soff, AUX); }
template <int AUX> DI u32x2 ld8(rsrc_t rs, int voff, int soff) { return __builtin_amdgcn_raw_buffer_load_b64(rs, voff, soff, AUX); }
template <int AUX> DI void st16(u32x4 v, rsrc_t rs, int voff, int soff) { __builtin_amdgcn_raw_buffer_store_b128(v, rs, voff, soff, AUX); }
template <int AUX> DI void st8(u32x2 v, rsrc_t rs, int voff, int soff) { __builtin_amdgcn_raw_buffer_store_b64(v, rs, voff, soff, AUX); }
template <int AUX> DI unsigned ld4(rsrc_t rs, int voff, int soff) { return __builtin_amdgcn_raw_buffer_load_b32(rs, voff, soff, AUX); }
template <int AUX> DI void st4(unsigned v, rsrc_t rs, int voff, int soff) { __builtin_amdgcn_raw_buffer_store_b32(v, rs, voff, soff, AUX); }
DI ldsp opaque(ldsp p) { unsigned a = (unsigned)(uintptr_t)p; asm volatile("" : "+v"(a)); return (ldsp)(uintptr_t)a; }
DI u32x4 lds_r16(ldsp p) { return *(LAS u32x4*)p; }
DI f32x4 lds_rf4(ldsp p) { return *(LAS f32x4*)p; }
DI void lds_w16(ldsp p, u32x4 v) { *(LAS u32x4*)p = v; }
DI void lds_wf4(ldsp p, f32x4 v) { *(LAS f32x4*)p = v; }
DI unsigned umax3(unsigned a, unsigned b, unsigned c) { return max(max(a, b), c); }
DI unsigned smax(unsigned m, u32x4 v) { return umax3(umax3(m, v[0], v[1]), v[2], v[3]); }
DI void glds16(const void* g, unsigned lds_base) {
  unsigned sv;
  asm volatile("s_mov_b32 %0, m0\n\ts_mov_b32 m0, %2\n\ts_nop 0\n\tglobal_load_lds_dwordx4 %1, off\n\ts_mov_b32 m0, %0" : "=&s"(sv) : "v"(g), "s"(lds_base) : "memory");
}
DI u32x4 pack8(const f32x16& a, int s) {
  u32x4 r;
#pragma unroll
  for (int i = 0; i < 4; ++i) r[i] = pk(a[8 * s + 2 * i], a[8 * s + 2 * i + 1]);
  return r;
}
#define RETRY(tag) do { } while (0)
template <int N> DI void ld_handoff(u32x4 (&v)[N], rsrc_t rs, int voff, int soff) {
#pragma unroll
  for (int i = 0; i < N; ++i) { v[i] = ld16<16>(rs, voff + (i & 3) * KB, soff + (i >> 2) * 4 * KB); SBAR(); }
}
template <int N> DI bool any_sent(const u32x4 (&v)[N]) {
  unsigned m = 0;
#pragma unroll
  for (int i = 0; i < N; ++i) m = smax(m, v[i]);
  return __builtin_amdgcn_ballot_w64(m == SENT) != 0ull;
}
template <int N> DI void chk_handoff(u32x4 (&v)[N], rsrc_t rs, int voff, int soff, int tag) {
  if (__builtin_expect(any_sent<N>(v), 0)) {
#pragma unroll 1
    for (int spin = 0; spin < SPIN_CAP; ++spin) {
      CBAR();
      RETRY(tag);
#pragma unroll
      for (int i = 0; i < N; ++i)
        if (__builtin_amdgcn_ballot_w64(smax(0u, v[i]) == SENT) != 0ull) v[i] = ld16<16>(rs, voff + (i & 3) * KB, soff + (i >> 2) * 4 * KB);
      if (!any_sent<N>(v)) break;
    }
  }
}

#define STAMP_DECL
#define STAMP_DUMP() do { } while (0)
#define STAMP(i) do { } while (0)

struct Params {
  const unsigned short *x, *wq, *wk, *wv, *woa, *wqkv, *wob;
  unsigned short* out;
  unsigned char* ws;
};

DI void ln_fix_tok(f32x16& a, ldsp cs, int h, float mu, float sc) {
#pragma unroll
  for (int g = 0; g < 4; ++g) {
    f32x4 c = lds_rf4(cs + (8 * g + 4 * h) * 4);
#pragma unroll
    for (int i = 0; i < 4; ++i) a[4 * g + i] = (a[4 * g + i] - mu * c[i]) * sc;
  }
}
DI void ln_fix_col(f32x16& a, ldsp mup, ldsp rsp, int h, float cs) {
#pragma unroll
  for (int g = 0; g < 4; ++g) {
    f32x4 m = lds_rf4(mup + (8 * g + 4 * h) * 4), r = lds_rf4(rsp + (8 * g + 4 * h) * 4);
#pragma unroll
    for (int i = 0; i < 4; ++i) a[4 * g + i] = (a[4 * g + i] - m[i] * cs) * r[i];
  }
}

constexpr int XI_KS = 1056;
constexpr int P_XI = 0, P_WI = 2 * 28 * XI_KS, P_ST = P_WI + 448 * 128, P_CS = P_ST + 512, P_CS2 = P_CS + 4 * 128, P_TI = P_CS2 + 4 * 128, P_BYTES = P_TI + 448 * 64;
__global__ void __launch_bounds__(512, 2) k_pre(Params p) {
  __shared__ __attribute__((aligned(16))) char smem[P_BYTES];
  const ldsp lds = (ldsp)smem;
  const int tid = threadIdx.x, lane = tid & 63, r = lane & 31, h = lane >> 5;
  const int w = __builtin_amdgcn_readfirstlane(tid >> 6);
  const int b = blockIdx.x;
  const rsrc_t ws = mkrs(p.ws, WS_BYTES);
  const int pair = -1; (void)pair;
  STAMP_DECL; STAMP(0);
  if (w >= 4) {
    if (w == 4) *(unsigned*)(p.ws + OFF_PAIR + h * PAIR_SZ + R_FLG + b * 128 + r * 4) = 0u;
    if (w == 5) *(unsigned*)(p.ws + OFF_PAIR + PAIR_SZ + R_FKV + b * 256 + lane * 4) = 0u;
    const int w4 = w - 4;
    if (b < 64) {
      const unsigned short* src; int pitch; bool pad = true;
      if (b < 14) { src = p.wq + 32 * b; pitch = 448; pad = false; }
      else if (b < 16) { src = b == 14 ? p.wk : p.wv; pitch = 28; }
      else { src = p.wqkv + ((b - 16) % 3) * 448 + ((b - 16) / 3) * 28; pitch = 1344; }
      const int pc = lane & 7, rw = lane >> 3;
      const bool dead = pad && pc == 7;
      src += (size_t)(w4 * 112 + rw) * pitch + (dead ? 0 : pc * 4);
      u32x2 v[14];
#pragma unroll
      for (int i = 0; i < 14; ++i) v[i] = *(const u32x2*)(src + (size_t)i * 8 * pitch);
      const ldsp img = lds + P_TI + w4 * 112 * 64;
#pragma unroll
      for (int i = 0; i < 14; ++i) *(LAS u32x2*)(img + i * 512 + lane * 8) = dead ? u32x2{0u, 0u} : v[i];
      CBAR();
      const int i16 = lane & 15;
      const ldsp tb = img + (4 * h + (i16 >> 2)) * 64 + ((lane >> 4) & 1) * 32 + (i16 & 3) * 8;
      float cs = 0.f;
#pragma unroll
      for (int ks = 0; ks < 7; ++ks) {
        const s16x4 lo = __builtin_amdgcn_ds_read_tr16_b64_v4i16((LAS s16x4*)(tb + ks * 1024));
        const s16x4 hi = __builtin_amdgcn_ds_read_tr16_b64_v4i16((LAS s16x4*)(tb + ks * 1024 + 512));
        const u32x4 o = __builtin_bit_cast(u32x4, __builtin_shufflevector(lo, hi, 0, 1, 2, 3, 4, 5, 6, 7));
        st16<PRE_AUX>(o, ws, lane * 16, (b * 28 + w4 * 7 + ks) * KB);
#pragma unroll
        for (int i = 0; i < 4; ++i) cs = dot2(o[i], ONES2, cs);
      }
      cs = xhalf_add(cs);
      if (h == 0) *(LAS float*)(lds + P_CS2 + w4 * 128 + r * 4) = cs;
      BAR();
      if (w4 == 0 && h == 0) {
        const float t = (*(LAS float*)(lds + P_CS2 + r * 4) + *(LAS float*)(lds + P_CS2 + 128 + r * 4)) + (*(LAS float*)(lds + P_CS2 + 256 + r * 4) + *(LAS float*)(lds + P_CS2 + 384 + r * 4));
        *(float*)(p.ws + OFF_CS + b * 128 + r * 4) = t;
      }
    } else {
      unsigned short e[2][8];
#pragma unroll
      for (int u = 0; u < 2; ++u) {
        int g = (b - 64) * 4 + w4 + u * 768;
        if (g >= 896) g = 0;
        const unsigned short* wo = g >= 448 ? p.wob : p.woa;
        if (g >= 448) g -= 448;
        const int ct = g >> 5, ks = g & 31, head = ks >> 1;
        const int k0 = head * 28 + 16 * (ks & 1) + 4 * h, kmax = head * 28 + 28;
#pragma unroll
        for (int j = 0; j < 8; ++j) {
          const int k = k0 + 8 * (j >> 2) + (j & 3);
          const unsigned short v = wo[(k < kmax ? k : 0) * 448 + 32 * ct + r];
          e[u][j] = k < kmax ? v : (unsigned short)0;
        }
      }
#pragma unroll
      for (int u = 0; u < 2; ++u) {
        const int g = (b - 64) * 4 + w4 + u * 768;
        u32x4 o;
#pragma unroll
        for (int i = 0; i < 4; ++i) o[i] = (unsigned)e[u][2 * i] | ((unsigned)e[u][2 * i + 1] << 16);
        if (g < 896) st16<PRE_AUX>(o, ws, lane * 16, OFF_WOA + g * KB);
      }
    }
    STAMP(1); STAMP_DUMP();
    return;
  }
  const int xcd = b & 7, jj = b >> 3, rb64 = 4 * xcd + (jj >> 3), cp = jj & 7;
  const int q8 = tid & 7, row8 = tid >> 3;
  u32x4 xv[2][7], wv[14];
  {
    const unsigned short* xr = p.x + (size_t)(rb64 * 64 + row8) * D_ + q8 * 8;
#pragma unroll
    for (int ps = 0; ps < 2; ++ps)
#pragma unroll
      for (int i = 0; i < 7; ++i) xv[ps][i] = *(const u32x4*)(xr + ps * 32 * D_ + i * 64);
  }
  if (cp < 7) {
    const unsigned short* wr = p.wq + (size_t)row8 * 448 + cp * 64 + q8 * 8;
#pragma unroll
    for (int i = 0; i < 14; ++i) wv[i] = *(const u32x4*)(wr + i * 32 * 448);
  } else {
    const unsigned short* wr = ((q8 >> 2) ? p.wv : p.wk) + (size_t)row8 * 28 + (q8 & 3) * 8;
    const bool last = (q8 & 3) == 3;
#pragma unroll
    for (int i = 0; i < 14; ++i) {
      const u32x2 a = *(const u32x2*)(wr + i * 32 * 28);
      u32x2 c = *(const u32x2*)(wr + i * 32 * 28 + (last ? 0 : 4));
      if (last) { c[0] = 0; c[1] = 0; }
      wv[i] = u32x4{a[0], a[1], c[0], c[1]};
    }
  }
#pragma unroll
  for (int ps = 0; ps < 2; ++ps) {
    float sm = 0.f, sq = 0.f;
#pragma unroll
    for (int i = 0; i < 7; ++i) {
#pragma unroll
      for (int d = 0; d < 4; ++d) { sm = dot2(xv[ps][i][d], ONES2, sm); sq = dot2(xv[ps][i][d], xv[ps][i][d], sq); }
      const int ks = 4 * i + (q8 >> 1);
      const ldsp dst = lds + P_XI + (ps * 28 + ks) * XI_KS + row8 * 16 + (q8 & 1) * 8;
      *(LAS u32x2*)dst = u32x2{xv[ps][i][0], xv[ps][i][1]};
      *(LAS u32x2*)(dst + 512) = u32x2{xv[ps][i][2], xv[ps][i][3]};
    }
#pragma unroll
    for (int o = 1; o < 8; o <<= 1) { sm += __shfl_xor(sm, o); sq += __shfl_xor(sq, o); }
    const float mu = sm * (1.f / D_), var = __builtin_fmaxf(sq * (1.f / D_) - mu * mu, 0.f);
    if (q8 == 0) { *(LAS float*)(lds + P_ST + (ps * 32 + row8) * 4) = mu; *(LAS float*)(lds + P_ST + 256 + (ps * 32 + row8) * 4) = __builtin_amdgcn_rsqf(var + EPS); }
  }
#pragma unroll
  for (int i = 0; i < 14; ++i) {
    const int k = row8 + 32 * i;
    lds_w16(lds + P_WI + k * 128 + (((q8 >> 2) ^ ((k >> 1) & 1)) * 64) + (q8 & 3) * 16, wv[i]);
  }
  BAR();
  STAMP(1);
  const int rt2 = w >> 1, ct2 = w & 1, tile = 2 * cp + ct2, rt = rb64 * 2 + rt2;
  const bool isV = tile == 15;
  const int i16 = lane & 15, tq = i16 >> 2, tp = i16 & 3, blk = (lane >> 4) & 1;
  const ldsp wbase = lds + P_WI + (4 * h + tq) * 128 + ((ct2 ^ ((tq >> 1) & 1)) * 64) + blk * 32 + tp * 8;
  const ldsp xbase = lds + P_XI + rt2 * 28 * XI_KS + lane * 16;
  f32x16 acc = {}, csa = {};
  const u32x4 ones = {ONES2, ONES2, ONES2, ONES2};
  u32x4 wr[4], xr[4];
  const ldsp wb0 = wbase + rt2 * 2048, wb1 = wbase + (1 - rt2) * 2048, xb0 = xbase + rt2 * XI_KS, xb1 = xbase + (1 - rt2) * XI_KS;
#define PRE_LD(i) do { const ldsp wp_ = (((i) & 1) ? wb1 : wb0) + ((i) >> 1) * 4096; \
    const s16x4 lo_ = __builtin_amdgcn_ds_read_tr16_b64_v4i16((LAS s16x4*)(wp_)), hi_ = __builtin_amdgcn_ds_read_tr16_b64_v4i16((LAS s16x4*)(wp_ + 1024)); \
    wr[(i) & 3] = __builtin_bit_cast(u32x4, __builtin_shufflevector(lo_, hi_, 0, 1, 2, 3, 4, 5, 6, 7)); \
    xr[(i) & 3] = lds_r16((((i) & 1) ? xb1 : xb0) + ((i) >> 1) * 2 * XI_KS); } while (0)
#define PRE_LOOP(SW) \
  PRE_LD(0); PRE_LD(1); PRE_LD(2); SBAR(); \
  _Pragma("unroll") for (int i = 0; i < 28; ++i) { \
    if (i + 3 < 28) PRE_LD(i + 3); \
    SBAR(); \
    acc = SW ? MFMA(xr[i & 3], wr[i & 3], acc) : MFMA(wr[i & 3], xr[i & 3], acc); \
    if (!(i & 1)) csa = SW ? MFMA(ones, wr[i & 3], csa) : MFMA(wr[i & 3], ones, csa); \
    SBAR(); \
  }
  u32x4 o0, o1;
  if (!isV) {
    PRE_LOOP(false)
    if (r == 0) {
#pragma unroll
      for (int g = 0; g < 4; ++g) lds_wf4(lds + P_CS + w * 128 + (8 * g + 4 * h) * 4, f32x4{csa[4 * g], csa[4 * g + 1], csa[4 * g + 2], csa[4 * g + 3]});
    }
    const float mu = *(LAS float*)(lds + P_ST + (rt2 * 32 + r) * 4), rs = *(LAS float*)(lds + P_ST + 256 + (rt2 * 32 + r) * 4);
    BAR();
    const float sc = tile < 14 ? rs * C2 : rs;
#pragma unroll
    for (int g = 0; g < 4; ++g) {
      const f32x4 c = lds_rf4(lds + P_CS + (w ^ 2) * 128 + (8 * g + 4 * h) * 4);
#pragma unroll
      for (int i = 0; i < 4; ++i) acc[4 * g + i] = (acc[4 * g + i] - mu * (c[i] + csa[4 * g + i])) * sc;
    }
    o0 = pack8(acc, 0); o1 = pack8(acc, 1);
    if (tile == 14 && h == 1) { o1[2] = ONES2; o1[3] = 0x00003F80u; }
  } else {
    PRE_LOOP(true)
    if (h == 0) *(LAS float*)(lds + P_CS + w * 128 + r * 4) = csa[0];
    BAR();
    ln_fix_col(acc, lds + P_ST + rt2 * 128, lds + P_ST + 256 + rt2 * 128, h, csa[0] + *(LAS float*)(lds + P_CS + (w ^ 2) * 128 + r * 4));
    o0 = pack8(acc, 0); o1 = pack8(acc, 1);
    if (r >= 28) { const unsigned f = r == 28 ? ONES2 : 0u; o0 = u32x4{f, f, f, f}; o1 = o0; }
  }
  const int dst = OFF_PAIR + (tile < 14 ? R_QA + (rt * 14 + tile) * 2 * KB : R_KVA + rt * 4 * KB + (tile - 14) * 2 * KB);
  st16<0>(o0, ws, lane * 16, dst);
  st16<0>(o1, ws, lane * 16 + KB, dst);
  STAMP(2); STAMP_DUMP();
}

constexpr int L_SCR = 0;
constexpr int L_WB = 64 * KB;
constexpr int L_QF = 148 * KB;
constexpr int L_MISC = L_QF + 8 * KB;
constexpr int M_SIG = L_MISC;
constexpr int M_ST = M_SIG + 512;
constexpr int L_BYTES = M_ST + 1280;
static_assert(L_BYTES <= 160 * KB, "LDS");

DI void exp_pack(const f32x16& s, u32x4& p0, u32x4& p1) {
#pragma unroll
  for (int i = 0; i < 4; ++i) p0[i] = pk(__builtin_amdgcn_exp2f(s[2 * i]), __builtin_amdgcn_exp2f(s[2 * i + 1]));
#pragma unroll
  for (int i = 0; i < 4; ++i) p1[i] = pk(__builtin_amdgcn_exp2f(s[8 + 2 * i]), __builtin_amdgcn_exp2f(s[8 + 2 * i + 1]));
}
DI void set_shift(u32x4& q1, float m, int h, unsigned hi31) {
  const unsigned a = pk(m, m); const float r1 = m - bflo(a);
  const unsigned c = pk(r1, r1); const float r2 = r1 - bflo(c);
  const unsigned e = pk(r2, 0.f);
  const unsigned d2 = ((a & 0xFFFFu) | (c << 16)) ^ 0x80008000u, d3 = (e ^ 0x00008000u) | hi31;
  q1[2] = h ? d2 : q1[2]; q1[3] = h ? d3 : q1[3];
}
#define PIN(x) asm volatile("" : "+v"(x))
template <bool DOQK, bool DOPVP, class FA, class FB, class FC>
DI void unit(const f32x16& s, f32x16& sn, const f32x16& cn, f32x16& o, f32x16& op, u32x4& pp, const u32x4& kn0, const u32x4& kn1, const u32x4& qn0,
             const u32x4& qn1, const u32x4& v0, const u32x4& v1p, FA ha, FB hb, FC hc) {
  float e[16]; u32x4 p0;
#define E_(i) e[i] = __builtin_amdgcn_exp2f(s[i]); PIN(e[i]); SBAR()
#define C0_(i) p0[i] = pk(e[2 * i], e[2 * i + 1]); PIN(p0[i]); SBAR()
#define C1_(i) pp[i] = pk(e[8 + 2 * i], e[8 + 2 * i + 1]); PIN(pp[i]); SBAR()
  SBAR();
  if (DOQK) { sn = MFMA(kn0, qn0, cn); SBAR(); }
  E_(0); E_(1); E_(2); E_(3);
  if (DOPVP) { op = MFMA(v1p, pp, op); SBAR(); }
  ha(); SBAR();
  E_(4); C0_(0); E_(5); C0_(1); E_(6); E_(7);
  if (DOQK) { sn = MFMA(kn1, qn1, sn); SBAR(); }
  hb(); SBAR();
  E_(8); C0_(2); E_(9); C0_(3); E_(10); E_(11);
  o = MFMA(v0, p0, o); SBAR();
  hc(); SBAR();
  E_(12); C1_(0); E_(13); C1_(1); E_(14); E_(15); C1_(2); C1_(3);
}

struct Ctx { unsigned char* flg; ldsp sig; int lane; int w; };
DI unsigned now32() { return (unsigned)__builtin_amdgcn_s_memrealtime(); }
struct Clk { unsigned c, r; };
DI Clk clk_now() { Clk k; k.c = (unsigned)__builtin_amdgcn_s_memtime(); k.r = now32(); return k; }
DI unsigned eta(const Clk& k0, int dcyc) {
  const Clk k = clk_now();
  return (k.r + (unsigned)((float)dcyc * (float)(k.r - k0.r) * __builtin_amdgcn_rcpf((float)(k.c - k0.c)))) | 1u;
}
DI void post(const Ctx& c, int b, int slot, const Clk& k0, int dcyc) {
  const unsigned t = eta(k0, dcyc);
  if (c.lane == 0) __hip_atomic_store((GAS unsigned*)(c.flg + b * 128 + slot * 4), t, __ATOMIC_RELAXED, __HIP_MEMORY_SCOPE_AGENT);
}
DI unsigned flag_ld(const Ctx& c, int pb, int slot) { return __hip_atomic_load((GAS unsigned*)(c.flg + pb * 128 + slot * 4), __ATOMIC_RELAXED, __HIP_MEMORY_SCOPE_AGENT); }
template <int SLP = 1> DI void poll_wait(const Ctx& c, int pb, int slot) {
  unsigned f = 0;
#pragma unroll 1
  for (int spin = 0; spin < SPIN_CAP; ++spin) {
    f = __hip_atomic_load((GAS unsigned*)(c.flg + pb * 128 + slot * 4), __ATOMIC_RELAXED, __HIP_MEMORY_SCOPE_AGENT);
    if (__builtin_amdgcn_ballot_w64(f == 0u) == 0ull) break;
    __builtin_amdgcn_s_sleep(SLP);
  }
#pragma unroll 1
  for (int spin = 0; spin < 256; ++spin) {
    if (__builtin_amdgcn_ballot_w64((int)(f - now32()) > 0) == 0ull) break;
    __builtin_amdgcn_s_sleep(1);
  }
  CBAR();
}
DI void lds_sig(const Ctx& c, int idx) {
  CBAR();
  *(volatile LAS unsigned*)(c.sig + idx * 16 + c.w * 4) = 1u;
  CBAR();
}
template <int SLP> DI void lds_wait(const Ctx& c, int idx, unsigned n) {
#pragma unroll 1
  for (int spin = 0; spin < (SPIN_CAP << 6); ++spin) {
    const u32x4 v = *(volatile LAS u32x4*)(c.sig + idx * 16);
    if (__builtin_amdgcn_readfirstlane(v[0] + v[1] + v[2] + v[3]) >= n) break;
    if (SLP) __builtin_amdgcn_s_sleep(SLP);
  }
  CBAR();
}
template <int N, bool SWAP, class F> DI f32x16 gemm_lds(const u32x4 (&wr)[N], ldsp bb, F mid) {
  f32x16 acc = {};
  u32x4 bf[8];
#pragma unroll
  for (int i = 0; i < 8; ++i) bf[i] = lds_r16(bb + i * KB);
  SBAR();
#pragma unroll
  for (int i = 0; i < N; ++i) {
    acc = SWAP ? MFMA(bf[i & 7], wr[i], acc) : MFMA(wr[i], bf[i & 7], acc);
    SBAR();
    if (i + 8 < N) bf[i & 7] = lds_r16(bb + (i + 8) * KB);
    mid(i);
    SBAR();
  }
  return acc;
}
DI void add_res(const f32x16& a, const u32x4& x0, const u32x4& x1, u32x4& y0, u32x4& y1) {
#pragma unroll
  for (int i = 0; i < 4; ++i) {
    y0[i] = pk(a[2 * i] + bflo(x0[i]), a[2 * i + 1] + bfhi(x0[i]));
    y1[i] = pk(a[8 + 2 * i] + bflo(x1[i]), a[8 + 2 * i + 1] + bfhi(x1[i]));
  }
}
DI float row_stats(const u32x4& y0, const u32x4& y1, int h) {
  float s0 = 0.f, s1 = 0.f, q0 = 0.f, q1 = 0.f;
#pragma unroll
  for (int i = 0; i < 4; ++i) { s0 = dot2(y0[i], ONES2, s0); s1 = dot2(y1[i], ONES2, s1); q0 = dot2(y0[i], y0[i], q0); q1 = dot2(y1[i], y1[i], q1); }
  const float s = xhalf_add(s0 + s1), q = xhalf_add(q0 + q1);
  return h ? q : s;
}
DI void stats_finish(float v, float& mu, float& rs) {
  auto t = __builtin_amdgcn_permlane32_swap(__float_as_uint(v), __float_as_uint(v), false, false);
  mu = __uint_as_float(t[0]) * (1.f / D_);
  rs = __builtin_amdgcn_rsqf(__builtin_fmaxf(__uint_as_float(t[1]) * (1.f / D_) - mu * mu, 0.f) + EPS);
}
DI void norm_pack(const f32x16& o, int h, u32x4& y0, u32x4& y1) {
  const float inv = __builtin_amdgcn_rcpf(from_hi(o[12]));
#pragma unroll
  for (int i = 0; i < 4; ++i) { y0[i] = pk(o[2 * i] * inv, o[2 * i + 1] * inv); y1[i] = pk(o[8 + 2 * i] * inv, o[8 + 2 * i + 1] * inv); }
  if (h == 1) { y1[2] = 0; y1[3] = 0; }
}
constexpr float LBIG = 1e30f;
constexpr unsigned NEG31 = 0xF1490000u;

__global__ void __launch_bounds__(512, 2) k_main(Params p) {
  __shared__ __attribute__((aligned(16))) char smem[L_BYTES];
  const ldsp lds = (ldsp)smem;
  const int tid = threadIdx.x;
  const int w8 = __builtin_amdgcn_readfirstlane(tid >> 6);
  const rsrc_t wt = mkrs(p.ws, OFF_PAIR);
  auto nop = []() {};
  const f32x16 Z = {};
  STAMP_DECL;
#define PAIR_SETUP() \
    int lane = tid & 63, b = blockIdx.x, w = w8 & 3; \
    asm volatile("" : "+v"(lane), "+s"(b), "+s"(w)); \
    lane &= 63; b &= 255; w &= 3; \
    const int r = lane & 31, h = lane >> 5, l16 = lane * 16, xcd = b & 7, jj = b >> 3; \
    const int rt = 8 * xcd + (jj >> 2), sub = jj & 3;          \
    const int rb = 2 * xcd + (jj >> 4), hd = jj & 15;          \
    const ldsp scr = lds + L_SCR + l16; \
    unsigned char* const reg = p.ws + OFF_PAIR + pair * PAIR_SZ; \
    const rsrc_t ws = mkrs(reg, 2 * PAIR_SZ - pair * PAIR_SZ); \
    const Ctx cx = {reg + R_FLG, lds + M_SIG + pair * 256, lane, w}; \
    STAMP(0)

  if (w8 < 4) {
    if (tid < 128) *(LAS unsigned*)(lds + M_SIG + tid * 4) = 0u;
    unsigned kvf = 0u;
#pragma unroll 1
   for (int pair = 0; pair < 2; ++pair) {
    PAIR_SETUP();
    const int kvb = R_KVA + w * 64 * KB;
    const int kpb = 256 + xcd * 64 + 16 * w + ((lane >> 1) & 15), kps = h ? 0 : 1 + (lane & 1);
    unsigned rdy = pair ? 0u : 0x55555555u, rem = 0x55555555u, chk = pair ? 14u : 0u;
    auto kv_mask = [&]() { const unsigned bl = (unsigned)__builtin_amdgcn_ballot_w64(kvf != 0u); rdy = bl & (bl >> 1) & 0x55555555u; };
    auto kv_next = [&]() -> int {
      if (__builtin_expect((rdy & rem) == 0u, 0)) {
#pragma unroll 1
        for (int spin = 0; spin < SPIN_CAP; ++spin) {
          kvf = flag_ld(cx, kpb, kps);
          kv_mask();
          if ((rdy & rem) != 0u) break;
          __builtin_amdgcn_s_sleep(2);
        }
        if ((rdy & rem) == 0u) rdy = rem;
        CBAR();
      }
      const int t2 = __builtin_ctz(rdy & rem);
      rem &= ~(1u << t2);
      return kvb + t2 * 2 * KB;
    };
    {
      u32x4 q[4][2], K[4][2], V[4][2];
      int kb[4];
      if (pair) {
#pragma unroll 1
        for (int spin = 0; spin < SPIN_CAP; ++spin) {
          if ((spin & 7) == 7) kvf = flag_ld(cx, kpb, kps);
          rdy = (unsigned)(__builtin_amdgcn_ballot_w64(kvf != 0u && (int)(kvf - now32()) <= 0) >> 32) & 0x55555555u;
          if (__builtin_popcount(rdy) >= 4) break;
          __builtin_amdgcn_s_sleep(1);
        }
        if (__builtin_popcount(rdy) < 4) rdy = rem;
        CBAR();
      }
      __builtin_amdgcn_s_sleep(PRO_DLY);
      STAMP(23);
#pragma unroll
      for (int t = 0; t < 4; ++t) {
        kb[t] = kv_next();
        K[t][0] = ld16<16>(ws, l16, kb[t]); K[t][1] = ld16<16>(ws, l16 + KB, kb[t]);
        V[t][0] = ld16<16>(ws, l16 + 2 * KB, kb[t]); if (t < 3) V[t][1] = ld16<16>(ws, l16 + 3 * KB, kb[t]);
        SBAR();
      }
      if (pair) {
        chk_handoff<2>(K[0], ws, l16, kb[0], 6); chk_handoff<2>(V[0], ws, l16 + 2 * KB, kb[0], 6);
        rdy = 0u;
      }
      f32x16 o0 = {}, o1 = {}, o2 = {}, o3 = {};
      PIN(o0); PIN(o1); PIN(o2); PIN(o3);
      if (pair) kvf = flag_ld(cx, kpb, kps);
      STAMP(24);
      const Clk cka = clk_now();
      BAR();
#pragma unroll
      for (int a = 0; a < 4; ++a) { q[a][0] = lds_r16(lds + L_QF + l16 + 2 * a * KB); q[a][1] = lds_r16(lds + L_QF + l16 + (2 * a + 1) * KB); }
      STAMP(1);
      f32x16 sa = MFMA(K[0][0], q[0][0], Z), sb;
      sa = MFMA(K[0][1], q[0][1], sa);
      u32x4 pp = {};
      V[3][1] = u32x4{};
#define TILE(c, LD, LDV1) { \
        constexpr int n = (c + 1) & 3, pv = (c + 3) & 3; \
        auto ldV1 = [&]() { if (LDV1) V[pv][1] = ld16<16>(ws, l16 + 3 * KB, kb[pv]); }; \
        auto ldK = [&]() { if (LD) { kb[c] = kv_next(); K[c][0] = ld16<16>(ws, l16, kb[c]); K[c][1] = ld16<16>(ws, l16 + KB, kb[c]); } }; \
        auto ldV0 = [&]() { if (LD) V[c][0] = ld16<16>(ws, l16 + 2 * KB, kb[c]); }; \
        unit<true, true>(sa, sb, Z, o0, o3, pp, K[c][0], K[c][1], q[1][0], q[1][1], V[c][0], V[pv][1], ldV1, nop, nop); \
        unit<true, true>(sb, sa, Z, o1, o0, pp, K[c][0], K[c][1], q[2][0], q[2][1], V[c][0], V[c][1], nop, nop, nop); \
        unit<true, true>(sa, sb, Z, o2, o1, pp, K[c][0], K[c][1], q[3][0], q[3][1], V[c][0], V[c][1], nop, ldK, nop); \
        if (LD && __builtin_expect((chk >> n) & 1u, 0)) { chk &= ~(1u << n); chk_handoff<2>(K[n], ws, l16, kb[n], 6); chk_handoff<2>(V[n], ws, l16 + 2 * KB, kb[n], 6); } \
        unit<true, true>(sb, sa, Z, o3, o2, pp, K[n][0], K[n][1], q[0][0], q[0][1], V[c][0], V[c][1], nop, nop, ldV0); }
#pragma unroll 1
      for (int tt = 0; tt < 12; tt += 4) {
        if (pair) { kv_mask(); kvf = flag_ld(cx, kpb, kps); }
        TILE(0, true, true) TILE(1, true, true)
        if (pair) { kv_mask(); kvf = flag_ld(cx, kpb, kps); }
        TILE(2, true, true) TILE(3, true, true)
      }
      STAMP(2);
      TILE(0, false, true) TILE(1, false, false)
      STAMP(3);
      const ldsp red = scr + w * 4 * KB;
      if (w == 0) { lds_wait<1>(cx, 8, 4u); post(cx, b, 0, cka, DT_OA); }
#define WR_LO(o, a) [&]() { lds_wf4(red + (a) * 16 * KB, f32x4{o[0], o[1], o[2], o[3]}); lds_wf4(red + (a) * 16 * KB + KB, f32x4{o[4], o[5], o[6], o[7]}); }
#define WR_HI(o, a) [&]() { lds_wf4(red + (a) * 16 * KB + 2 * KB, f32x4{o[8], o[9], o[10], o[11]}); lds_wf4(red + (a) * 16 * KB + 3 * KB, f32x4{o[12], o[13], o[14], o[15]}); lds_sig(cx, a); }
      unit<true, true>(sa, sb, Z, o0, o3, pp, K[3][0], K[3][1], q[0][0], q[0][1], V[2][0], V[1][1], nop, nop, nop);
      unit<true, true>(sb, sa, Z, o0, o0, pp, K[2][0], K[2][1], q[1][0], q[1][1], V[3][0], V[2][1], nop, nop, nop);
      unit<true, true>(sa, sb, Z, o1, o0, pp, K[3][0], K[3][1], q[1][0], q[1][1], V[2][0], V[3][1], nop, WR_LO(o0, 0), WR_HI(o0, 0));
      unit<true, true>(sb, sa, Z, o1, o1, pp, K[2][0], K[2][1], q[2][0], q[2][1], V[3][0], V[2][1], nop, nop, nop);
      unit<true, true>(sa, sb, Z, o2, o1, pp, K[3][0], K[3][1], q[2][0], q[2][1], V[2][0], V[3][1], nop, WR_LO(o1, 1), WR_HI(o1, 1));
      unit<true, true>(sb, sa, Z, o2, o2, pp, K[2][0], K[2][1], q[3][0], q[3][1], V[3][0], V[2][1], nop, nop, nop);
      unit<true, true>(sa, sb, Z, o3, o2, pp, K[3][0], K[3][1], q[3][0], q[3][1], V[2][0], V[3][1], nop, WR_LO(o2, 2), WR_HI(o2, 2));
      unit<false, true>(sb, sa, Z, o3, o3, pp, K[2][0], K[2][1], q[3][0], q[3][1], V[3][0], V[2][1], nop, nop, nop);
      o3 = MFMA(V[3][1], pp, o3);
      WR_LO(o3, 3)(); WR_HI(o3, 3)();
    }
    STAMP(4);
    {
      u32x4 of[8] = {};
      const int src = R_OFA + (rt * 32 + 8 * w) * KB;
      if (w != sub) {
        poll_wait(cx, (((jj & ~3) | w) << 3) | xcd, 0);
        STAMP(5);
        if (DLY_OA) __builtin_amdgcn_s_sleep(DLY_OA);
        ld_handoff<8>(of, ws, l16, src);
      }
      BAR();
      STAMP(6);
      const unsigned bad = __builtin_amdgcn_readfirstlane(*(LAS unsigned*)(cx.sig + 10 * 16));
      if (__builtin_expect((bad >> w) & 1u, 0)) {
        const u32x4 q0 = lds_r16(lds + L_QF + l16 + 2 * w * KB);
        u32x4 q1 = lds_r16(lds + L_QF + l16 + (2 * w + 1) * KB);
        if (h == 1) { q1[2] = 0; q1[3] = 0; }
        float m = -INFINITY;
#pragma unroll 1
        for (int t = 0; t < 64; ++t) {
          f32x16 s = MFMA(ld16<16>(ws, l16, R_KVA + t * 4 * KB), q0, Z);
          s = MFMA(ld16<16>(ws, l16 + KB, R_KVA + t * 4 * KB), q1, s);
          m = max16(s, m);
        }
        set_shift(q1, xhalf_max(m), h, 0u);
        f32x16 o = {};
#pragma unroll 1
        for (int t = 0; t < 64; ++t) {
          f32x16 s = MFMA(ld16<16>(ws, l16, R_KVA + t * 4 * KB), q0, Z);
          s = MFMA(ld16<16>(ws, l16 + KB, R_KVA + t * 4 * KB), q1, s);
          u32x4 p0, p1;
          exp_pack(s, p0, p1);
          o = MFMA(ld16<16>(ws, l16 + 2 * KB, R_KVA + t * 4 * KB), p0, o);
          o = MFMA(ld16<16>(ws, l16 + 3 * KB, R_KVA + t * 4 * KB), p1, o);
        }
        u32x4 y0, y1;
        norm_pack(o, h, y0, y1);
        st16<16>(y0, ws, l16, R_OFA + (rt * 32 + sub * 8 + 2 * w) * KB);
        st16<16>(y1, ws, l16 + KB, R_OFA + (rt * 32 + sub * 8 + 2 * w) * KB);
        lds_w16(scr + (sub * 8 + 2 * w) * KB, y0); lds_w16(scr + (sub * 8 + 2 * w + 1) * KB, y1);
      }
      if (w != sub) {
        chk_handoff<8>(of, ws, l16, src, 0);
        STAMP(7);
#pragma unroll
        for (int i = 0; i < 8; ++i) lds_w16(scr + (8 * w + i) * KB, of[i]);
      }
      BAR();
    }
    STAMP(8);
    f32x4 ck[4], cq[4];
    const float cv = __uint_as_float(ld4<0>(wt, r * 4, OFF_CS + (16 + hd * 3 + 2) * 128));
#pragma unroll
    for (int g = 0; g < 4; ++g) {
      cq[g] = __builtin_bit_cast(f32x4, ld16<0>(wt, (8 * g + 4 * h) * 4, OFF_CS + (16 + hd * 3) * 128));
      ck[g] = __builtin_bit_cast(f32x4, ld16<0>(wt, (8 * g + 4 * h) * 4 + 128, OFF_CS + (16 + hd * 3) * 128));
    }
    BAR();
    STAMP(9);
    u32x4 q0, q1, k0, k1, v0, v1;
    const Clk ck0 = clk_now();
    {
      const int xo = R_XF1 + (rb * 4 + w) * 28 * KB, so = R_ST1 + (rb * 4 + w) * 14 * 256;
      const ldsp wb0 = opaque(lds + L_WB + l16), wb2 = opaque(lds + L_WB + 56 * KB + l16);
      const ldsp st = lds + M_ST + w * 256;
      f32x4 m4[4], r4[4];
      poll_wait(cx, (((jj & ~15) | (w << 2) | (lane & 3)) << 3) | xcd, 1);
      STAMP(10);
      if (DLY_X1) __builtin_amdgcn_s_sleep(DLY_X1);
      f32x16 aq, ak, av;
      float mu, rs;
      {
        u32x4 xf[28], wr[3][3];
        unsigned sv[14];
#define LDX(i) do { xf[i] = ld16<16>(ws, l16 + ((i) & 3) * KB, xo + ((i) >> 2) * 4 * KB); SBAR(); } while (0)
#define LDS_(i) do { sv[i] = ld4<16>(ws, lane * 4 + (i) * 256, so); SBAR(); } while (0)
#define CHKX(i) do { if (__builtin_expect(__builtin_amdgcn_ballot_w64(smax(0u, xf[i]) == SENT) != 0ull, 0)) { \
          _Pragma("unroll 1") for (int spin = 0; spin < SPIN_CAP; ++spin) { CBAR(); RETRY(1); LDX(i); if (__builtin_amdgcn_ballot_w64(smax(0u, xf[i]) == SENT) == 0ull) break; } } SBAR(); } while (0)
#pragma unroll
        for (int i = 0; i < X1_AHEAD; ++i) LDX(i);
#pragma unroll
        for (int i = 0; i < 2; ++i) { wr[i][0] = lds_r16(wb0 + i * KB); wr[i][1] = lds_r16(wb0 + (28 + i) * KB); wr[i][2] = lds_r16(wb2 + i * KB); }
        SBAR();
        CHKX(0);
        unsigned mx = 0;
        float t0 = 0.f, t1 = 0.f;
#pragma unroll
        for (int ks = 0; ks < 28; ++ks) {
          aq = MFMA(wr[ks % 3][0], xf[ks], ks ? aq : Z); SBAR();
          if (ks + 2 < 28) { wr[(ks + 2) % 3][0] = lds_r16(wb0 + (ks + 2) * KB); wr[(ks + 2) % 3][1] = lds_r16(wb0 + (28 + ks + 2) * KB); }
          if (X1_AHEAD + 2 * ks < 28) LDX(X1_AHEAD + 2 * ks); else if (X1_AHEAD + 2 * ks < 42) LDS_(X1_AHEAD + 2 * ks - 28);
          SBAR();
          ak = MFMA(wr[ks % 3][1], xf[ks], ks ? ak : Z); SBAR();
          if (X1_AHEAD + 2 * ks + 1 < 28) LDX(X1_AHEAD + 2 * ks + 1); else if (X1_AHEAD + 2 * ks + 1 < 42) LDS_(X1_AHEAD + 2 * ks + 1 - 28);
          if (ks + 2 < 28) wr[(ks + 2) % 3][2] = lds_r16(wb2 + (ks + 2) * KB);
          if (ks >= 19 && ks < 23) {
#pragma unroll
            for (int i = (ks - 19) * 4; i < (ks - 19) * 4 + 4 && i < 14; i += 2) { t0 += __uint_as_float(sv[i]); t1 += __uint_as_float(sv[i + 1]); mx = umax3(mx, sv[i], sv[i + 1]); }
            PIN(t0); PIN(t1); PIN(mx);
          }
          if (ks == 23) {
            if (__builtin_expect(__builtin_amdgcn_ballot_w64(mx == SENT) != 0ull, 0)) {
#pragma unroll 1
              for (int spin = 0; spin < SPIN_CAP; ++spin) {
                CBAR(); RETRY(1);
                t0 = 0.f; t1 = 0.f; mx = 0u;
#pragma unroll
                for (int i = 0; i < 14; ++i) LDS_(i);
#pragma unroll
                for (int i = 0; i < 14; i += 2) { t0 += __uint_as_float(sv[i]); t1 += __uint_as_float(sv[i + 1]); mx = umax3(mx, sv[i], sv[i + 1]); }
                if (__builtin_amdgcn_ballot_w64(mx == SENT) == 0ull) break;
              }
            }
            stats_finish(t0 + t1, mu, rs); PIN(mu); PIN(rs);
          }
          if (ks == 24) { *(LAS float*)(st + lane * 4) = h ? rs : mu; CBAR(); }
          if (ks == 26) {
#pragma unroll
            for (int g = 0; g < 4; ++g) { m4[g] = lds_rf4(st + (8 * g + 4 * h) * 4); r4[g] = lds_rf4(st + 128 + (8 * g + 4 * h) * 4); }
          }
          SBAR();
          av = MFMA(xf[ks], wr[ks % 3][2], ks ? av : Z); SBAR();
          if (ks + 1 < 28) CHKX(ks + 1);
          if (ks == 0) { STAMP(11); } if (ks == 13) { STAMP(12); }
          if (ks == POST_KV) post(cx, b, 4 + w, ck0, DT_KV);
        }
      }
      STAMP(13);
      const int ko = R_KVB + (hd * 64 + rb * 4 + w) * 4 * KB;
#pragma unroll
      for (int i = 0; i < 16; ++i) ak[i] = (ak[i] - mu * ck[i >> 2][i & 3]) * rs;
      k0 = pack8(ak, 0); k1 = pack8(ak, 1);
      if (h == 1) { k1[2] = ONES2; k1[3] = 0x00003F80u; }
      st16<16>(k0, ws, l16, ko); st16<16>(k1, ws, l16 + KB, ko);
      SBAR();
#pragma unroll
      for (int i = 0; i < 16; ++i) av[i] = (av[i] - m4[i >> 2][i & 3] * cv) * r4[i >> 2][i & 3];
      v0 = pack8(av, 0); v1 = pack8(av, 1);
      if (r >= 28) { const unsigned f = r == 28 ? ONES2 : 0u; v0 = u32x4{f, f, f, f}; v1 = v0; }
      st16<16>(v0, ws, l16 + 2 * KB, ko); st16<16>(v1, ws, l16 + 3 * KB, ko);
      SBAR();
      lds_w16(scr + (32 + 4 * w) * KB, k0); lds_w16(scr + (33 + 4 * w) * KB, k1);
      lds_w16(scr + (34 + 4 * w) * KB, v0); lds_w16(scr + (35 + 4 * w) * KB, v1);
      lds_sig(cx, 4);
      const float rq = rs * C2;
#pragma unroll
      for (int i = 0; i < 16; ++i) aq[i] = (aq[i] - mu * cq[i >> 2][i & 3]) * rq;
      q0 = pack8(aq, 0); q1 = pack8(aq, 1);
      if (h == 1) { q1[2] = 0; q1[3] = NEG31; }
    }
    STAMP(14);
    {
      f32x16 cd, cf;
#pragma unroll
      for (int i = 0; i < 16; ++i) {
        const int kk = (i & 3) + 8 * (i >> 2) + 4 * h;
        cd[i] = kk <= r ? 0.f : -1e30f; cf[i] = kk <= r ? -1e30f : 0.f;
      }
      f32x16 sa = MFMA(k0, q0, cd), sb;
      sa = MFMA(k1, q1, sa);
      set_shift(q1, xhalf_max(max16(sa, sa[0])), h, NEG31);
      const ldsp kp = scr + (8 + w) * 4 * KB;
      auto need = [&](int u) {
        const int ti = 8 + w - u;
        if (u == 1 && w > 0) lds_wait<0>(cx, 4, 4u);
        if (__builtin_expect((ti & 3) == 3, 0)) lds_wait<1>(cx, 5 + (ti >> 2), 2u);
      };
      u32x4 K[2][2], V[2][2], pp;
      f32x16 o = {};
      sa = MFMA(k0, q0, cd); sa = MFMA(k1, q1, sa);
      V[0][0] = v0; V[0][1] = v1;
      need(1);
      STAMP(15);
      K[1][0] = lds_r16(kp - 4 * KB); K[1][1] = lds_r16(kp - 3 * KB); V[1][0] = lds_r16(kp - 2 * KB);
#pragma unroll
      for (int u = 0; u < 9; ++u) {
        const int c = u & 1, n = c ^ 1;
        auto ha = [&]() {
          if (u + 1 < 9) V[n][1] = lds_r16(kp - (u + 1) * 4 * KB + 3 * KB);
          if (u + 2 < 9) { need(u + 2); K[c][0] = lds_r16(kp - (u + 2) * 4 * KB); K[c][1] = lds_r16(kp - (u + 2) * 4 * KB + KB); }
        };
        auto hc = [&]() { if (u + 2 < 9) V[c][0] = lds_r16(kp - (u + 2) * 4 * KB + 2 * KB); if (u >= 4 && u <= 6 && __builtin_expect(u == (w < 2 ? 4 : 3 + w), 0)) post(cx, b, 8 + w, ck0, DT_OB + (8 - u) * DT_UNIT); };
        f32x16& s = c ? sb : sa; f32x16& sn = c ? sa : sb;
        if (u == 4) { STAMP(16); }
        if (u == 0) unit<true, false>(s, sn, Z, o, o, pp, K[n][0], K[n][1], q0, q1, V[c][0], V[n][1], ha, nop, hc);
        else if (u < 7) unit<true, true>(s, sn, Z, o, o, pp, K[n][0], K[n][1], q0, q1, V[c][0], V[n][1], ha, nop, hc);
        else if (u == 7) unit<true, true>(s, sn, cf, o, o, pp, K[n][0], K[n][1], q0, q1, V[c][0], V[n][1], ha, nop, hc);
        else unit<false, true>(s, sn, Z, o, o, pp, K[n][0], K[n][1], q0, q1, V[c][0], V[n][1], ha, nop, hc);
      }
      o = MFMA(V[0][1], pp, o);
      if (__builtin_expect(__builtin_amdgcn_ballot_w64(h == 1 && !(o[12] < LBIG)) != 0ull, 0)) {
        if (h == 1) { q1[2] = 0; q1[3] = NEG31; }
        float m = -INFINITY;
#pragma unroll 1
        for (int u = 0; u < 9; ++u) {
          f32x16 s;
#pragma unroll
          for (int i = 0; i < 16; ++i) s[i] = u == 0 ? cd[i] : u == 8 ? cf[i] : 0.f;
          s = MFMA(lds_r16(kp - u * 4 * KB), q0, s); s = MFMA(lds_r16(kp - u * 4 * KB + KB), q1, s);
          m = max16(s, m);
        }
        set_shift(q1, xhalf_max(m), h, NEG31);
        o = Z;
#pragma unroll 1
        for (int u = 0; u < 9; ++u) {
          f32x16 s;
#pragma unroll
          for (int i = 0; i < 16; ++i) s[i] = u == 0 ? cd[i] : u == 8 ? cf[i] : 0.f;
          s = MFMA(lds_r16(kp - u * 4 * KB), q0, s); s = MFMA(lds_r16(kp - u * 4 * KB + KB), q1, s);
          u32x4 p0, p1;
          exp_pack(s, p0, p1);
          o = MFMA(lds_r16(kp - u * 4 * KB + 2 * KB), p0, o); o = MFMA(lds_r16(kp - u * 4 * KB + 3 * KB), p1, o);
        }
      }
      u32x4 y0, y1;
      norm_pack(o, h, y0, y1);
      const int oo = R_OFB + ((rb * 4 + w) * 16 + hd) * 2 * KB;
      st16<16>(y0, ws, l16, oo); st16<16>(y1, ws, l16 + KB, oo);
      lds_sig(cx, 9);
    }
    STAMP(17);
    u32x4 wa1[WA1];
    {
      u32x4 of[8];
      const int src = R_OFB + (rt * 32 + 8 * w) * KB;
#pragma unroll
      for (int i = 0; i < WA1; ++i) { wa1[i] = ld16<0>(wt, l16 + (i & 3) * KB, pair ? OFF_PAIR : OFF_WAT + ((4 * sub + w) * 28 + (i & ~3)) * KB); SBAR(); }
      poll_wait(cx, (((jj & ~15) | (w << 2) | (lane & 3)) << 3) | xcd, 8 + (rt & 3));
      STAMP(18);
      if (DLY_OB) __builtin_amdgcn_s_sleep(DLY_OB);
      ld_handoff<8>(of, ws, l16, src);
      chk_handoff<8>(of, ws, l16, src, 3);
      STAMP(19);
      lds_wait<1>(cx, 9, 4u);
#pragma unroll
      for (int i = 0; i < 8; ++i) lds_w16(scr + (8 * w + i) * KB, of[i]);
      BAR();
      STAMP(20);
    }
    if (pair == 0) {
      u32x4 wa[28];
#pragma unroll
      for (int i = 0; i < WA1; ++i) wa[i] = wa1[i];
#pragma unroll
      for (int i = WA1; i < 28; ++i) { wa[i] = ld16<0>(wt, l16 + (i & 3) * KB, OFF_WAT + ((4 * sub + w) * 28 + (i & ~3)) * KB); SBAR(); }
      const int tile = 4 * sub + w;
      f32x4 cq[4];
#pragma unroll
      for (int g = 0; g < 4; ++g) cq[g] = __builtin_bit_cast(f32x4, ld16<0>(wt, (8 * g + 4 * h) * 4, OFF_CS + tile * 128));
      const float cs = __uint_as_float(ld4<0>(wt, r * 4, OFF_CS + tile * 128));
      BAR();
      STAMP(21);
      if (w == 0 || tile == 14) {
        const unsigned t = eta(ck0, DT_Q);
        if (lane < (w == 0 ? 1 : 8))
          __hip_atomic_store((GAS unsigned*)(cx.flg + PAIR_SZ + (w == 0 ? b * 128 + 3 * 4 : (256 + lane * 64 + rt) * 128)), t, __ATOMIC_RELAXED, __HIP_MEMORY_SCOPE_AGENT);
      }
      const float mu = *(LAS float*)(lds + M_ST + 4 * 256 + r * 4), rs = *(LAS float*)(lds + M_ST + 4 * 256 + 128 + r * 4);
      auto ldeta = [&](int i) { if (i == ETA_AT) kvf = flag_ld(cx, kpb + PAIR_SZ / 128, kps); };
      u32x4 y0, y1;
      if (tile != 15) {
        f32x16 acc = gemm_lds<28, false>(wa, scr + 32 * KB, ldeta);
        const float sc = tile < 14 ? rs * C2 : rs;
#pragma unroll
        for (int i = 0; i < 16; ++i) acc[i] = (acc[i] - mu * cq[i >> 2][i & 3]) * sc;
        y0 = pack8(acc, 0); y1 = pack8(acc, 1);
        if (tile == 14 && h == 1) { y1[2] = ONES2; y1[3] = 0x00003F80u; }
      } else {
        const ldsp st = lds + M_ST + 4 * 256;
        f32x4 r4[4];
#pragma unroll
        for (int g = 0; g < 4; ++g) { cq[g] = lds_rf4(st + (8 * g + 4 * h) * 4); r4[g] = lds_rf4(st + 128 + (8 * g + 4 * h) * 4); }
        f32x16 acc = gemm_lds<28, true>(wa, scr + 32 * KB, ldeta);
#pragma unroll
        for (int i = 0; i < 16; ++i) acc[i] = (acc[i] - cq[i >> 2][i & 3] * cs) * r4[i >> 2][i & 3];
        y0 = pack8(acc, 0); y1 = pack8(acc, 1);
        if (r >= 28) { const unsigned f = r == 28 ? ONES2 : 0u; y0 = u32x4{f, f, f, f}; y1 = y0; }
      }
      const int dst = PAIR_SZ + (tile < 14 ? R_QA + (rt * 14 + tile) * 2 * KB : R_KVA + rt * 4 * KB + (tile - 14) * 2 * KB);
      st16<16>(y0, ws, l16, dst); st16<16>(y1, ws, l16 + KB, dst);
      STAMP(25);
      if (tile >= 14) {
        asm volatile("s_waitcnt vmcnt(0)" ::: "memory");
        if (lane < 8) __hip_atomic_store((GAS unsigned*)(cx.flg + PAIR_SZ + (256 + lane * 64 + rt) * 128 + (tile - 13) * 4), 1u, __ATOMIC_RELAXED, __HIP_MEMORY_SCOPE_AGENT);
      }
    }
    STAMP(22); STAMP_DUMP();
   }
   return;
  }

  u32x4 x0 = {}, x1 = {};
#pragma unroll 1
 for (int pair = 0; pair < 2; ++pair) {
  PAIR_SETUP();
  __builtin_amdgcn_s_setprio(2);
  const int ct0 = sub < 2 ? 4 * sub : 3 * sub + 2, nct = sub < 2 ? 4 : 3;
  const bool ctok = w < nct;
  const int ct = ct0 + (ctok ? w : 0);
  const int xo1 = R_XF1 + (rt * 28 + ct * 2) * KB, xo2 = R_XF2 + (rt * 28 + ct * 2) * KB;
  const int so1 = R_ST1 + (rt * 14 + ct) * 256, so2 = R_ST2 + (rt * 14 + ct) * 256;
  {
    const int qa = R_QA + rt * 28 * KB;
    if (pair) poll_wait<2>(cx, (((jj & ~3) | (lane & 3)) << 3) | xcd, 3);
    STAMP(16);
    u32x4 q[2], k0, k1;
#pragma unroll 1
    for (int spin = 0; spin < SPIN_CAP; ++spin) {
      CBAR();
#pragma unroll
      for (int s = 0; s < 2; ++s)
#pragma unroll
        for (int jh = 0; jh < 2; ++jh) {
          const int dg = 4 * s + 2 * jh + h, G = 7 * (4 * sub + w) + (dg < 7 ? dg : 6);
          const int g8 = G & 7;
          u32x2 v = ld8<16>(ws, ((G >> 3) * 2 + (g8 >> 2)) * KB + (r + 32 * (g8 & 1)) * 16 + ((g8 >> 1) & 1) * 8, qa);
          if (dg == 7) { v[0] = 0; v[1] = 0; }
          q[s][2 * jh] = v[0]; q[s][2 * jh + 1] = v[1];
        }
      k0 = ld16<16>(ws, l16, R_KVA + rt * 4 * KB); k1 = ld16<16>(ws, l16 + KB, R_KVA + rt * 4 * KB);
      if (pair == 0 || __builtin_amdgcn_ballot_w64(smax(smax(smax(smax(0u, q[0]), q[1]), k0), k1) == SENT) == 0ull) break;
      RETRY(7);
    }
    f32x16 s0 = MFMA(k0, q[0], Z);
    s0 = MFMA(k1, q[1], s0);
    lds_w16(lds + L_QF + l16 + 2 * w * KB, q[0]);
    set_shift(q[1], xhalf_max(max16(s0, s0[0])), h, 0u);
    lds_w16(lds + L_QF + l16 + (2 * w + 1) * KB, q[1]);
    STAMP(17);
    BAR();
  }
  STAMP(1);
  __builtin_amdgcn_s_setprio(HPRIO);
  u32x4 wf[32];
  Clk ck0 = clk_now();
  {
    const u32x4 sv = {SENT, SENT, SENT, SENT};
#define PC() do { SBAR(); if (PACE) __builtin_amdgcn_s_sleep(PACE); SBAR(); } while (0)
    if (NOH) { for (int i = 0; i < NOH; ++i) __builtin_amdgcn_s_sleep(127); }
    auto fill = [&](int i) {
      const int hi = (i & 1) * KB;
      if (i < 2) st16<16>(sv, ws, l16 + hi, R_OFA + (rt * 32 + sub * 8 + 2 * w) * KB);
      else if (i < 4) { if (ctok) { st16<16>(sv, ws, l16 + hi, xo1); if (i == 2) st4<16>(SENT, ws, lane * 4, so1); } }
      else if (i < 8) st16<16>(sv, ws, l16 + (i - 4) * KB, R_KVB + (hd * 64 + rb * 4 + w) * 4 * KB);
      else if (i < 10) st16<16>(sv, ws, l16 + hi, R_OFB + ((rb * 4 + w) * 16 + hd) * 2 * KB);
      else if (i < 12) { if (ctok && pair == 0) { st16<16>(sv, ws, l16 + hi, xo2); if (i == 10) st4<16>(SENT, ws, lane * 4, so2); } }
      else if (pair == 0) st16<16>(sv, ws, l16 + hi, PAIR_SZ + (4 * sub + w < 14 ? R_QA + (rt * 14 + 4 * sub + w) * 2 * KB : R_KVA + rt * 4 * KB + (4 * sub + w - 14) * 2 * KB));
      PC();
    };
    const unsigned char* wbsrc = p.ws + OFF_WBT + (hd * 84 + w * 21) * KB + l16;
    const unsigned wbdst = (unsigned)(uintptr_t)(lds + L_WB) + w * 21 * KB;
#pragma unroll
    for (int i = 0; i < 32; ++i) {
      wf[i] = ld16<0>(wt, l16 + (i & 3) * KB, OFF_WOA + (ct * 32 + (i & ~3)) * KB); PC();
      if (i % 3 != 2 || i > 29) { const int g = i - (i + 1) / 3; if (g < 21) { if (pair == 0) glds16(wbsrc + g * KB, wbdst + g * KB); else __builtin_amdgcn_s_sleep(PACE3); PC(); } }
      if ((i & 1) == 0 && i < 28) fill(i >> 1);
      if (i == 10) { asm volatile("s_waitcnt vmcnt(5)" ::: "memory"); lds_sig(cx, 8); }
    }
    if (pair == 0) {
      const unsigned short* xr = p.x + (size_t)(rt * 32 + r) * D_ + ct * 32 + 4 * h;
      const u32x2 a0 = *(const u32x2*)xr, a1 = *(const u32x2*)(xr + 8), a2 = *(const u32x2*)(xr + 16), a3 = *(const u32x2*)(xr + 24);
      x0 = u32x4{a0[0], a0[1], a1[0], a1[1]}; x1 = u32x4{a2[0], a2[1], a3[0], a3[1]};
    }
    if (WARM && pair == 0) {
      const int piece = (jj * 4 + w) * 3584;
      unsigned t = 0;
      if (lane < 28) t = ld4<0>(wt, piece + lane * 128, OFF_WOB) ;
      else if (lane < 56) t = ld4<0>(wt, piece + (lane - 28) * 128, OFF_WAT);
      asm volatile("" :: "v"(t));
    }
    asm volatile("s_waitcnt vmcnt(0)" ::: "memory");
  }
  STAMP(2);
  {
    u32x4 keep[2];
    const int s = w & 1;
    __builtin_amdgcn_s_setprio(2);
    ck0 = clk_now();
#pragma unroll
    for (int it = 0; it < 2; ++it) {
      const int T = 2 * it + (w >> 1);
      if (it == 0) lds_wait<2>(cx, T, 4u); else lds_wait<1>(cx, T, 4u);

      const ldsp red = scr + T * 16 * KB;
      f32x4 a0[4], a1[4]; float l4[4];
#pragma unroll
      for (int k = 0; k < 4; ++k) {
        l4[k] = *(LAS float*)(lds + L_SCR + T * 16 * KB + k * 4 * KB + 3 * KB + (32 + r) * 16);
        a0[k] = lds_rf4(red + k * 4 * KB + (2 * s) * KB);
        a1[k] = lds_rf4(red + k * 4 * KB + (2 * s + 1) * KB);
      }
      const float l = (l4[0] + l4[1]) + (l4[2] + l4[3]);
      const f32x4 b0 = (a0[0] + a0[1]) + (a0[2] + a0[3]), b1 = (a1[0] + a1[1]) + (a1[2] + a1[3]);
      const float inv = __builtin_amdgcn_rcpf(l);
      u32x4 o = {pk(b0[0] * inv, b0[1] * inv), pk(b0[2] * inv, b0[3] * inv), pk(b1[0] * inv, b1[1] * inv), pk(b1[2] * inv, b1[3] * inv)};
      if (s == 1 && h == 1) { o[2] = 0; o[3] = 0; }
      if (__builtin_expect(__builtin_amdgcn_ballot_w64(!(l < LBIG)) == 0ull, 1)) st16<16>(o, ws, l16, R_OFA + (rt * 32 + sub * 8 + 2 * T + s) * KB);
      else if (lane == 0) __hip_atomic_fetch_or((LAS unsigned*)(cx.sig + 10 * 16), 1u << T, __ATOMIC_RELAXED, __HIP_MEMORY_SCOPE_WORKGROUP);
      keep[it] = o;
      if (it == 0) { STAMP(3); } else { STAMP(4); }
    }
    BAR();
    STAMP(5);
    const unsigned bad = __builtin_amdgcn_readfirstlane(*(LAS unsigned*)(cx.sig + 10 * 16));
#pragma unroll
    for (int it = 0; it < 2; ++it) {
      const int T = 2 * it + (w >> 1);
      if (!((bad >> T) & 1u)) lds_w16(scr + (sub * 8 + 2 * T + s) * KB, keep[it]);
    }
    BAR();
  }
  STAMP(6);
  {
    const f32x16 acc = gemm_lds<32, false>(wf, scr, [&](int i) { if (i == 0 && w == 0) post(cx, b, 1, ck0, DT_X); if (i == 24) BAR();   });
    add_res(acc, x0, x1, x0, x1);
    if (ctok) { st16<16>(x0, ws, l16, xo1); st16<16>(x1, ws, l16 + KB, xo1); SBAR(); st4<16>(__float_as_uint(row_stats(x0, x1, h)), ws, lane * 4, so1); }
  }
  STAMP(7);
  {
    __builtin_amdgcn_s_setprio(0);
    const int kt = 4 * rb - 8 + 2 * w;
    u32x4 hv[8];
    STAMP(8);
    if (kt >= 0) {
      const int prb = kt >> 2;
      __builtin_amdgcn_s_setprio(0);
      poll_wait<4>(cx, (((prb & 1) * 16 + hd) << 3) | (prb >> 1), 4 + (kt & 3) + (lane & 1));
      __builtin_amdgcn_s_setprio(2);
      STAMP(9);
      if (DLY_KV) __builtin_amdgcn_s_sleep(DLY_KV);
      ld_handoff<8>(hv, ws, l16, R_KVB + (hd * 64 + kt) * 4 * KB);
      chk_handoff<8>(hv, ws, l16, R_KVB + (hd * 64 + kt) * 4 * KB, 2);
    } else {
#pragma unroll
      for (int i = 0; i < 8; ++i) hv[i] = u32x4{0u, 0u, 0u, ((i & 3) == 1 && h == 1) ? 0x3F800000u : 0u};
    }
#pragma unroll
    for (int i = 0; i < 8; ++i) lds_w16(scr + (8 * w + i) * KB, hv[i]);
    lds_sig(cx, 5 + (w >> 1));
    STAMP(10);
    __builtin_amdgcn_s_setprio(0);
#pragma unroll
    for (int i = 0; i < 32; ++i) { wf[i] = ld16<0>(wt, l16 + (i & 3) * KB, OFF_WOB + (ct * 32 + (i & ~3)) * KB); SBAR(); if (PACE2) __builtin_amdgcn_s_sleep(PACE2); SBAR(); }
    BAR();
  }
  STAMP(11);
  __builtin_amdgcn_s_setprio(2);
  {
    const f32x16 acc = gemm_lds<32, false>(wf, scr, [&](int i) { if (i == 0 && w == 0 && pair == 0) post(cx, b, 2, ck0, DT_X); });
    add_res(acc, x0, x1, x0, x1);
    if (pair == 1) {
      if (ctok) {
        unsigned short* orow = p.out + (size_t)(rt * 32 + r) * D_ + ct * 32 + 4 * h;
        *(u32x2*)orow = u32x2{x0[0], x0[1]}; *(u32x2*)(orow + 8) = u32x2{x0[2], x0[3]};
        *(u32x2*)(orow + 16) = u32x2{x1[0], x1[1]}; *(u32x2*)(orow + 24) = u32x2{x1[2], x1[3]};
      }
      STAMP(12); STAMP_DUMP();
      return;
    }
    if (ctok) { st16<16>(x0, ws, l16, xo2); st16<16>(x1, ws, l16 + KB, xo2); SBAR(); st4<16>(__float_as_uint(row_stats(x0, x1, h)), ws, lane * 4, so2); }
  }
  STAMP(12);
  {
    u32x4 xf[7];
    const int src = R_XF2 + (rt * 28 + 7 * w) * KB;
    poll_wait(cx, (((jj & ~3) | (lane & 3)) << 3) | xcd, 2);
    STAMP(13);
    if (DLY_X2) __builtin_amdgcn_s_sleep(DLY_X2);
    ld_handoff<7>(xf, ws, l16, src);
    if (w == 3) {
#pragma unroll 1
      for (int spin = 0; spin < SPIN_CAP; ++spin) {
        CBAR();
        unsigned sv[14], mx = 0;
#pragma unroll
        for (int i = 0; i < 14; ++i) sv[i] = ld4<16>(ws, lane * 4 + i * 256, R_ST2 + rt * 14 * 256);
        float t0 = 0.f, t1 = 0.f, mu, rs;
#pragma unroll
        for (int i = 0; i < 14; i += 2) { t0 += __uint_as_float(sv[i]); t1 += __uint_as_float(sv[i + 1]); mx = umax3(mx, sv[i], sv[i + 1]); }
        stats_finish(t0 + t1, mu, rs);
        *(LAS float*)(lds + M_ST + 4 * 256 + lane * 4) = h ? rs : mu;
        if (__builtin_amdgcn_ballot_w64(mx == SENT) == 0ull) break;
        RETRY(5);
      }
    }
    chk_handoff<7>(xf, ws, l16, src, 4);
    STAMP(14);
#pragma unroll
    for (int i = 0; i < 7; ++i) lds_w16(scr + (32 + 7 * w + i) * KB, xf[i]);
    BAR();
  }
  STAMP(15); STAMP_DUMP();
 }
}

extern "C" void kernel_launch(void* const* d_in, const int* in_sizes, int n_in, void* d_out, int out_size, void* d_ws, size_t ws_size,
                              hipStream_t stream) {
  Params p;
  p.x = (const unsigned short*)d_in[0];
  p.wq = (const unsigned short*)d_in[1];
  p.wk = (const unsigned short*)d_in[2];
  p.wv = (const unsigned short*)d_in[3];
  p.woa = (const unsigned short*)d_in[4];
  p.wqkv = (const unsigned short*)d_in[6];
  p.wob = (const unsigned short*)d_in[7];
  p.out = (unsigned short*)d_out;
  p.ws = (unsigned char*)d_ws;
  k_pre<<<256, 512, 0, stream>>>(p);
  k_main<<<256, 512, 0, stream>>>(p);
}
```

```cpp
#include <hip/hip_runtime.h>
#include <hip/hip_bf16.h>
#include <stdint.h>

typedef short bf16x8 __attribute__((ext_vector_type(8)));
typedef short s16x4 __attribute__((ext_vector_type(4)));
typedef float f32x16 __attribute__((ext_vector_type(16)));
typedef float f32x4 __attribute__((ext_vector_type(4)));
typedef float f32x2 __attribute__((ext_vector_type(2)));
typedef unsigned u32x4 __attribute__((ext_vector_type(4)));
typedef unsigned u32x2 __attribute__((ext_vector_type(2)));
typedef __bf16 bf16x2_t __attribute__((ext_vector_type(2)));
#define DI __device__ __forceinline__
#define LAS __attribute__((address_space(3)))
#define GAS __attribute__((address_space(1)))
typedef LAS char* ldsp;

#define MFMA(a, b, c) __builtin_amdgcn_mfma_f32_32x32x16_bf16(__builtin_bit_cast(bf16x8, (a)), __builtin_bit_cast(bf16x8, (b)), (c), 0, 0, 0)
#define BAR() asm volatile("s_waitcnt lgkmcnt(0)\n\ts_barrier" ::: "memory")
#define CBAR() asm volatile("" ::: "memory")
#define SBAR() __builtin_amdgcn_sched_barrier(0)

constexpr int S_ = 2048, D_ = 448;
constexpr float C2 = 0.18898223650461362f * 1.4426950408889634f;
constexpr float EPS = 1e-5f;
constexpr unsigned SENT = 0xFFFFFFFFu;
constexpr unsigned ONES2 = 0x3F803F80u;
constexpr int SPIN_CAP = 1 << 14;

constexpr int KB = 1024;
constexpr int OFF_WAT = 0;
constexpr int OFF_WBT = OFF_WAT + 448 * KB;
constexpr int OFF_WOA = OFF_WBT + 1344 * KB;
constexpr int OFF_WOB = OFF_WOA + 448 * KB;
constexpr int OFF_CS = OFF_WOB + 448 * KB;
constexpr int OFF_EPO = OFF_CS + 32 * KB;
constexpr int OFF_INI = OFF_EPO + KB;
constexpr int OFF_P0F = OFF_INI + 16 * KB;
constexpr int OFF_PAIR = OFF_P0F + 8 * KB;
constexpr int QA_SZ = 64 * 28 * KB;
constexpr int R_QA = 0;
constexpr int R_KVA = R_QA + QA_SZ;
constexpr int R_OFA = R_KVA + 256 * KB;
constexpr int R_XF1 = R_OFA + 2048 * KB;
constexpr int R_KVB = R_XF1 + QA_SZ;
constexpr int R_OFB = R_KVB + 4096 * KB;
constexpr int R_XF2 = R_OFB + 2048 * KB;
constexpr int R_ST1 = R_XF2 + QA_SZ;
constexpr int R_ST2 = R_ST1 + 64 * 14 * 256;
constexpr int R_FLG = R_ST2 + 64 * 14 * 256;
constexpr int R_FKV = R_FLG + 32 * KB;
constexpr int PAIR_SZ = R_FKV + 64 * KB;
constexpr int OFF_DBG = OFF_PAIR + 2 * PAIR_SZ;
constexpr int WS_BYTES = OFF_DBG + 2048 * KB;

#define NOSIDE 0
#define NOKV 0
#define HPRIO 0
#define NOH 0
#define SIG_X1 14
#define WOB1 0
#define DT_OA 3700
#define DT_X 1750
#define DT_KV 3500
#define DT_OB 900
#define DT_UNIT 240
#define X1_AHEAD 8
#define WA1 8
#define PRE_AUX 16
#define PRO_DLY 2
#define WARM 1
#define KV_DLY 6
#define Q_DLY 4
#define PACE3 4
#define DT_Q 1600
#define ETA_AT 26
#define RSLP 0
#define QS 14
#define ST_DLY 20
#define DLY_OA 0
#define DLY_X1 0
#define DLY_KV 0
#define DLY_OB 0
#define DLY_X2 0
#define DLY_WOB 24
#define PACE 1
#define PACE2 0
#define POST_OA -1
#define POST_OB 6
#define POST_KV 12
#define POST_GEMM 8

DI unsigned pk(float a, float b) { f32x2 v = {a, b}; bf16x2_t r = __builtin_convertvector(v, bf16x2_t); return __builtin_bit_cast(unsigned, r); }
DI float dot2(unsigned a, unsigned b, float c) { return __builtin_amdgcn_fdot2_f32_bf16(__builtin_bit_cast(bf16x2_t, a), __builtin_bit_cast(bf16x2_t, b), c, false); }
DI float bflo(unsigned u) { return __uint_as_float(u << 16); }
DI float bfhi(unsigned u) { return __uint_as_float(u & 0xFFFF0000u); }
DI float xhalf_add(float v) { auto r = __builtin_amdgcn_permlane32_swap(__float_as_uint(v), __float_as_uint(v), false, false); return __uint_as_float(r[0]) + __uint_as_float(r[1]); }
DI float xhalf_max(float v) { auto r = __builtin_amdgcn_permlane32_swap(__float_as_uint(v), __float_as_uint(v), false, false); return __builtin_fmaxf(__uint_as_float(r[0]), __uint_as_float(r[1])); }
DI float from_hi(float v) { auto r = __builtin_amdgcn_permlane32_swap(__float_as_uint(v), __float_as_uint(v), false, false); return __uint_as_float(r[1]); }
DI float max3f(float a, float b, float c) { return __builtin_fmaxf(__builtin_fmaxf(a, b), c); }
DI float max16(const f32x16& s, float m) {
#pragma unroll
  for (int i = 0; i < 16; i += 2) m = max3f(m, s[i], s[i + 1]);
  return m;
}
typedef __amdgpu_buffer_rsrc_t rsrc_t;
DI rsrc_t mkrs(const void* p, unsigned n) { return __builtin_amdgcn_make_buffer_rsrc((void*)p, 0, n, 0x00020000); }
template <int AUX> DI u32x4 ld16(rsrc_t rs, int voff, int soff) { return __builtin_amdgcn_raw_buffer_load_b128(rs, voff, soff, AUX); }
template <int AUX> DI u32x2 ld8(rsrc_t rs, int voff, int soff) { return __builtin_amdgcn_raw_buffer_load_b64(rs, voff, soff, AUX); }
template <int AUX> DI void st16(u32x4 v, rsrc_t rs, int voff, int soff) { __builtin_amdgcn_raw_buffer_store_b128(v, rs, voff, soff, AUX); }
template <int AUX> DI void st8(u32x2 v, rsrc_t rs, int voff, int soff) { __builtin_amdgcn_raw_buffer_store_b64(v, rs, voff, soff, AUX); }
template <int AUX> DI unsigned ld4(rsrc_t rs, int voff, int soff) { return __builtin_amdgcn_raw_buffer_load_b32(rs, voff, soff, AUX); }
template <int AUX> DI void st4(unsigned v, rsrc_t rs, int voff, int soff) { __builtin_amdgcn_raw_buffer_store_b32(v, rs, voff, soff, AUX); }
DI ldsp opaque(ldsp p) { unsigned a = (unsigned)(uintptr_t)p; asm volatile("" : "+v"(a)); return (ldsp)(uintptr_t)a; }
typedef unsigned long long u64;
DI u64 ld_tag(const unsigned char* a) { return __hip_atomic_load((GAS u64*)a, __ATOMIC_RELAXED, __HIP_MEMORY_SCOPE_AGENT); }
DI void st_tag(unsigned char* a, u64 v) { __hip_atomic_store((GAS u64*)a, v, __ATOMIC_RELAXED, __HIP_MEMORY_SCOPE_AGENT); }
DI u32x4 lds_r16(ldsp p) { return *(LAS u32x4*)p; }
DI f32x4 lds_rf4(ldsp p) { return *(LAS f32x4*)p; }
DI void lds_w16(ldsp p, u32x4 v) { *(LAS u32x4*)p = v; }
DI void lds_wf4(ldsp p, f32x4 v) { *(LAS f32x4*)p = v; }
DI unsigned umax3(unsigned a, unsigned b, unsigned c) { return max(max(a, b), c); }
DI unsigned smax(unsigned m, u32x4 v) { return umax3(umax3(m, v[0], v[1]), v[2], v[3]); }
DI void glds16_sc1(const void* g, unsigned lds_base) {
  unsigned sv;
  asm volatile("s_mov_b32 %0, m0\n\ts_mov_b32 m0, %2\n\ts_nop 0\n\tglobal_load_lds_dwordx4 %1, off sc1\n\ts_mov_b32 m0, %0" : "=&s"(sv) : "v"(g), "s"(lds_base) : "memory");
}
DI void glds16(const void* g, unsigned lds_base) {
  unsigned sv;
  asm volatile("s_mov_b32 %0, m0\n\ts_mov_b32 m0, %2\n\ts_nop 0\n\tglobal_load_lds_dwordx4 %1, off\n\ts_mov_b32 m0, %0" : "=&s"(sv) : "v"(g), "s"(lds_base) : "memory");
}
DI u32x4 pack8(const f32x16& a, int s) {
  u32x4 r;
#pragma unroll
  for (int i = 0; i < 4; ++i) r[i] = pk(a[8 * s + 2 * i], a[8 * s + 2 * i + 1]);
  return r;
}
#define RETRY(tag) do { } while (0)
template <int N> DI void ld_handoff(u32x4 (&v)[N], rsrc_t rs, int voff, int soff) {
#pragma unroll
  for (int i = 0; i < N; ++i) { v[i] = ld16<16>(rs, voff + (i & 3) * KB, soff + (i >> 2) * 4 * KB); SBAR(); }
}
template <int N> DI bool any_sent(const u32x4 (&v)[N]) {
  unsigned m = 0;
#pragma unroll
  for (int i = 0; i < N; ++i) m = smax(m, v[i]);
  return __builtin_amdgcn_ballot_w64(m == SENT) != 0ull;
}
template <int N> DI void chk_handoff(u32x4 (&v)[N], rsrc_t rs, int voff, int soff, int tag) {
  if (__builtin_expect(any_sent<N>(v), 0)) {
#pragma unroll 1
    for (int spin = 0; spin < SPIN_CAP; ++spin) {
      CBAR();
      RETRY(tag);
      if (RSLP) __builtin_amdgcn_s_sleep(RSLP);
#pragma unroll
      for (int i = 0; i < N; ++i)
        if (__builtin_amdgcn_ballot_w64(smax(0u, v[i]) == SENT) != 0ull) v[i] = ld16<16>(rs, voff + (i & 3) * KB, soff + (i >> 2) * 4 * KB);
      if (!any_sent<N>(v)) break;
    }
  }
}

#define STAMP_DECL
#define STAMP_DUMP() do { } while (0)
#define STAMP(i) do { } while (0)

#define SPAN_T(i) do { } while (0)
struct Params {
  const unsigned short *x, *wq, *wk, *wv, *woa, *wqkv, *wob;
  unsigned short* out;
  unsigned char* ws;
};

DI void ln_fix_tok(f32x16& a, ldsp cs, int h, float mu, float sc) {
#pragma unroll
  for (int g = 0; g < 4; ++g) {
    f32x4 c = lds_rf4(cs + (8 * g + 4 * h) * 4);
#pragma unroll
    for (int i = 0; i < 4; ++i) a[4 * g + i] = (a[4 * g + i] - mu * c[i]) * sc;
  }
}
DI void ln_fix_col(f32x16& a, ldsp mup, ldsp rsp, int h, float cs) {
#pragma unroll
  for (int g = 0; g < 4; ++g) {
    f32x4 m = lds_rf4(mup + (8 * g + 4 * h) * 4), r = lds_rf4(rsp + (8 * g + 4 * h) * 4);
#pragma unroll
    for (int i = 0; i < 4; ++i) a[4 * g + i] = (a[4 * g + i] - m[i] * cs) * r[i];
  }
}

constexpr int XI_KS = 1056;
constexpr int P_XI = 0, P_WI = 2 * 28 * XI_KS, P_ST = P_WI + 448 * 128, P_CS = P_ST + 512, P_CS2 = P_CS + 4 * 128, P_SG = P_CS2 + 4 * 128, P_TI = P_SG + 32, P_BYTES = P_TI + 4 * 2048;
DI u64 phase0(const Params& p, const ldsp lds) {
  const int tid = threadIdx.x, lane = tid & 63, r = lane & 31, h = lane >> 5;
  const int w = __builtin_amdgcn_readfirstlane(tid >> 6);
  const int b = blockIdx.x;
  const rsrc_t ws = mkrs(p.ws, WS_BYTES);
  const int pair = -1; (void)pair;
  STAMP_DECL; STAMP(0); SPAN_T(0);
  const u64 epo = ld_tag(p.ws + OFF_EPO);
  const int xcd = b & 7, jj = b >> 3, rb64 = 4 * xcd + (jj >> 3), cp = jj & 7;
  const int q8 = tid & 7, row8 = tid >> 3;
  u32x4 xv[7];
  {
    const unsigned short* xr = p.x + (size_t)(rb64 * 64 + row8) * D_ + q8 * 8;
#pragma unroll
    for (int i = 0; i < 7; ++i) xv[i] = *(const u32x4*)(xr + i * 64);
  }
  u32x4 wv[7];
  if (cp < 7) {
    const unsigned short* wr = p.wq + (size_t)row8 * 448 + cp * 64 + (q8 ^ ((row8 & 2) << 1)) * 8;
#pragma unroll
    for (int i = 0; i < 7; ++i) { glds16(wr + i * 64 * 448, (unsigned)(uintptr_t)(lds + P_WI) + (i * 8 + w) * KB); wv[i] = u32x4{}; }
  } else {
    const unsigned short* wr = ((q8 >> 2) ? p.wv : p.wk) + (size_t)row8 * 28 + (q8 & 3) * 8;
    const bool last = (q8 & 3) == 3;
#pragma unroll
    for (int i = 0; i < 7; ++i) {
      const u32x2 a = *(const u32x2*)(wr + i * 64 * 28);
      u32x2 c = *(const u32x2*)(wr + i * 64 * 28 + (last ? 0 : 4));
      wv[i] = u32x4{a[0], a[1], c[0], c[1]};
    }
  }
  const int w4 = w & 3, tt = b >> 2, tks = (b & 3) * 7 + 2 * w4, wog = b * 4 + w4;
  u32x2 tv[4];
  unsigned te[8];
  bool dead = false;
  if (w >= 4) {
    if (w == 4) st4<16>(0u, ws, h * PAIR_SZ + r * 4, OFF_PAIR + R_FLG + b * 128);
    if (w == 5 && (jj & 3) == 3 && lane < 8) st4<16>(0u, ws, lane * 64 * 128, OFF_PAIR + PAIR_SZ + R_FKV + (8 * xcd + (jj >> 2)) * 128);
    const unsigned short* src; int pitch; bool pad = true;
    if (tt < 14) { src = p.wq + 32 * tt; pitch = 448; pad = false; }
    else if (tt < 16) { src = tt == 14 ? p.wk : p.wv; pitch = 28; }
    else { src = p.wqkv + ((tt - 16) % 3) * 448 + ((tt - 16) / 3) * 28; pitch = 1344; }
    const int pc = lane & 7, rw = lane >> 3;
    dead = pad && pc == 7;
    src += (size_t)(tks * 16 + rw) * pitch + (dead ? 0 : pc * 4);
#pragma unroll
    for (int i = 0; i < 4; ++i) tv[i] = *(const u32x2*)(src + (size_t)((w4 == 3 && i >= 2) ? 0 : i * 8) * pitch);
    int g = wog < 896 ? wog : 0;
    const unsigned short* wo = g >= 448 ? p.wob : p.woa;
    if (g >= 448) g -= 448;
    const int ct = g >> 5, ks = g & 31, head = ks >> 1;
    const int k0 = head * 28 + 16 * (ks & 1) + 4 * h, kmax = head * 28 + 28;
#pragma unroll
    for (int j = 0; j < 8; ++j) {
      const int k = k0 + 8 * (j >> 2) + (j & 3);
      te[j] = wo[(k < kmax ? k : 0) * 448 + 32 * ct + r];
    }
  }
  SBAR();
  {
    float sm[2] = {}, sq[2] = {};
#pragma unroll
    for (int i = 0; i < 7; ++i) {
      const int ks = 4 * i + (q8 >> 1);
      const ldsp dst = lds + P_XI + ((row8 >> 5) * 28 + ks) * XI_KS + (row8 & 31) * 16 + (q8 & 1) * 8;
      *(LAS u32x2*)dst = u32x2{xv[i][0], xv[i][1]};
      *(LAS u32x2*)(dst + 512) = u32x2{xv[i][2], xv[i][3]};
#pragma unroll
      for (int d = 0; d < 4; ++d) { sm[d & 1] = dot2(xv[i][d], ONES2, sm[d & 1]); sq[d & 1] = dot2(xv[i][d], xv[i][d], sq[d & 1]); }
    }
    float a = sm[0] + sm[1], c = sq[0] + sq[1];
#define DPP_ADD(v, ctl) v += __uint_as_float(__builtin_amdgcn_update_dpp(0u, __float_as_uint(v), ctl, 0xF, 0xF, true))
    DPP_ADD(a, 0xB1); DPP_ADD(c, 0xB1); DPP_ADD(a, 0x4E); DPP_ADD(c, 0x4E); DPP_ADD(a, 0x141); DPP_ADD(c, 0x141);
    const float mu = a * (1.f / D_), var = __builtin_fmaxf(c * (1.f / D_) - mu * mu, 0.f);
    if (q8 == 0) { *(LAS float*)(lds + P_ST + row8 * 4) = mu; *(LAS float*)(lds + P_ST + 256 + row8 * 4) = __builtin_amdgcn_rsqf(var + EPS); }
  }
  if (cp == 7) {
#pragma unroll
    for (int i = 0; i < 7; ++i) lds_w16(lds + P_WI + (row8 + 64 * i) * 128 + (q8 ^ ((row8 & 2) << 1)) * 16, wv[i]);
  }
  if (tid < 8) *(LAS unsigned*)(lds + P_SG + tid * 4) = 0u;
#define PRE_SIG() do { CBAR(); *(volatile LAS unsigned*)(lds + P_SG + w * 4) = 1u; CBAR(); } while (0)
#define PRE_WAIT(k) do { _Pragma("unroll 1") for (int spin = 0; spin < (1 << 20); ++spin) { if (__builtin_amdgcn_readfirstlane(*(volatile LAS unsigned*)(lds + P_SG + (k) * 4))) break; } CBAR(); } while (0)
  asm volatile("s_waitcnt vmcnt(0)" ::: "memory");
  const u64 tag = (((u64)__builtin_amdgcn_readfirstlane((unsigned)(epo >> 32)) << 32) | __builtin_amdgcn_readfirstlane((unsigned)epo)) + 1ull;
  BAR();
  STAMP(1);
  if (w >= 4) {
    const ldsp img = lds + P_TI + w4 * 2048;
#pragma unroll
    for (int i = 0; i < 4; ++i) *(LAS u32x2*)(img + i * 512 + lane * 8) = dead ? u32x2{0u, 0u} : tv[i];
    CBAR();
    const int i16 = lane & 15;
    const ldsp tb = img + (4 * h + (i16 >> 2)) * 64 + ((lane >> 4) & 1) * 32 + (i16 & 3) * 8;
    float cs = 0.f;
#pragma unroll
    for (int ks = 0; ks < 2; ++ks) {
      const s16x4 lo = __builtin_amdgcn_ds_read_tr16_b64_v4i16((LAS s16x4*)(tb + ks * 1024));
      const s16x4 hi = __builtin_amdgcn_ds_read_tr16_b64_v4i16((LAS s16x4*)(tb + ks * 1024 + 512));
      const u32x4 o = __builtin_bit_cast(u32x4, __builtin_shufflevector(lo, hi, 0, 1, 2, 3, 4, 5, 6, 7));
      if (ks == 0 || w4 < 3) {
        st16<PRE_AUX>(o, ws, lane * 16, (tt * 28 + tks + ks) * KB);
#pragma unroll
        for (int i = 0; i < 4; ++i) cs = dot2(o[i], ONES2, cs);
      }
    }
    {
      u32x4 o;
#pragma unroll
      for (int i = 0; i < 4; ++i) { asm volatile("" : "+v"(te[2 * i]), "+v"(te[2 * i + 1])); o[i] = te[2 * i] | (te[2 * i + 1] << 16); }
      if ((wog & 1) && h == 1) { o[2] = 0u; o[3] = 0u; }
      if (wog < 896) st16<PRE_AUX>(o, ws, lane * 16, OFF_WOA + wog * KB);
    }
    cs = xhalf_add(cs);
    if (h == 0) *(LAS float*)(lds + P_CS2 + w4 * 128 + r * 4) = cs;
    if (w4) asm volatile("s_waitcnt vmcnt(0)" ::: "memory");
    PRE_SIG();
    if (w4 == 0) {
      PRE_WAIT(5); PRE_WAIT(6); PRE_WAIT(7);
      const float t = (*(LAS float*)(lds + P_CS2 + r * 4) + *(LAS float*)(lds + P_CS2 + 128 + r * 4)) + (*(LAS float*)(lds + P_CS2 + 256 + r * 4) + *(LAS float*)(lds + P_CS2 + 384 + r * 4));
      if (h == 0) st4<16>(__float_as_uint(t), ws, r * 4, OFF_CS + b * 128);
      asm volatile("s_waitcnt vmcnt(0)" ::: "memory");
      if (lane < 8) st_tag(p.ws + OFF_INI + (lane * 256 + b) * 8, tag);
    }
    STAMP(2); STAMP_DUMP();
    return tag;
  }
  const int rt2 = w >> 1, ct2 = w & 1, tile = 2 * cp + ct2, rt = rb64 * 2 + rt2;
  const bool isV = tile == 15;
  const int i16 = lane & 15, tq = i16 >> 2, tp = i16 & 3, blk = (lane >> 4) & 1;
  const ldsp wbase = lds + P_WI + (4 * h + tq) * 128 + ((ct2 ^ ((tq >> 1) & 1)) * 64) + blk * 32 + tp * 8;
  const ldsp xbase = lds + P_XI + rt2 * 28 * XI_KS + lane * 16;
  f32x16 acc = {}, csa = {};
  const u32x4 ones = {ONES2, ONES2, ONES2, ONES2};
  u32x4 wr[4], xr[4];
  const ldsp wb0 = wbase + rt2 * 2048, wb1 = wbase + (1 - rt2) * 2048, xb0 = xbase + rt2 * XI_KS, xb1 = xbase + (1 - rt2) * XI_KS;
#define PRE_LD(i) do { const ldsp wp_ = (((i) & 1) ? wb1 : wb0) + ((i) >> 1) * 4096; \
    const s16x4 lo_ = __builtin_amdgcn_ds_read_tr16_b64_v4i16((LAS s16x4*)(wp_)), hi_ = __builtin_amdgcn_ds_read_tr16_b64_v4i16((LAS s16x4*)(wp_ + 1024)); \
    wr[(i) & 3] = __builtin_bit_cast(u32x4, __builtin_shufflevector(lo_, hi_, 0, 1, 2, 3, 4, 5, 6, 7)); \
    xr[(i) & 3] = lds_r16((((i) & 1) ? xb1 : xb0) + ((i) >> 1) * 2 * XI_KS); } while (0)
#define PRE_LOOP(SW) \
  PRE_LD(0); PRE_LD(1); PRE_LD(2); SBAR(); \
  _Pragma("unroll") for (int i = 0; i < 28; ++i) { \
    if (i + 3 < 28) PRE_LD(i + 3); \
    SBAR(); \
    acc = SW ? MFMA(xr[i & 3], wr[i & 3], acc) : MFMA(wr[i & 3], xr[i & 3], acc); \
    if (!(i & 1)) csa = SW ? MFMA(ones, wr[i & 3], csa) : MFMA(wr[i & 3], ones, csa); \
    SBAR(); \
  }
  u32x4 o0, o1;
  if (!isV) {
    PRE_LOOP(false)
    if (r == 0) {
#pragma unroll
      for (int g = 0; g < 4; ++g) lds_wf4(lds + P_CS + w * 128 + (8 * g + 4 * h) * 4, f32x4{csa[4 * g], csa[4 * g + 1], csa[4 * g + 2], csa[4 * g + 3]});
    }
    const float mu = *(LAS float*)(lds + P_ST + (rt2 * 32 + r) * 4), rs = *(LAS float*)(lds + P_ST + 256 + (rt2 * 32 + r) * 4);
    PRE_SIG(); PRE_WAIT(w ^ 2);
    const float sc = tile < 14 ? rs * C2 : rs;
#pragma unroll
    for (int g = 0; g < 4; ++g) {
      const f32x4 c = lds_rf4(lds + P_CS + (w ^ 2) * 128 + (8 * g + 4 * h) * 4);
#pragma unroll
      for (int i = 0; i < 4; ++i) acc[4 * g + i] = (acc[4 * g + i] - mu * (c[i] + csa[4 * g + i])) * sc;
    }
    o0 = pack8(acc, 0); o1 = pack8(acc, 1);
    if (tile == 14 && h == 1) { o1[2] = ONES2; o1[3] = 0x00003F80u; }
  } else {
    PRE_LOOP(true)
    if (h == 0) *(LAS float*)(lds + P_CS + w * 128 + r * 4) = csa[0];
    PRE_SIG(); PRE_WAIT(w ^ 2);
    ln_fix_col(acc, lds + P_ST + rt2 * 128, lds + P_ST + 256 + rt2 * 128, h, csa[0] + *(LAS float*)(lds + P_CS + (w ^ 2) * 128 + r * 4));
    o0 = pack8(acc, 0); o1 = pack8(acc, 1);
    if (r >= 28) { const unsigned f = r == 28 ? ONES2 : 0u; o0 = u32x4{f, f, f, f}; o1 = o0; }
  }
  const int dst = OFF_PAIR + (tile < 14 ? R_QA + (rt * 14 + tile) * 2 * KB : R_KVA + rt * 4 * KB + (tile - 14) * 2 * KB);
  st16<16>(o0, ws, lane * 16, dst);
  st16<16>(o1, ws, lane * 16 + KB, dst);
  asm volatile("s_waitcnt vmcnt(0)" ::: "memory");
  if (lane == 8 || (lane < 8 && tile >= 14))
    st_tag(p.ws + (lane == 8 ? OFF_P0F + (rt * 16 + tile) * 8 : OFF_PAIR + R_FKV + (lane * 64 + rt) * 128 + (tile - 13) * 8), tag);
  *(volatile LAS unsigned*)(lds + P_SG + w * 4) = 2u;
  STAMP(2); STAMP_DUMP();
  return tag;
}

constexpr int L_SCR = 0;
constexpr int L_WB = 64 * KB;
constexpr int L_QF = 148 * KB;
constexpr int L_MISC = L_QF + 8 * KB;
constexpr int M_SIG = L_MISC;
constexpr int M_ST = M_SIG + 512;
constexpr int M_CS = M_ST + 1280;
constexpr int L_BYTES = M_CS + 1024;
static_assert(L_BYTES <= 160 * KB && P_BYTES <= L_QF, "LDS");

DI void exp_pack(const f32x16& s, u32x4& p0, u32x4& p1) {
#pragma unroll
  for (int i = 0; i < 4; ++i) p0[i] = pk(__builtin_amdgcn_exp2f(s[2 * i]), __builtin_amdgcn_exp2f(s[2 * i + 1]));
#pragma unroll
  for (int i = 0; i < 4; ++i) p1[i] = pk(__builtin_amdgcn_exp2f(s[8 + 2 * i]), __builtin_amdgcn_exp2f(s[8 + 2 * i + 1]));
}
DI void set_shift(u32x4& q1, float m, int h, unsigned hi31) {
  const unsigned a = pk(m, m); const float r1 = m - bflo(a);
  const unsigned c = pk(r1, r1); const float r2 = r1 - bflo(c);
  const unsigned e = pk(r2, 0.f);
  const unsigned d2 = ((a & 0xFFFFu) | (c << 16)) ^ 0x80008000u, d3 = (e ^ 0x00008000u) | hi31;
  q1[2] = h ? d2 : q1[2]; q1[3] = h ? d3 : q1[3];
}
#define PIN(x) asm volatile("" : "+v"(x))
template <bool DOQK, bool DOPVP, class FA, class FB, class FC>
DI void unit(const f32x16& s, f32x16& sn, const f32x16& cn, f32x16& o, f32x16& op, u32x4& pp, const u32x4& kn0, const u32x4& kn1, const u32x4& qn0,
             const u32x4& qn1, const u32x4& v0, const u32x4& v1p, FA ha, FB hb, FC hc) {
  float e[16]; u32x4 p0;
#define E_(i) e[i] = __builtin_amdgcn_exp2f(s[i]); PIN(e[i]); SBAR()
#define C0_(i) p0[i] = pk(e[2 * i], e[2 * i + 1]); PIN(p0[i]); SBAR()
#define C1_(i) pp[i] = pk(e[8 + 2 * i], e[8 + 2 * i + 1]); PIN(pp[i]); SBAR()
  SBAR();
  if (DOQK) { sn = MFMA(kn0, qn0, cn); SBAR(); }
  E_(0); E_(1); E_(2); E_(3);
  if (DOPVP) { op = MFMA(v1p, pp, op); SBAR(); }
  ha(); SBAR();
  E_(4); C0_(0); E_(5); C0_(1); E_(6); E_(7);
  if (DOQK) { sn = MFMA(kn1, qn1, sn); SBAR(); }
  hb(); SBAR();
  E_(8); C0_(2); E_(9); C0_(3); E_(10); E_(11);
  o = MFMA(v0, p0, o); SBAR();
  hc(); SBAR();
  E_(12); C1_(0); E_(13); C1_(1); E_(14); E_(15); C1_(2); C1_(3);
}

struct Ctx { unsigned char* flg; ldsp sig; int lane; int w; };
DI unsigned now32() { return (unsigned)__builtin_amdgcn_s_memrealtime(); }
struct Clk { unsigned c, r; };
DI Clk clk_now() { Clk k; k.c = (unsigned)__builtin_amdgcn_s_memtime(); k.r = now32(); return k; }
DI unsigned eta(const Clk& k0, int dcyc) {
  const Clk k = clk_now();
  return (k.r + (unsigned)((float)dcyc * (float)(k.r - k0.r) * __builtin_amdgcn_rcpf((float)(k.c - k0.c)))) | 1u;
}
DI void post(const Ctx& c, int b, int slot, const Clk& k0, int dcyc) {
  const unsigned t = eta(k0, dcyc);
  if (c.lane == 0) __hip_atomic_store((GAS unsigned*)(c.flg + b * 128 + slot * 4), t, __ATOMIC_RELAXED, __HIP_MEMORY_SCOPE_AGENT);
}
DI unsigned flag_ld(const Ctx& c, int pb, int slot) { return __hip_atomic_load((GAS unsigned*)(c.flg + pb * 128 + slot * 4), __ATOMIC_RELAXED, __HIP_MEMORY_SCOPE_AGENT); }
template <int SLP = 1> DI void poll_wait(const Ctx& c, int pb, int slot) {
  unsigned f = 0;
#pragma unroll 1
  for (int spin = 0; spin < SPIN_CAP; ++spin) {
    f = __hip_atomic_load((GAS unsigned*)(c.flg + pb * 128 + slot * 4), __ATOMIC_RELAXED, __HIP_MEMORY_SCOPE_AGENT);
    if (__builtin_amdgcn_ballot_w64(f == 0u) == 0ull) break;
    __builtin_amdgcn_s_sleep(SLP);
  }
#pragma unroll 1
  for (int spin = 0; spin < 256; ++spin) {
    if (__builtin_amdgcn_ballot_w64((int)(f - now32()) > 0) == 0ull) break;
    __builtin_amdgcn_s_sleep(1);
  }
  CBAR();
}
DI void lds_sig(const Ctx& c, int idx) {
  CBAR();
  *(volatile LAS unsigned*)(c.sig + idx * 16 + c.w * 4) = 1u;
  CBAR();
}
template <int SLP> DI void lds_wait(const Ctx& c, int idx, unsigned n) {
#pragma unroll 1
  for (int spin = 0; spin < (SPIN_CAP << 6); ++spin) {
    const u32x4 v = *(volatile LAS u32x4*)(c.sig + idx * 16);
    if (__builtin_amdgcn_readfirstlane(v[0] + v[1] + v[2] + v[3]) >= n) break;
    if (SLP) __builtin_amdgcn_s_sleep(SLP);
  }
  CBAR();
}
template <int N, bool SWAP, class F> DI f32x16 gemm_lds(const u32x4 (&wr)[N], ldsp bb, F mid) {
  f32x16 acc = {};
  u32x4 bf[8];
#pragma unroll
  for (int i = 0; i < 8; ++i) bf[i] = lds_r16(bb + i * KB);
  SBAR();
#pragma unroll
  for (int i = 0; i < N; ++i) {
    acc = SWAP ? MFMA(bf[i & 7], wr[i], acc) : MFMA(wr[i], bf[i & 7], acc);
    SBAR();
    if (i + 8 < N) bf[i & 7] = lds_r16(bb + (i + 8) * KB);
    mid(i);
    SBAR();
  }
  return acc;
}
DI void add_res(const f32x16& a, const u32x4& x0, const u32x4& x1, u32x4& y0, u32x4& y1) {
#pragma unroll
  for (int i = 0; i < 4; ++i) {
    y0[i] = pk(a[2 * i] + bflo(x0[i]), a[2 * i + 1] + bfhi(x0[i]));
    y1[i] = pk(a[8 + 2 * i] + bflo(x1[i]), a[8 + 2 * i + 1] + bfhi(x1[i]));
  }
}
DI float row_stats(const u32x4& y0, const u32x4& y1, int h) {
  float s0 = 0.f, s1 = 0.f, q0 = 0.f, q1 = 0.f;
#pragma unroll
  for (int i = 0; i < 4; ++i) { s0 = dot2(y0[i], ONES2, s0); s1 = dot2(y1[i], ONES2, s1); q0 = dot2(y0[i], y0[i], q0); q1 = dot2(y1[i], y1[i], q1); }
  const float s = xhalf_add(s0 + s1), q = xhalf_add(q0 + q1);
  return h ? q : s;
}
DI void stats_finish(float v, float& mu, float& rs) {
  auto t = __builtin_amdgcn_permlane32_swap(__float_as_uint(v), __float_as_uint(v), false, false);
  mu = __uint_as_float(t[0]) * (1.f / D_);
  rs = __builtin_amdgcn_rsqf(__builtin_fmaxf(__uint_as_float(t[1]) * (1.f / D_) - mu * mu, 0.f) + EPS);
}
DI void stats_gather(rsrc_t ws, int so, ldsp dst, int lane) {
#pragma unroll 1
  for (int spin = 0; spin < SPIN_CAP; ++spin) {
    CBAR();
    unsigned sv[14], mx = 0;
#pragma unroll
    for (int i = 0; i < 14; ++i) sv[i] = ld4<16>(ws, lane * 4 + i * 256, so);
    float t0 = 0.f, t1 = 0.f, mu, rs;
#pragma unroll
    for (int i = 0; i < 14; i += 2) { t0 += __uint_as_float(sv[i]); t1 += __uint_as_float(sv[i + 1]); mx = umax3(mx, sv[i], sv[i + 1]); }
    stats_finish(t0 + t1, mu, rs);
    *(LAS float*)(dst + lane * 4) = lane >= 32 ? rs : mu;
    if (__builtin_amdgcn_ballot_w64(mx == SENT) == 0ull) break;
    RETRY(5);
  }
}
DI void norm_pack(const f32x16& o, int h, u32x4& y0, u32x4& y1) {
  const float inv = __builtin_amdgcn_rcpf(from_hi(o[12]));
#pragma unroll
  for (int i = 0; i < 4; ++i) { y0[i] = pk(o[2 * i] * inv, o[2 * i + 1] * inv); y1[i] = pk(o[8 + 2 * i] * inv, o[8 + 2 * i + 1] * inv); }
  if (h == 1) { y1[2] = 0; y1[3] = 0; }
}
constexpr float LBIG = 1e30f;
constexpr unsigned NEG31 = 0xF1490000u;

__global__ void __launch_bounds__(512, 2) k_main(Params p) {
  __shared__ __attribute__((aligned(16))) char smem[L_BYTES];
  const ldsp lds = (ldsp)smem;
  const int tid = threadIdx.x;
  const int w8 = __builtin_amdgcn_readfirstlane(tid >> 6);
  const rsrc_t wt = mkrs(p.ws, OFF_PAIR);
  auto nop = []() {};
  const f32x16 Z = {};
  if (tid < 128) *(LAS unsigned*)(lds + M_SIG + tid * 4) = 0u;
  const u64 tag = phase0(p, lds);
  STAMP_DECL;
#define PAIR_SETUP() \
    int lane = tid & 63, b = blockIdx.x, w = w8 & 3; \
    asm volatile("" : "+v"(lane), "+s"(b), "+s"(w)); \
    lane &= 63; b &= 255; w &= 3; \
    const int r = lane & 31, h = lane >> 5, l16 = lane * 16, xcd = b & 7, jj = b >> 3; \
    const int rt = 8 * xcd + (jj >> 2), sub = jj & 3;          \
    const int rb = 2 * xcd + (jj >> 4), hd = jj & 15;          \
    const ldsp scr = lds + L_SCR + l16; \
    unsigned char* const reg = p.ws + OFF_PAIR + pair * PAIR_SZ; \
    const rsrc_t ws = mkrs(reg, 2 * PAIR_SZ - pair * PAIR_SZ); \
    const Ctx cx = {reg + R_FLG, lds + M_SIG + pair * 256, lane, w}; \
    STAMP(0)

  if (w8 < 4) {
    u64 kvf = 0ull;
#pragma unroll 1
   for (int pair = 0; pair < 2; ++pair) {
    PAIR_SETUP();
    const unsigned char* const kpa = reg + R_FKV + (xcd * 64 + 4 * ((2 * xcd + (lane >> 1)) & 15) + w) * 128 + (h ? 0 : 8 + (lane & 1) * 8);
    unsigned rdy = 0xFFFFFFFFu, chk = pair ? 14u : 0u;
    int kvi = 0;
    auto kv_mask = [&]() { const unsigned bl = (unsigned)__builtin_amdgcn_ballot_w64(kvf == tag); rdy = bl & (bl >> 1); };
    auto kv_next = [&]() -> int {
      if (__builtin_expect(((rdy >> (2 * kvi)) & 1u) == 0u, 0)) {
#pragma unroll 1
        for (int spin = 0; spin < SPIN_CAP; ++spin) {
          kvf = ld_tag(kpa);
          kv_mask();
          if ((rdy >> (2 * kvi)) & 1u) break;
          __builtin_amdgcn_s_sleep(2);
        }
        CBAR();
      }
      const int o = R_KVA + (4 * ((2 * xcd + kvi) & 15) + w) * 4 * KB;
      ++kvi;
      return o;
    };
    {
      u32x4 q[4][2], K[4][2], V[4][2];
      int kb[4];
      if (pair) {
#pragma unroll 1
        for (int spin = 0; spin < SPIN_CAP; ++spin) {
          if ((spin & 7) == 7) kvf = ld_tag(kpa);
          if (((unsigned)(__builtin_amdgcn_ballot_w64((unsigned)kvf == 0u || (int)((unsigned)kvf - now32()) > 0) >> 32) & 0xFFu) == 0u) break;
          __builtin_amdgcn_s_sleep(1);
        }
      } else {
        __builtin_amdgcn_s_sleep(KV_DLY);
#pragma unroll 1
        for (int spin = 0; spin < SPIN_CAP; ++spin) {
          if (((unsigned)__builtin_amdgcn_ballot_w64(ld_tag(kpa) != tag) & 0xFFu) == 0u) break;
          __builtin_amdgcn_s_sleep(2);
        }
      }
      CBAR();
      __builtin_amdgcn_s_sleep(PRO_DLY);
      STAMP(23);
#pragma unroll
      for (int t = 0; t < 4; ++t) {
        kb[t] = kv_next();
        K[t][0] = ld16<16>(ws, l16, kb[t]); K[t][1] = ld16<16>(ws, l16 + KB, kb[t]);
        V[t][0] = ld16<16>(ws, l16 + 2 * KB, kb[t]); if (t < 3) V[t][1] = ld16<16>(ws, l16 + 3 * KB, kb[t]);
        SBAR();
      }
      if (pair) {
        chk_handoff<2>(K[0], ws, l16, kb[0], 6); chk_handoff<2>(V[0], ws, l16 + 2 * KB, kb[0], 6);
      }
      f32x16 o0 = {}, o1 = {}, o2 = {}, o3 = {};
      PIN(o0); PIN(o1); PIN(o2); PIN(o3);
      kvf = ld_tag(kpa);
      STAMP(24);
      const Clk cka = clk_now();
      BAR();
#pragma unroll
      for (int a = 0; a < 4; ++a) { q[a][0] = lds_r16(lds + L_QF + l16 + 2 * a * KB); q[a][1] = lds_r16(lds + L_QF + l16 + (2 * a + 1) * KB); }
      STAMP(1);
      f32x16 sa = MFMA(K[0][0], q[0][0], Z), sb;
      sa = MFMA(K[0][1], q[0][1], sa);
      u32x4 pp = {};
      V[3][1] = u32x4{};
#define TILE(c, LD, LDV1) { \
        constexpr int n = (c + 1) & 3, pv = (c + 3) & 3; \
        auto ldV1 = [&]() { if (LDV1) V[pv][1] = ld16<16>(ws, l16 + 3 * KB, kb[pv]); }; \
        auto ldK = [&]() { if (LD) { kb[c] = kv_next(); K[c][0] = ld16<16>(ws, l16, kb[c]); K[c][1] = ld16<16>(ws, l16 + KB, kb[c]); } }; \
        auto ldV0 = [&]() { if (LD) V[c][0] = ld16<16>(ws, l16 + 2 * KB, kb[c]); }; \
        unit<true, true>(sa, sb, Z, o0, o3, pp, K[c][0], K[c][1], q[1][0], q[1][1], V[c][0], V[pv][1], ldV1, nop, nop); \
        unit<true, true>(sb, sa, Z, o1, o0, pp, K[c][0], K[c][1], q[2][0], q[2][1], V[c][0], V[c][1], nop, nop, nop); \
        unit<true, true>(sa, sb, Z, o2, o1, pp, K[c][0], K[c][1], q[3][0], q[3][1], V[c][0], V[c][1], nop, ldK, nop); \
        if (LD && __builtin_expect((chk >> n) & 1u, 0)) { chk &= ~(1u << n); chk_handoff<2>(K[n], ws, l16, kb[n], 6); chk_handoff<2>(V[n], ws, l16 + 2 * KB, kb[n], 6); } \
        unit<true, true>(sb, sa, Z, o3, o2, pp, K[n][0], K[n][1], q[0][0], q[0][1], V[c][0], V[c][1], nop, nop, ldV0); }
#pragma unroll 1
      for (int tt = 0; tt < 12; tt += 4) {
        kv_mask(); kvf = ld_tag(kpa);
        TILE(0, true, true) TILE(1, true, true)
        kv_mask(); kvf = ld_tag(kpa);
        TILE(2, true, true) TILE(3, true, true)
      }
      STAMP(2);
      TILE(0, false, true) TILE(1, false, false)
      STAMP(3);
      const ldsp red = scr + w * 4 * KB;
      if (w == 0) { lds_wait<1>(cx, 8, 4u); post(cx, b, 0, cka, DT_OA); }
#define WR_LO(o, a) [&]() { lds_wf4(red + (a) * 16 * KB, f32x4{o[0], o[1], o[2], o[3]}); lds_wf4(red + (a) * 16 * KB + KB, f32x4{o[4], o[5], o[6], o[7]}); }
#define WR_HI(o, a) [&]() { lds_wf4(red + (a) * 16 * KB + 2 * KB, f32x4{o[8], o[9], o[10], o[11]}); lds_wf4(red + (a) * 16 * KB + 3 * KB, f32x4{o[12], o[13], o[14], o[15]}); lds_sig(cx, a); }
      unit<true, true>(sa, sb, Z, o0, o3, pp, K[3][0], K[3][1], q[0][0], q[0][1], V[2][0], V[1][1], nop, nop, nop);
      unit<true, true>(sb, sa, Z, o0, o0, pp, K[2][0], K[2][1], q[1][0], q[1][1], V[3][0], V[2][1], nop, nop, nop);
      unit<true, true>(sa, sb, Z, o1, o0, pp, K[3][0], K[3][1], q[1][0], q[1][1], V[2][0], V[3][1], nop, WR_LO(o0, 0), WR_HI(o0, 0));
      unit<true, true>(sb, sa, Z, o1, o1, pp, K[2][0], K[2][1], q[2][0], q[2][1], V[3][0], V[2][1], nop, nop, nop);
      unit<true, true>(sa, sb, Z, o2, o1, pp, K[3][0], K[3][1], q[2][0], q[2][1], V[2][0], V[3][1], nop, WR_LO(o1, 1), WR_HI(o1, 1));
      unit<true, true>(sb, sa, Z, o2, o2, pp, K[2][0], K[2][1], q[3][0], q[3][1], V[3][0], V[2][1], nop, nop, nop);
      unit<true, true>(sa, sb, Z, o3, o2, pp, K[3][0], K[3][1], q[3][0], q[3][1], V[2][0], V[3][1], nop, WR_LO(o2, 2), WR_HI(o2, 2));
      unit<false, true>(sb, sa, Z, o3, o3, pp, K[2][0], K[2][1], q[3][0], q[3][1], V[3][0], V[2][1], nop, nop, nop);
      o3 = MFMA(V[3][1], pp, o3);
      WR_LO(o3, 3)(); WR_HI(o3, 3)();
    }
    STAMP(4);
    {
      u32x4 of[8] = {};
      const int src = R_OFA + (rt * 32 + 8 * w) * KB;
      if (w != sub) {
        lds_wait<1>(cx, 8, 4u);
        poll_wait(cx, (((jj & ~3) | w) << 3) | xcd, 0);
        STAMP(5);
        if (DLY_OA) __builtin_amdgcn_s_sleep(DLY_OA);
        ld_handoff<8>(of, ws, l16, src);
      }
      BAR();
      STAMP(6);
      const unsigned bad = __builtin_amdgcn_readfirstlane(*(LAS unsigned*)(cx.sig + 10 * 16));
      if (__builtin_expect((bad >> w) & 1u, 0)) {
        const u32x4 q0 = lds_r16(lds + L_QF + l16 + 2 * w * KB);
        u32x4 q1 = lds_r16(lds + L_QF + l16 + (2 * w + 1) * KB);
        if (h == 1) { q1[2] = 0; q1[3] = 0; }
        float m = -INFINITY;
#pragma unroll 1
        for (int t = 0; t < 64; ++t) {
          f32x16 s = MFMA(ld16<16>(ws, l16, R_KVA + t * 4 * KB), q0, Z);
          s = MFMA(ld16<16>(ws, l16 + KB, R_KVA + t * 4 * KB), q1, s);
          m = max16(s, m);
        }
        set_shift(q1, xhalf_max(m), h, 0u);
        f32x16 o = {};
#pragma unroll 1
        for (int t = 0; t < 64; ++t) {
          f32x16 s = MFMA(ld16<16>(ws, l16, R_KVA + t * 4 * KB), q0, Z);
          s = MFMA(ld16<16>(ws, l16 + KB, R_KVA + t * 4 * KB), q1, s);
          u32x4 p0, p1;
          exp_pack(s, p0, p1);
          o = MFMA(ld16<16>(ws, l16 + 2 * KB, R_KVA + t * 4 * KB), p0, o);
          o = MFMA(ld16<16>(ws, l16 + 3 * KB, R_KVA + t * 4 * KB), p1, o);
        }
        u32x4 y0, y1;
        norm_pack(o, h, y0, y1);
        st16<16>(y0, ws, l16, R_OFA + (rt * 32 + sub * 8 + 2 * w) * KB);
        st16<16>(y1, ws, l16 + KB, R_OFA + (rt * 32 + sub * 8 + 2 * w) * KB);
        lds_w16(scr + (sub * 8 + 2 * w) * KB, y0); lds_w16(scr + (sub * 8 + 2 * w + 1) * KB, y1);
      }
      if (w != sub) {
        chk_handoff<8>(of, ws, l16, src, 0);
        STAMP(7);
#pragma unroll
        for (int i = 0; i < 8; ++i) lds_w16(scr + (8 * w + i) * KB, of[i]);
      }
      BAR();
    }
    STAMP(8);
    f32x4 ck[4], cq[4];
    const float cv = *(LAS float*)(lds + M_CS + 6 * 128 + r * 4);
#pragma unroll
    for (int g = 0; g < 4; ++g) { cq[g] = lds_rf4(lds + M_CS + 4 * 128 + (8 * g + 4 * h) * 4); ck[g] = lds_rf4(lds + M_CS + 5 * 128 + (8 * g + 4 * h) * 4); }
    BAR();
    STAMP(9);
    u32x4 q0, q1, k0, k1, v0, v1;
    const Clk ck0 = clk_now();
    {
      const int xo = R_XF1 + (rb * 4 + w) * 28 * KB;
      const ldsp wb0 = opaque(lds + L_WB + l16), wb1 = opaque(lds + L_WB + 28 * KB + l16);
      const ldsp st = lds + M_ST + w * 256;
      f32x4 m4[4], r4[4];
      poll_wait(cx, (((jj & ~15) | (w << 2) | (lane & 3)) << 3) | xcd, 1);
      STAMP(10);
      f32x16 aq, ak, av;
      float mu, rs;
      u32x4 xf[28], wk[3], wv[3], wq[4];
#define LDX(i) do { xf[i] = ld16<16>(ws, l16 + ((i) & 3) * KB, xo + ((i) >> 2) * 4 * KB); SBAR(); } while (0)
#define CHKX(i) do { if (__builtin_expect(__builtin_amdgcn_ballot_w64(smax(0u, xf[i]) == SENT) != 0ull, 0)) { \
          _Pragma("unroll 1") for (int spin = 0; spin < SPIN_CAP; ++spin) { CBAR(); RETRY(1); LDX(i); if (__builtin_amdgcn_ballot_w64(smax(0u, xf[i]) == SENT) == 0ull) break; } } SBAR(); } while (0)
#define QST(i) do { if ((i) < 28) { SBAR(); aq = MFMA(wq[(i) & 3], xf[(i) < 28 ? (i) : 0], (i) ? aq : Z); SBAR(); if ((i) + 3 < 28) wq[((i) + 3) & 3] = lds_r16(wb0 + ((i) + 3) * KB); SBAR(); } } while (0)
#pragma unroll
      for (int i = 0; i < X1_AHEAD; ++i) LDX(i);
#pragma unroll
      for (int i = 0; i < 2; ++i) { wk[i] = lds_r16(wb1 + i * KB); wv[i] = lds_r16(wb1 + (28 + i) * KB); }
#pragma unroll
      for (int i = 0; i < 3; ++i) wq[i] = lds_r16(wb0 + i * KB);
      SBAR();
      CHKX(0);
#pragma unroll
      for (int ks = 0; ks < 28; ++ks) {
        ak = MFMA(wk[ks % 3], xf[ks], ks ? ak : Z); SBAR();
        if (ks + 2 < 28) { wk[(ks + 2) % 3] = lds_r16(wb1 + (ks + 2) * KB); wv[(ks + 2) % 3] = lds_r16(wb1 + (28 + ks + 2) * KB); }
        if (X1_AHEAD + 2 * ks < 28) LDX(X1_AHEAD + 2 * ks);
        if (ks == 25) {
          lds_wait<0>(cx, 12 + w, 1u);
          mu = *(LAS float*)(st + r * 4); rs = *(LAS float*)(st + 128 + r * 4);
        }
        if (ks == 26) {
#pragma unroll
          for (int g = 0; g < 4; ++g) { m4[g] = lds_rf4(st + (8 * g + 4 * h) * 4); r4[g] = lds_rf4(st + 128 + (8 * g + 4 * h) * 4); }
        }
        SBAR();
        av = MFMA(xf[ks], wv[ks % 3], ks ? av : Z); SBAR();
        if (X1_AHEAD + 2 * ks + 1 < 28) LDX(X1_AHEAD + 2 * ks + 1);
        if (ks < QS) QST(ks);
        if (ks + 1 < 28) CHKX(ks + 1);
        if (ks == 0) { STAMP(11); } if (ks == 13) { STAMP(12); }
        if (ks == POST_KV) post(cx, b, 4 + w, ck0, DT_KV);
      }
      STAMP(13);
      const int ko = R_KVB + (hd * 64 + rb * 4 + w) * 4 * KB;
      QST(QS); QST(QS + 1);
#pragma unroll
      for (int g = 0; g < 4; ++g) {
#pragma unroll
        for (int i = 4 * g; i < 4 * g + 4; ++i) ak[i] = (ak[i] - mu * ck[g][i & 3]) * rs;
        unsigned a0 = pk(ak[4 * g], ak[4 * g + 1]), a1 = pk(ak[4 * g + 2], ak[4 * g + 3]);
        if (g == 3 && h == 1) { a0 = ONES2; a1 = 0x00003F80u; }
        PIN(a0); PIN(a1);
        if (g < 2) { k0[2 * g] = a0; k0[2 * g + 1] = a1; } else { k1[2 * g - 4] = a0; k1[2 * g - 3] = a1; }
        if (g == 1) st16<16>(k0, ws, l16, ko);
        if (g == 3) st16<16>(k1, ws, l16 + KB, ko);
        QST(QS + 2 + g);
      }
      const unsigned vpad = r == 28 ? ONES2 : 0u;
#pragma unroll
      for (int g = 0; g < 4; ++g) {
#pragma unroll
        for (int i = 4 * g; i < 4 * g + 4; ++i) av[i] = (av[i] - m4[g][i & 3] * cv) * r4[g][i & 3];
        unsigned a0 = pk(av[4 * g], av[4 * g + 1]), a1 = pk(av[4 * g + 2], av[4 * g + 3]);
        if (r >= 28) { a0 = vpad; a1 = vpad; }
        PIN(a0); PIN(a1);
        if (g < 2) { v0[2 * g] = a0; v0[2 * g + 1] = a1; } else { v1[2 * g - 4] = a0; v1[2 * g - 3] = a1; }
        if (g == 1) st16<16>(v0, ws, l16 + 2 * KB, ko);
        if (g == 3) st16<16>(v1, ws, l16 + 3 * KB, ko);
        QST(QS + 6 + g);
      }
      STAMP(26);
      lds_w16(scr + (32 + 4 * w) * KB, k0); QST(QS + 10); lds_w16(scr + (33 + 4 * w) * KB, k1); QST(QS + 11);
      lds_w16(scr + (34 + 4 * w) * KB, v0); QST(QS + 12); lds_w16(scr + (35 + 4 * w) * KB, v1);
      lds_sig(cx, 4);
#pragma unroll
      for (int i = QS + 13; i < 28; ++i) QST(i);
      const float rq = rs * C2;
#pragma unroll
      for (int i = 0; i < 16; ++i) aq[i] = (aq[i] - mu * cq[i >> 2][i & 3]) * rq;
      q0 = pack8(aq, 0); q1 = pack8(aq, 1);
      if (h == 1) { q1[2] = 0; q1[3] = NEG31; }
    }
    STAMP(14);
    {
      f32x16 cd, cf;
#pragma unroll
      for (int i = 0; i < 16; ++i) {
        const int kk = (i & 3) + 8 * (i >> 2) + 4 * h;
        cd[i] = kk <= r ? 0.f : -1e30f; cf[i] = kk <= r ? -1e30f : 0.f;
      }
      f32x16 sa = MFMA(k0, q0, cd), sb;
      sa = MFMA(k1, q1, sa);
      set_shift(q1, xhalf_max(max16(sa, sa[0])), h, NEG31);
      const ldsp kp = scr + (8 + w) * 4 * KB;
      auto need = [&](int u) {
        const int ti = 8 + w - u;
        if (u == 1 && w > 0) lds_wait<0>(cx, 4, 4u);
        if (__builtin_expect((ti & 3) == 3, 0)) lds_wait<1>(cx, 5 + (ti >> 2), 2u);
      };
      u32x4 K[2][2], V[2][2], pp;
      f32x16 o = {};
      sa = MFMA(k0, q0, cd); sa = MFMA(k1, q1, sa);
      V[0][0] = v0; V[0][1] = v1;
      need(1);
      STAMP(15);
      K[1][0] = lds_r16(kp - 4 * KB); K[1][1] = lds_r16(kp - 3 * KB); V[1][0] = lds_r16(kp - 2 * KB);
#pragma unroll
      for (int u = 0; u < 9; ++u) {
        const int c = u & 1, n = c ^ 1;
        auto ha = [&]() {
          if (u + 1 < 9) V[n][1] = lds_r16(kp - (u + 1) * 4 * KB + 3 * KB);
          if (u + 2 < 9) { need(u + 2); K[c][0] = lds_r16(kp - (u + 2) * 4 * KB); K[c][1] = lds_r16(kp - (u + 2) * 4 * KB + KB); }
        };
        auto hc = [&]() { if (u + 2 < 9) V[c][0] = lds_r16(kp - (u + 2) * 4 * KB + 2 * KB); if (u >= 4 && u <= 6 && __builtin_expect(u == (w < 2 ? 4 : 3 + w), 0)) post(cx, b, 8 + w, ck0, DT_OB + (8 - u) * DT_UNIT); };
        f32x16& s = c ? sb : sa; f32x16& sn = c ? sa : sb;
        if (u == 4) { STAMP(16); }
        if (u == 0) unit<true, false>(s, sn, Z, o, o, pp, K[n][0], K[n][1], q0, q1, V[c][0], V[n][1], ha, nop, hc);
        else if (u < 7) unit<true, true>(s, sn, Z, o, o, pp, K[n][0], K[n][1], q0, q1, V[c][0], V[n][1], ha, nop, hc);
        else if (u == 7) unit<true, true>(s, sn, cf, o, o, pp, K[n][0], K[n][1], q0, q1, V[c][0], V[n][1], ha, nop, hc);
        else unit<false, true>(s, sn, Z, o, o, pp, K[n][0], K[n][1], q0, q1, V[c][0], V[n][1], ha, nop, hc);
      }
      o = MFMA(V[0][1], pp, o);
      if (__builtin_expect(__builtin_amdgcn_ballot_w64(h == 1 && !(o[12] < LBIG)) != 0ull, 0)) {
        if (h == 1) { q1[2] = 0; q1[3] = NEG31; }
        float m = -INFINITY;
#pragma unroll 1
        for (int u = 0; u < 9; ++u) {
          f32x16 s;
#pragma unroll
          for (int i = 0; i < 16; ++i) s[i] = u == 0 ? cd[i] : u == 8 ? cf[i] : 0.f;
          s = MFMA(lds_r16(kp - u * 4 * KB), q0, s); s = MFMA(lds_r16(kp - u * 4 * KB + KB), q1, s);
          m = max16(s, m);
        }
        set_shift(q1, xhalf_max(m), h, NEG31);
        o = Z;
#pragma unroll 1
        for (int u = 0; u < 9; ++u) {
          f32x16 s;
#pragma unroll
          for (int i = 0; i < 16; ++i) s[i] = u == 0 ? cd[i] : u == 8 ? cf[i] : 0.f;
          s = MFMA(lds_r16(kp - u * 4 * KB), q0, s); s = MFMA(lds_r16(kp - u * 4 * KB + KB), q1, s);
          u32x4 p0, p1;
          exp_pack(s, p0, p1);
          o = MFMA(lds_r16(kp - u * 4 * KB + 2 * KB), p0, o); o = MFMA(lds_r16(kp - u * 4 * KB + 3 * KB), p1, o);
        }
      }
      u32x4 y0, y1;
      norm_pack(o, h, y0, y1);
      const int oo = R_OFB + ((rb * 4 + w) * 16 + hd) * 2 * KB;
      st16<16>(y0, ws, l16, oo); st16<16>(y1, ws, l16 + KB, oo);
      lds_sig(cx, 9);
    }
    STAMP(17);
    u32x4 wa1[WA1];
    {
      u32x4 of[8];
      const int src = R_OFB + (rt * 32 + 8 * w) * KB;
#pragma unroll
      for (int i = 0; i < WA1; ++i) { wa1[i] = ld16<16>(wt, l16 + (i & 3) * KB, pair ? OFF_PAIR : OFF_WAT + ((4 * sub + w) * 28 + (i & ~3)) * KB); SBAR(); }
      poll_wait(cx, (((jj & ~15) | (w << 2) | (lane & 3)) << 3) | xcd, 8 + (rt & 3));
      STAMP(18);
      if (DLY_OB) __builtin_amdgcn_s_sleep(DLY_OB);
      ld_handoff<8>(of, ws, l16, src);
      chk_handoff<8>(of, ws, l16, src, 3);
      STAMP(19);
      lds_wait<1>(cx, 9, 4u);
#pragma unroll
      for (int i = 0; i < 8; ++i) lds_w16(scr + (8 * w + i) * KB, of[i]);
      BAR();
      STAMP(20);
    }
    if (pair == 0) {
      u32x4 wa[28];
#pragma unroll
      for (int i = 0; i < WA1; ++i) wa[i] = wa1[i];
#pragma unroll
      for (int i = WA1; i < 28; ++i) { wa[i] = ld16<16>(wt, l16 + (i & 3) * KB, OFF_WAT + ((4 * sub + w) * 28 + (i & ~3)) * KB); SBAR(); }
      const int tile = 4 * sub + w;
      f32x4 cq[4];
#pragma unroll
      for (int g = 0; g < 4; ++g) cq[g] = lds_rf4(lds + M_CS + w * 128 + (8 * g + 4 * h) * 4);
      const float cs = *(LAS float*)(lds + M_CS + w * 128 + r * 4);
      BAR();
      STAMP(21);
      if (w == 0 || tile == 14) {
        const unsigned t = eta(ck0, DT_Q);
        if (lane < (w == 0 ? 1 : 8))
          __hip_atomic_store((GAS unsigned*)(cx.flg + PAIR_SZ + (w == 0 ? b * 128 + 3 * 4 : (256 + lane * 64 + rt) * 128)), t, __ATOMIC_RELAXED, __HIP_MEMORY_SCOPE_AGENT);
      }
      const float mu = *(LAS float*)(lds + M_ST + 4 * 256 + r * 4), rs = *(LAS float*)(lds + M_ST + 4 * 256 + 128 + r * 4);
      auto ldeta = [&](int i) { if (i == ETA_AT) kvf = ld_tag(kpa + PAIR_SZ); };
      u32x4 y0, y1;
      if (tile != 15) {
        f32x16 acc = gemm_lds<28, false>(wa, scr + 32 * KB, ldeta);
        const float sc = tile < 14 ? rs * C2 : rs;
#pragma unroll
        for (int i = 0; i < 16; ++i) acc[i] = (acc[i] - mu * cq[i >> 2][i & 3]) * sc;
        y0 = pack8(acc, 0); y1 = pack8(acc, 1);
        if (tile == 14 && h == 1) { y1[2] = ONES2; y1[3] = 0x00003F80u; }
      } else {
        const ldsp st = lds + M_ST + 4 * 256;
        f32x4 r4[4];
#pragma unroll
        for (int g = 0; g < 4; ++g) { cq[g] = lds_rf4(st + (8 * g + 4 * h) * 4); r4[g] = lds_rf4(st + 128 + (8 * g + 4 * h) * 4); }
        f32x16 acc = gemm_lds<28, true>(wa, scr + 32 * KB, ldeta);
#pragma unroll
        for (int i = 0; i < 16; ++i) acc[i] = (acc[i] - cq[i >> 2][i & 3] * cs) * r4[i >> 2][i & 3];
        y0 = pack8(acc, 0); y1 = pack8(acc, 1);
        if (r >= 28) { const unsigned f = r == 28 ? ONES2 : 0u; y0 = u32x4{f, f, f, f}; y1 = y0; }
      }
      const int dst = PAIR_SZ + (tile < 14 ? R_QA + (rt * 14 + tile) * 2 * KB : R_KVA + rt * 4 * KB + (tile - 14) * 2 * KB);
      st16<16>(y0, ws, l16, dst); st16<16>(y1, ws, l16 + KB, dst);
      STAMP(25);
      if (tile >= 14) {
        asm volatile("s_waitcnt vmcnt(0)" ::: "memory");
        if (lane < 8) st_tag(cx.flg + PAIR_SZ + (256 + lane * 64 + rt) * 128 + (tile - 13) * 8, tag);
      }
    }
    STAMP(22); STAMP_DUMP();
   }
   return;
  }

  u32x4 x0 = {}, x1 = {};
#pragma unroll 1
 for (int pair = 0; pair < 2; ++pair) {
  PAIR_SETUP();
  __builtin_amdgcn_s_setprio(2);
  const int ct0 = sub < 2 ? 4 * sub : 3 * sub + 2, nct = sub < 2 ? 4 : 3;
  const bool ctok = w < nct;
  const int ct = ct0 + (ctok ? w : 0);
  const int xo1 = R_XF1 + (rt * 28 + ct * 2) * KB, xo2 = R_XF2 + (rt * 28 + ct * 2) * KB;
  const int so1 = R_ST1 + (rt * 14 + ct) * 256, so2 = R_ST2 + (rt * 14 + ct) * 256;
  {
    const int qa = R_QA + rt * 28 * KB;
    if (pair) poll_wait<2>(cx, (((jj & ~3) | (lane & 3)) << 3) | xcd, 3);
    else {
#pragma unroll 1
      for (int spin = 0; spin < (SPIN_CAP << 4); ++spin) {
        const u32x4 v = *(volatile LAS u32x4*)(lds + P_SG);
        if (__builtin_amdgcn_readfirstlane(v[0] + v[1] + v[2] + v[3]) == 8u) break;
        __builtin_amdgcn_s_sleep(2);
      }
      __builtin_amdgcn_s_sleep(Q_DLY);
      const int g0 = 7 * (4 * sub + w), tl = (lane & 3) == 0 ? g0 >> 3 : (lane & 3) == 1 ? (g0 + 6) >> 3 : 14;
#pragma unroll 1
      for (int spin = 0; spin < SPIN_CAP; ++spin) {
        if (__builtin_amdgcn_ballot_w64(ld_tag(p.ws + OFF_P0F + (rt * 16 + tl) * 8) != tag) == 0ull) break;
        __builtin_amdgcn_s_sleep(1);
      }
      CBAR();
    }
    STAMP(16);
    u32x4 q[2], k0, k1;
#pragma unroll 1
    for (int spin = 0; spin < SPIN_CAP; ++spin) {
      CBAR();
#pragma unroll
      for (int s = 0; s < 2; ++s)
#pragma unroll
        for (int jh = 0; jh < 2; ++jh) {
          const int dg = 4 * s + 2 * jh + h, G = 7 * (4 * sub + w) + (dg < 7 ? dg : 6);
          const int g8 = G & 7;
          u32x2 v = ld8<16>(ws, ((G >> 3) * 2 + (g8 >> 2)) * KB + (r + 32 * (g8 & 1)) * 16 + ((g8 >> 1) & 1) * 8, qa);
          if (dg == 7) { v[0] = 0; v[1] = 0; }
          q[s][2 * jh] = v[0]; q[s][2 * jh + 1] = v[1];
        }
      k0 = ld16<16>(ws, l16, R_KVA + rt * 4 * KB); k1 = ld16<16>(ws, l16 + KB, R_KVA + rt * 4 * KB);
      if (pair == 0 || __builtin_amdgcn_ballot_w64(smax(smax(smax(smax(0u, q[0]), q[1]), k0), k1) == SENT) == 0ull) break;
      RETRY(7);
    }
    f32x16 s0 = MFMA(k0, q[0], Z);
    s0 = MFMA(k1, q[1], s0);
    lds_w16(lds + L_QF + l16 + 2 * w * KB, q[0]);
    set_shift(q[1], xhalf_max(max16(s0, s0[0])), h, 0u);
    lds_w16(lds + L_QF + l16 + (2 * w + 1) * KB, q[1]);
    STAMP(17);
    BAR();
  }
  STAMP(1);
  __builtin_amdgcn_s_setprio(HPRIO);
  u32x4 wf[32];
  Clk ck0 = clk_now();
  {
    if (pair == 0) {
#pragma unroll 1
      for (int spin = 0; spin < SPIN_CAP; ++spin) {
        bool ok = true;
#pragma unroll
        for (int i = 0; i < 4; ++i) ok &= ld_tag(p.ws + OFF_INI + (xcd * 256 + i * 64 + lane) * 8) == tag;
        if (__builtin_amdgcn_ballot_w64(!ok) == 0ull) break;
        __builtin_amdgcn_s_sleep(8);
      }
      CBAR();
    }
    const u32x4 sv = {SENT, SENT, SENT, SENT};
#define PC() do { SBAR(); if (PACE) __builtin_amdgcn_s_sleep(PACE); SBAR(); } while (0)
    if (NOH) { for (int i = 0; i < NOH; ++i) __builtin_amdgcn_s_sleep(127); }
    auto fill = [&](int i) {
      const int hi = (i & 1) * KB;
      if (i < 2) st16<16>(sv, ws, l16 + hi, R_OFA + (rt * 32 + sub * 8 + 2 * w) * KB);
      else if (i < 4) { if (ctok) { st16<16>(sv, ws, l16 + hi, xo1); if (i == 2) st4<16>(SENT, ws, lane * 4, so1); } }
      else if (i < 8) st16<16>(sv, ws, l16 + (i - 4) * KB, R_KVB + (hd * 64 + rb * 4 + w) * 4 * KB);
      else if (i < 10) st16<16>(sv, ws, l16 + hi, R_OFB + ((rb * 4 + w) * 16 + hd) * 2 * KB);
      else if (i < 12) { if (ctok && pair == 0) { st16<16>(sv, ws, l16 + hi, xo2); if (i == 10) st4<16>(SENT, ws, lane * 4, so2); } }
      else if (pair == 0) st16<16>(sv, ws, l16 + hi, PAIR_SZ + (4 * sub + w < 14 ? R_QA + (rt * 14 + 4 * sub + w) * 2 * KB : R_KVA + rt * 4 * KB + (4 * sub + w - 14) * 2 * KB));
      PC();
    };
    const unsigned char* wbsrc = p.ws + OFF_WBT + (hd * 84 + w * 21) * KB + l16;
    const unsigned wbdst = (unsigned)(uintptr_t)(lds + L_WB) + w * 21 * KB;
#pragma unroll
    for (int i = 0; i < 32; ++i) {
      wf[i] = ld16<16>(wt, l16 + (i & 3) * KB, OFF_WOA + (ct * 32 + (i & ~3)) * KB); PC();
      if (i % 3 != 2 || i > 29) { const int g = i - (i + 1) / 3; if (g < 21) { if (pair == 0) glds16_sc1(wbsrc + g * KB, wbdst + g * KB); else __builtin_amdgcn_s_sleep(PACE3); PC(); } }
      if ((i & 1) == 0 && i < 28) fill(i >> 1);
      if (i == 10) { asm volatile("s_waitcnt vmcnt(5)" ::: "memory"); lds_sig(cx, 8); }
    }
    if (pair == 0) {
      const unsigned short* xr = p.x + (size_t)(rt * 32 + r) * D_ + ct * 32 + 4 * h;
      const u32x2 a0 = *(const u32x2*)xr, a1 = *(const u32x2*)(xr + 8), a2 = *(const u32x2*)(xr + 16), a3 = *(const u32x2*)(xr + 24);
      x0 = u32x4{a0[0], a0[1], a1[0], a1[1]}; x1 = u32x4{a2[0], a2[1], a3[0], a3[1]};
    }
    if (pair == 0) {
      const int co = (h ? 16 + hd * 3 + (w < 3 ? w : 0) : 4 * sub + w) * 512 + r * 4;
      const float t = (__uint_as_float(ld4<16>(wt, co, OFF_CS)) + __uint_as_float(ld4<16>(wt, co + 128, OFF_CS))) + (__uint_as_float(ld4<16>(wt, co + 256, OFF_CS)) + __uint_as_float(ld4<16>(wt, co + 384, OFF_CS)));
      *(LAS float*)(lds + M_CS + (4 * h + w) * 128 + r * 4) = t;
    }
    if (WARM && pair == 0) {
      const int piece = (jj * 4 + w) * 3584;
      unsigned t = 0;
      if (lane < 28) t = ld4<16>(wt, piece + lane * 128, OFF_WOB) ;
      else if (lane < 56) t = ld4<16>(wt, piece + (lane - 28) * 128, OFF_WAT);
      asm volatile("" :: "v"(t));
    }
    asm volatile("s_waitcnt vmcnt(0)" ::: "memory");
  }
  STAMP(2);
  {
    u32x4 keep[2];
    const int s = w & 1;
    __builtin_amdgcn_s_setprio(2);
    ck0 = clk_now();
#pragma unroll
    for (int it = 0; it < 2; ++it) {
      const int T = 2 * it + (w >> 1);
      if (it == 0) lds_wait<2>(cx, T, 4u); else lds_wait<1>(cx, T, 4u);

      const ldsp red = scr + T * 16 * KB;
      f32x4 a0[4], a1[4]; float l4[4];
#pragma unroll
      for (int k = 0; k < 4; ++k) {
        l4[k] = *(LAS float*)(lds + L_SCR + T * 16 * KB + k * 4 * KB + 3 * KB + (32 + r) * 16);
        a0[k] = lds_rf4(red + k * 4 * KB + (2 * s) * KB);
        a1[k] = lds_rf4(red + k * 4 * KB + (2 * s + 1) * KB);
      }
      const float l = (l4[0] + l4[1]) + (l4[2] + l4[3]);
      const f32x4 b0 = (a0[0] + a0[1]) + (a0[2] + a0[3]), b1 = (a1[0] + a1[1]) + (a1[2] + a1[3]);
      const float inv = __builtin_amdgcn_rcpf(l);
      u32x4 o = {pk(b0[0] * inv, b0[1] * inv), pk(b0[2] * inv, b0[3] * inv), pk(b1[0] * inv, b1[1] * inv), pk(b1[2] * inv, b1[3] * inv)};
      if (s == 1 && h == 1) { o[2] = 0; o[3] = 0; }
      if (__builtin_expect(__builtin_amdgcn_ballot_w64(!(l < LBIG)) == 0ull, 1)) st16<16>(o, ws, l16, R_OFA + (rt * 32 + sub * 8 + 2 * T + s) * KB);
      else if (lane == 0) __hip_atomic_fetch_or((LAS unsigned*)(cx.sig + 10 * 16), 1u << T, __ATOMIC_RELAXED, __HIP_MEMORY_SCOPE_WORKGROUP);
      keep[it] = o;
      if (it == 0) { STAMP(3); } else { STAMP(4); }
    }
    BAR();
    STAMP(5);
    const unsigned bad = __builtin_amdgcn_readfirstlane(*(LAS unsigned*)(cx.sig + 10 * 16));
#pragma unroll
    for (int it = 0; it < 2; ++it) {
      const int T = 2 * it + (w >> 1);
      if (!((bad >> T) & 1u)) lds_w16(scr + (sub * 8 + 2 * T + s) * KB, keep[it]);
    }
    BAR();
  }
  STAMP(6);
  {
    const f32x16 acc = gemm_lds<32, false>(wf, scr, [&](int i) { if (i == 0 && w == 0) post(cx, b, 1, ck0, DT_X); if (i == 24) BAR();   });
    add_res(acc, x0, x1, x0, x1);
    if (ctok) { st16<16>(x0, ws, l16, xo1); st16<16>(x1, ws, l16 + KB, xo1); SBAR(); st4<16>(__float_as_uint(row_stats(x0, x1, h)), ws, lane * 4, so1); }
  }
  STAMP(7);
  {
    __builtin_amdgcn_s_setprio(0);
    poll_wait<2>(cx, (((jj & ~15) | (w << 2) | (lane & 3)) << 3) | xcd, 1);
    __builtin_amdgcn_s_sleep(ST_DLY);
    stats_gather(ws, R_ST1 + (rb * 4 + w) * 14 * 256, lds + M_ST + w * 256, lane);
    lds_sig(cx, 12 + w);
    const int kt = 4 * rb - 8 + 2 * w;
    u32x4 hv[8];
    STAMP(8);
    if (kt >= 0) {
      const int prb = kt >> 2;
      __builtin_amdgcn_s_setprio(0);
      poll_wait<4>(cx, (((prb & 1) * 16 + hd) << 3) | (prb >> 1), 4 + (kt & 3) + (lane & 1));
      __builtin_amdgcn_s_setprio(2);
      STAMP(9);
      if (DLY_KV) __builtin_amdgcn_s_sleep(DLY_KV);
      ld_handoff<8>(hv, ws, l16, R_KVB + (hd * 64 + kt) * 4 * KB);
      chk_handoff<8>(hv, ws, l16, R_KVB + (hd * 64 + kt) * 4 * KB, 2);
    } else {
#pragma unroll
      for (int i = 0; i < 8; ++i) hv[i] = u32x4{0u, 0u, 0u, ((i & 3) == 1 && h == 1) ? 0x3F800000u : 0u};
    }
#pragma unroll
    for (int i = 0; i < 8; ++i) lds_w16(scr + (8 * w + i) * KB, hv[i]);
    lds_sig(cx, 5 + (w >> 1));
    STAMP(10);
    __builtin_amdgcn_s_setprio(0);
#pragma unroll
    for (int i = 0; i < 32; ++i) { wf[i] = ld16<16>(wt, l16 + (i & 3) * KB, OFF_WOB + (ct * 32 + (i & ~3)) * KB); SBAR(); if (PACE2) __builtin_amdgcn_s_sleep(PACE2); SBAR(); }
    BAR();
  }
  STAMP(11);
  __builtin_amdgcn_s_setprio(2);
  {
    const f32x16 acc = gemm_lds<32, false>(wf, scr, [&](int i) { if (i == 0 && w == 0 && pair == 0) post(cx, b, 2, ck0, DT_X); });
    add_res(acc, x0, x1, x0, x1);
    if (pair == 1) {
      if (ctok) {
        unsigned short* orow = p.out + (size_t)(rt * 32 + r) * D_ + ct * 32 + 4 * h;
        *(u32x2*)orow = u32x2{x0[0], x0[1]}; *(u32x2*)(orow + 8) = u32x2{x0[2], x0[3]};
        *(u32x2*)(orow + 16) = u32x2{x1[0], x1[1]}; *(u32x2*)(orow + 24) = u32x2{x1[2], x1[3]};
      }
      if (b == 0 && w == 0 && lane == 0) st_tag(p.ws + OFF_EPO, tag);
      STAMP(12); STAMP_DUMP(); SPAN_T(1);
      return;
    }
    if (ctok) { st16<16>(x0, ws, l16, xo2); st16<16>(x1, ws, l16 + KB, xo2); SBAR(); st4<16>(__float_as_uint(row_stats(x0, x1, h)), ws, lane * 4, so2); }
  }
  STAMP(12);
  {
    u32x4 xf[7];
    const int src = R_XF2 + (rt * 28 + 7 * w) * KB;
    poll_wait(cx, (((jj & ~3) | (lane & 3)) << 3) | xcd, 2);
    STAMP(13);
    if (DLY_X2) __builtin_amdgcn_s_sleep(DLY_X2);
    ld_handoff<7>(xf, ws, l16, src);
    if (w == 3) stats_gather(ws, R_ST2 + rt * 14 * 256, lds + M_ST + 4 * 256, lane);
    chk_handoff<7>(xf, ws, l16, src, 4);
    STAMP(14);
#pragma unroll
    for (int i = 0; i < 7; ++i) lds_w16(scr + (32 + 7 * w + i) * KB, xf[i]);
    BAR();
  }
  STAMP(15); STAMP_DUMP();
 }
}

extern "C" void kernel_launch(void* const* d_in, const int* in_sizes, int n_in, void* d_out, int out_size, void* d_ws, size_t ws_size,
                              hipStream_t stream) {
  Params p;
  p.x = (const unsigned short*)d_in[0];
  p.wq = (const unsigned short*)d_in[1];
  p.wk = (const unsigned short*)d_in[2];
  p.wv = (const unsigned short*)d_in[3];
  p.woa = (const unsigned short*)d_in[4];
  p.wqkv = (const unsigned short*)d_in[6];
  p.wob = (const unsigned short*)d_in[7];
  p.out = (unsigned short*)d_out;
  p.ws = (unsigned char*)d_ws;
  k_main<<<256, 512, 0, stream>>>(p);
}
```

```cpp
#include <hip/hip_runtime.h>
#include <hip/hip_bf16.h>
#include <stdint.h>

typedef short bf16x8 __attribute__((ext_vector_type(8)));
typedef short s16x4 __attribute__((ext_vector_type(4)));
typedef float f32x16 __attribute__((ext_vector_type(16)));
typedef float f32x4 __attribute__((ext_vector_type(4)));
typedef float f32x2 __attribute__((ext_vector_type(2)));
typedef unsigned u32x4 __attribute__((ext_vector_type(4)));
typedef unsigned u32x2 __attribute__((ext_vector_type(2)));
typedef __bf16 bf16x2_t __attribute__((ext_vector_type(2)));
#define DI __device__ __forceinline__
#define LAS __attribute__((address_space(3)))
#define GAS __attribute__((address_space(1)))
typedef LAS char* ldsp;

#define MFMA(a, b, c) __builtin_amdgcn_mfma_f32_32x32x16_bf16(__builtin_bit_cast(bf16x8, (a)), __builtin_bit_cast(bf16x8, (b)), (c), 0, 0, 0)
#define BAR() asm volatile("s_waitcnt lgkmcnt(0)\n\ts_barrier" ::: "memory")
#define CBAR() asm volatile("" ::: "memory")
#define SBAR() __builtin_amdgcn_sched_barrier(0)

constexpr int S_ = 2048, D_ = 448;
constexpr float C2 = 0.18898223650461362f * 1.4426950408889634f;
constexpr float EPS = 1e-5f;
constexpr unsigned SENT = 0xFFFFFFFFu;
constexpr unsigned ONES2 = 0x3F803F80u;
constexpr int SPIN_CAP = 1 << 14;

constexpr int KB = 1024;
constexpr int OFF_WAT = 0;
constexpr int OFF_WBT = OFF_WAT + 448 * KB;
constexpr int OFF_WOA = OFF_WBT + 1344 * KB;
constexpr int OFF_WOB = OFF_WOA + 448 * KB;
constexpr int OFF_CS = OFF_WOB + 448 * KB;
constexpr int OFF_EPO = OFF_CS + 32 * KB;
constexpr int OFF_INI = OFF_EPO + KB;
constexpr int OFF_P0F = OFF_INI + 16 * KB;
constexpr int OFF_PAIR = OFF_P0F + 8 * KB;
constexpr int QA_SZ = 64 * 28 * KB;
constexpr int R_QA = 0;
constexpr int R_KVA = R_QA + QA_SZ;
constexpr int R_OFA = R_KVA + 256 * KB;
constexpr int R_XF1 = R_OFA + 2048 * KB;
constexpr int R_KVB = R_XF1 + QA_SZ;
constexpr int R_OFB = R_KVB + 4096 * KB;
constexpr int R_XF2 = R_OFB + 2048 * KB;
constexpr int R_ST1 = R_XF2 + QA_SZ;
constexpr int R_ST2 = R_ST1 + 64 * 14 * 256;
constexpr int R_FLG = R_ST2 + 64 * 14 * 256;
constexpr int R_FKV = R_FLG + 32 * KB;
constexpr int PAIR_SZ = R_FKV + 64 * KB;
constexpr int OFF_DBG = OFF_PAIR + 2 * PAIR_SZ;
constexpr int WS_BYTES = OFF_DBG + 2048 * KB;

#define NOSIDE 0
#define NOKV 0
#define HPRIO 0
#define NOH 0
#define SIG_X1 14
#define WOB1 0
#define DT_OA 2200
#define DT_X 1850
#define DT_KV 3700
#define DT_OB 1000
#define DT_UNIT 240
#define X1_AHEAD 8
#define WA1 8
#define PRE_AUX 16
#define PRO_DLY 2
#define WARM 1
#define KV_DLY 6
#define Q_DLY 4
#define PACE3 8
#define DT_Q 1800
#define ETA_AT 26
#define RSLP 0
#define QS 14
#define ST_DLY 20
#define CODE_KB 46
#define DT_V 300
#define NOFILL 0
#define NOWB 0
#define NOWO 0
#define PGRP 2
#define DOZE 14
#define DLY_OA 0
#define DLY_X1 0
#define DLY_KV 0
#define DLY_OB 0
#define DLY_X2 0
#define DLY_WOB 24
#define PACE 6
#define PACE2 0
#define POST_OA 2
#define POST_OB 6
#define POST_KV 12
#define POST_GEMM 8

DI unsigned pk(float a, float b) { f32x2 v = {a, b}; bf16x2_t r = __builtin_convertvector(v, bf16x2_t); return __builtin_bit_cast(unsigned, r); }
DI float dot2(unsigned a, unsigned b, float c) { return __builtin_amdgcn_fdot2_f32_bf16(__builtin_bit_cast(bf16x2_t, a), __builtin_bit_cast(bf16x2_t, b), c, false); }
DI float bflo(unsigned u) { return __uint_as_float(u << 16); }
DI float bfhi(unsigned u) { return __uint_as_float(u & 0xFFFF0000u); }
DI float xhalf_add(float v) { auto r = __builtin_amdgcn_permlane32_swap(__float_as_uint(v), __float_as_uint(v), false, false); return __uint_as_float(r[0]) + __uint_as_float(r[1]); }
DI float xhalf_max(float v) { auto r = __builtin_amdgcn_permlane32_swap(__float_as_uint(v), __float_as_uint(v), false, false); return __builtin_fmaxf(__uint_as_float(r[0]), __uint_as_float(r[1])); }
DI float from_hi(float v) { auto r = __builtin_amdgcn_permlane32_swap(__float_as_uint(v), __float_as_uint(v), false, false); return __uint_as_float(r[1]); }
DI float max3f(float a, float b, float c) { return __builtin_fmaxf(__builtin_fmaxf(a, b), c); }
DI float max16(const f32x16& s, float m) {
#pragma unroll
  for (int i = 0; i < 16; i += 2) m = max3f(m, s[i], s[i + 1]);
  return m;
}
typedef __amdgpu_buffer_rsrc_t rsrc_t;
DI rsrc_t mkrs(const void* p, unsigned n) { return __builtin_amdgcn_make_buffer_rsrc((void*)p, 0, n, 0x00020000); }
template <int AUX> DI u32x4 ld16(rsrc_t rs, int voff, int soff) { return __builtin_amdgcn_raw_buffer_load_b128(rs, voff, soff, AUX); }
template <int AUX> DI u32x2 ld8(rsrc_t rs, int voff, int soff) { return __builtin_amdgcn_raw_buffer_load_b64(rs, voff, soff, AUX); }
template <int AUX> DI void st16(u32x4 v, rsrc_t rs, int voff, int soff) { __builtin_amdgcn_raw_buffer_store_b128(v, rs, voff, soff, AUX); }
template <int AUX> DI void st8(u32x2 v, rsrc_t rs, int voff, int soff) { __builtin_amdgcn_raw_buffer_store_b64(v, rs, voff, soff, AUX); }
template <int AUX> DI unsigned ld4(rsrc_t rs, int voff, int soff) { return __builtin_amdgcn_raw_buffer_load_b32(rs, voff, soff, AUX); }
template <int AUX> DI void st4(unsigned v, rsrc_t rs, int voff, int soff) { __builtin_amdgcn_raw_buffer_store_b32(v, rs, voff, soff, AUX); }
DI ldsp opaque(ldsp p) { unsigned a = (unsigned)(uintptr_t)p; asm volatile("" : "+v"(a)); return (ldsp)(uintptr_t)a; }
typedef unsigned long long u64;
DI u64 ld_tag(const unsigned char* a) { return __hip_atomic_load((GAS u64*)a, __ATOMIC_RELAXED, __HIP_MEMORY_SCOPE_AGENT); }
DI void st_tag(unsigned char* a, u64 v) { __hip_atomic_store((GAS u64*)a, v, __ATOMIC_RELAXED, __HIP_MEMORY_SCOPE_AGENT); }
DI u32x4 lds_r16(ldsp p) { return *(LAS u32x4*)p; }
DI f32x4 lds_rf4(ldsp p) { return *(LAS f32x4*)p; }
DI void lds_w16(ldsp p, u32x4 v) { *(LAS u32x4*)p = v; }
DI void lds_wf4(ldsp p, f32x4 v) { *(LAS f32x4*)p = v; }
DI unsigned umax3(unsigned a, unsigned b, unsigned c) { return max(max(a, b), c); }
DI unsigned smax(unsigned m, u32x4 v) { return umax3(umax3(m, v[0], v[1]), v[2], v[3]); }
DI void glds16_sc1(const void* g, unsigned lds_base) {
  unsigned sv;
  asm volatile("s_mov_b32 %0, m0\n\ts_mov_b32 m0, %2\n\ts_nop 0\n\tglobal_load_lds_dwordx4 %1, off sc1\n\ts_mov_b32 m0, %0" : "=&s"(sv) : "v"(g), "s"(lds_base) : "memory");
}
DI void glds16(const void* g, unsigned lds_base) {
  unsigned sv;
  asm volatile("s_mov_b32 %0, m0\n\ts_mov_b32 m0, %2\n\ts_nop 0\n\tglobal_load_lds_dwordx4 %1, off\n\ts_mov_b32 m0, %0" : "=&s"(sv) : "v"(g), "s"(lds_base) : "memory");
}
DI u32x4 pack8(const f32x16& a, int s) {
  u32x4 r;
#pragma unroll
  for (int i = 0; i < 4; ++i) r[i] = pk(a[8 * s + 2 * i], a[8 * s + 2 * i + 1]);
  return r;
}
#define RETRY(tag) do { } while (0)
template <int N> DI void ld_handoff(u32x4 (&v)[N], rsrc_t rs, int voff, int soff) {
#pragma unroll
  for (int i = 0; i < N; ++i) { v[i] = ld16<16>(rs, voff + (i & 3) * KB, soff + (i >> 2) * 4 * KB); SBAR(); }
}
template <int N> DI bool any_sent(const u32x4 (&v)[N]) {
  unsigned m = 0;
#pragma unroll
  for (int i = 0; i < N; ++i) m = smax(m, v[i]);
  return __builtin_amdgcn_ballot_w64(m == SENT) != 0ull;
}
template <int N> DI void chk_handoff(u32x4 (&v)[N], rsrc_t rs, int voff, int soff, int tag) {
  if (__builtin_expect(any_sent<N>(v), 0)) {
#pragma unroll 1
    for (int spin = 0; spin < SPIN_CAP; ++spin) {
      CBAR();
      RETRY(tag);
      if (RSLP) __builtin_amdgcn_s_sleep(RSLP);
#pragma unroll
      for (int i = 0; i < N; ++i)
        if (__builtin_amdgcn_ballot_w64(smax(0u, v[i]) == SENT) != 0ull) v[i] = ld16<16>(rs, voff + (i & 3) * KB, soff + (i >> 2) * 4 * KB);
      if (!any_sent<N>(v)) break;
    }
  }
}

#define STAMP_DECL
#define STAMP_DUMP() do { } while (0)
#define STAMP(i) do { } while (0)

#define SPAN_T(i) do { } while (0)
struct Params {
  const unsigned short *x, *wq, *wk, *wv, *woa, *wqkv, *wob;
  unsigned short* out;
  unsigned char* ws;
};

DI void ln_fix_tok(f32x16& a, ldsp cs, int h, float mu, float sc) {
#pragma unroll
  for (int g = 0; g < 4; ++g) {
    f32x4 c = lds_rf4(cs + (8 * g + 4 * h) * 4);
#pragma unroll
    for (int i = 0; i < 4; ++i) a[4 * g + i] = (a[4 * g + i] - mu * c[i]) * sc;
  }
}
DI void ln_fix_col(f32x16& a, ldsp mup, ldsp rsp, int h, float cs) {
#pragma unroll
  for (int g = 0; g < 4; ++g) {
    f32x4 m = lds_rf4(mup + (8 * g + 4 * h) * 4), r = lds_rf4(rsp + (8 * g + 4 * h) * 4);
#pragma unroll
    for (int i = 0; i < 4; ++i) a[4 * g + i] = (a[4 * g + i] - m[i] * cs) * r[i];
  }
}

constexpr int XI_KS = 1056;
constexpr int P_XI = 0, P_WI = 2 * 28 * XI_KS, P_ST = P_WI + 448 * 128, P_CS = P_ST + 512, P_CS2 = P_CS + 4 * 128, P_SG = P_CS2 + 4 * 128, P_TI = P_SG + 32, P_BYTES = P_TI + 4 * 2048;
DI u64 phase0(const Params& p, const ldsp lds) {
  const int tid = threadIdx.x, lane = tid & 63, r = lane & 31, h = lane >> 5;
  const int w = __builtin_amdgcn_readfirstlane(tid >> 6);
  const int b = blockIdx.x;
  const rsrc_t ws = mkrs(p.ws, WS_BYTES);
  const int pair = -1; (void)pair;
  STAMP_DECL; STAMP(0); SPAN_T(0);
  const u64 epo = ld_tag(p.ws + OFF_EPO);
  const int xcd = b & 7, jj = b >> 3, rb64 = 4 * xcd + (jj >> 3), cp = jj & 7;
  const int q8 = tid & 7, row8 = tid >> 3;
  u32x4 xv[7];
  {
    const unsigned short* xr = p.x + (size_t)(rb64 * 64 + row8) * D_ + q8 * 8;
#pragma unroll
    for (int i = 0; i < 7; ++i) xv[i] = *(const u32x4*)(xr + i * 64);
  }
  u32x4 wv[7];
  if (cp < 7) {
    const unsigned short* wr = p.wq + (size_t)row8 * 448 + cp * 64 + (q8 ^ ((row8 & 2) << 1)) * 8;
#pragma unroll
    for (int i = 0; i < 7; ++i) { glds16(wr + i * 64 * 448, (unsigned)(uintptr_t)(lds + P_WI) + (i * 8 + w) * KB); wv[i] = u32x4{}; }
  } else {
    const unsigned short* wr = ((q8 >> 2) ? p.wv : p.wk) + (size_t)row8 * 28 + (q8 & 3) * 8;
    const bool last = (q8 & 3) == 3;
#pragma unroll
    for (int i = 0; i < 7; ++i) {
      const u32x2 a = *(const u32x2*)(wr + i * 64 * 28);
      u32x2 c = *(const u32x2*)(wr + i * 64 * 28 + (last ? 0 : 4));
      wv[i] = u32x4{a[0], a[1], c[0], c[1]};
    }
  }
  const int w4 = w & 3, tt = b >> 2, tks = (b & 3) * 7 + 2 * w4, wog = b * 4 + w4;
  u32x2 tv[4];
  unsigned te[8];
  bool dead = false;
  if (w >= 4) {
    if (w == 4) st4<16>(0u, ws, h * PAIR_SZ + r * 4, OFF_PAIR + R_FLG + b * 128);
    const unsigned short* src; int pitch; bool pad = true;
    if (tt < 14) { src = p.wq + 32 * tt; pitch = 448; pad = false; }
    else if (tt < 16) { src = tt == 14 ? p.wk : p.wv; pitch = 28; }
    else { src = p.wqkv + ((tt - 16) % 3) * 448 + ((tt - 16) / 3) * 28; pitch = 1344; }
    const int pc = lane & 7, rw = lane >> 3;
    dead = pad && pc == 7;
    src += (size_t)(tks * 16 + rw) * pitch + (dead ? 0 : pc * 4);
#pragma unroll
    for (int i = 0; i < 4; ++i) tv[i] = *(const u32x2*)(src + (size_t)((w4 == 3 && i >= 2) ? 0 : i * 8) * pitch);
    int g = wog < 896 ? wog : 0;
    const unsigned short* wo = g >= 448 ? p.wob : p.woa;
    if (g >= 448) g -= 448;
    const int ct = g >> 5, ks = g & 31, head = ks >> 1;
    const int k0 = head * 28 + 16 * (ks & 1) + 4 * h, kmax = head * 28 + 28;
#pragma unroll
    for (int j = 0; j < 8; ++j) {
      const int k = k0 + 8 * (j >> 2) + (j & 3);
      te[j] = wo[(k < kmax ? k : 0) * 448 + 32 * ct + r];
    }
  }
  SBAR();
  {
    float sm[2] = {}, sq[2] = {};
#pragma unroll
    for (int i = 0; i < 7; ++i) {
      const int ks = 4 * i + (q8 >> 1);
      const ldsp dst = lds + P_XI + ((row8 >> 5) * 28 + ks) * XI_KS + (row8 & 31) * 16 + (q8 & 1) * 8;
      *(LAS u32x2*)dst = u32x2{xv[i][0], xv[i][1]};
      *(LAS u32x2*)(dst + 512) = u32x2{xv[i][2], xv[i][3]};
#pragma unroll
      for (int d = 0; d < 4; ++d) { sm[d & 1] = dot2(xv[i][d], ONES2, sm[d & 1]); sq[d & 1] = dot2(xv[i][d], xv[i][d], sq[d & 1]); }
    }
    float a = sm[0] + sm[1], c = sq[0] + sq[1];
#define DPP_ADD(v, ctl) v += __uint_as_float(__builtin_amdgcn_update_dpp(0u, __float_as_uint(v), ctl, 0xF, 0xF, true))
    DPP_ADD(a, 0xB1); DPP_ADD(c, 0xB1); DPP_ADD(a, 0x4E); DPP_ADD(c, 0x4E); DPP_ADD(a, 0x141); DPP_ADD(c, 0x141);
    const float mu = a * (1.f / D_), var = __builtin_fmaxf(c * (1.f / D_) - mu * mu, 0.f);
    if (q8 == 0) { *(LAS float*)(lds + P_ST + row8 * 4) = mu; *(LAS float*)(lds + P_ST + 256 + row8 * 4) = __builtin_amdgcn_rsqf(var + EPS); }
  }
  if (cp == 7) {
#pragma unroll
    for (int i = 0; i < 7; ++i) lds_w16(lds + P_WI + (row8 + 64 * i) * 128 + (q8 ^ ((row8 & 2) << 1)) * 16, wv[i]);
  }
  if (tid < 8) *(LAS unsigned*)(lds + P_SG + tid * 4) = 0u;
#define PRE_SIG() do { CBAR(); *(volatile LAS unsigned*)(lds + P_SG + w * 4) = 1u; CBAR(); } while (0)
#define PRE_WAIT(k) do { _Pragma("unroll 1") for (int spin = 0; spin < (1 << 20); ++spin) { if (__builtin_amdgcn_readfirstlane(*(volatile LAS unsigned*)(lds + P_SG + (k) * 4))) break; } CBAR(); } while (0)
  asm volatile("s_waitcnt vmcnt(0)" ::: "memory");
  const u64 tag = (((u64)__builtin_amdgcn_readfirstlane((unsigned)(epo >> 32)) << 32) | __builtin_amdgcn_readfirstlane((unsigned)epo)) + 1ull;
  BAR();
  STAMP(1);
  if (w >= 4) {
    const ldsp img = lds + P_TI + w4 * 2048;
#pragma unroll
    for (int i = 0; i < 4; ++i) *(LAS u32x2*)(img + i * 512 + lane * 8) = dead ? u32x2{0u, 0u} : tv[i];
    CBAR();
    const int i16 = lane & 15;
    const ldsp tb = img + (4 * h + (i16 >> 2)) * 64 + ((lane >> 4) & 1) * 32 + (i16 & 3) * 8;
    float cs = 0.f;
#pragma unroll
    for (int ks = 0; ks < 2; ++ks) {
      const s16x4 lo = __builtin_amdgcn_ds_read_tr16_b64_v4i16((LAS s16x4*)(tb + ks * 1024));
      const s16x4 hi = __builtin_amdgcn_ds_read_tr16_b64_v4i16((LAS s16x4*)(tb + ks * 1024 + 512));
      const u32x4 o = __builtin_bit_cast(u32x4, __builtin_shufflevector(lo, hi, 0, 1, 2, 3, 4, 5, 6, 7));
      if (ks == 0 || w4 < 3) {
        st16<PRE_AUX>(o, ws, lane * 16, (tt * 28 + tks + ks) * KB);
#pragma unroll
        for (int i = 0; i < 4; ++i) cs = dot2(o[i], ONES2, cs);
      }
    }
    {
      u32x4 o;
#pragma unroll
      for (int i = 0; i < 4; ++i) { asm volatile("" : "+v"(te[2 * i]), "+v"(te[2 * i + 1])); o[i] = te[2 * i] | (te[2 * i + 1] << 16); }
      if ((wog & 1) && h == 1) { o[2] = 0u; o[3] = 0u; }
      if (wog < 896) st16<PRE_AUX>(o, ws, lane * 16, OFF_WOA + wog * KB);
    }
    cs = xhalf_add(cs);
    if (h == 0) *(LAS float*)(lds + P_CS2 + w4 * 128 + r * 4) = cs;
    if (w4) asm volatile("s_waitcnt vmcnt(0)" ::: "memory");
    PRE_SIG();
    if (w4 == 0) {
      PRE_WAIT(5); PRE_WAIT(6); PRE_WAIT(7);
      const float t = (*(LAS float*)(lds + P_CS2 + r * 4) + *(LAS float*)(lds + P_CS2 + 128 + r * 4)) + (*(LAS float*)(lds + P_CS2 + 256 + r * 4) + *(LAS float*)(lds + P_CS2 + 384 + r * 4));
      if (h == 0) st4<16>(__float_as_uint(t), ws, r * 4, OFF_CS + b * 128);
      asm volatile("s_waitcnt vmcnt(0)" ::: "memory");
      if (lane < 8) st_tag(p.ws + OFF_INI + (lane * 256 + b) * 8, tag);
    }
    STAMP(2); STAMP_DUMP();
    return tag;
  }
  const int rt2 = w >> 1, ct2 = w & 1, tile = 2 * cp + ct2, rt = rb64 * 2 + rt2;
  const bool isV = tile == 15;
  const int i16 = lane & 15, tq = i16 >> 2, tp = i16 & 3, blk = (lane >> 4) & 1;
  const ldsp wbase = lds + P_WI + (4 * h + tq) * 128 + ((ct2 ^ ((tq >> 1) & 1)) * 64) + blk * 32 + tp * 8;
  const ldsp xbase = lds + P_XI + rt2 * 28 * XI_KS + lane * 16;
  f32x16 acc = {}, csa = {};
  const u32x4 ones = {ONES2, ONES2, ONES2, ONES2};
  u32x4 wr[4], xr[4];
  const ldsp wb0 = wbase + rt2 * 2048, wb1 = wbase + (1 - rt2) * 2048, xb0 = xbase + rt2 * XI_KS, xb1 = xbase + (1 - rt2) * XI_KS;
#define PRE_LD(i) do { const ldsp wp_ = (((i) & 1) ? wb1 : wb0) + ((i) >> 1) * 4096; \
    const s16x4 lo_ = __builtin_amdgcn_ds_read_tr16_b64_v4i16((LAS s16x4*)(wp_)), hi_ = __builtin_amdgcn_ds_read_tr16_b64_v4i16((LAS s16x4*)(wp_ + 1024)); \
    wr[(i) & 3] = __builtin_bit_cast(u32x4, __builtin_shufflevector(lo_, hi_, 0, 1, 2, 3, 4, 5, 6, 7)); \
    xr[(i) & 3] = lds_r16((((i) & 1) ? xb1 : xb0) + ((i) >> 1) * 2 * XI_KS); } while (0)
#define PRE_LOOP(SW) \
  PRE_LD(0); PRE_LD(1); PRE_LD(2); SBAR(); \
  _Pragma("unroll") for (int i = 0; i < 28; ++i) { \
    if (i + 3 < 28) PRE_LD(i + 3); \
    SBAR(); \
    acc = SW ? MFMA(xr[i & 3], wr[i & 3], acc) : MFMA(wr[i & 3], xr[i & 3], acc); \
    if (!(i & 1)) csa = SW ? MFMA(ones, wr[i & 3], csa) : MFMA(wr[i & 3], ones, csa); \
    SBAR(); \
  }
  u32x4 o0, o1;
  if (!isV) {
    PRE_LOOP(false)
    if (r == 0) {
#pragma unroll
      for (int g = 0; g < 4; ++g) lds_wf4(lds + P_CS + w * 128 + (8 * g + 4 * h) * 4, f32x4{csa[4 * g], csa[4 * g + 1], csa[4 * g + 2], csa[4 * g + 3]});
    }
    const float mu = *(LAS float*)(lds + P_ST + (rt2 * 32 + r) * 4), rs = *(LAS float*)(lds + P_ST + 256 + (rt2 * 32 + r) * 4);
    PRE_SIG(); PRE_WAIT(w ^ 2);
    const float sc = tile < 14 ? rs * C2 : rs;
#pragma unroll
    for (int g = 0; g < 4; ++g) {
      const f32x4 c = lds_rf4(lds + P_CS + (w ^ 2) * 128 + (8 * g + 4 * h) * 4);
#pragma unroll
      for (int i = 0; i < 4; ++i) acc[4 * g + i] = (acc[4 * g + i] - mu * (c[i] + csa[4 * g + i])) * sc;
    }
    o0 = pack8(acc, 0); o1 = pack8(acc, 1);
    if (tile == 14 && h == 1) { o1[2] = ONES2; o1[3] = 0x00003F80u; }
  } else {
    PRE_LOOP(true)
    if (h == 0) *(LAS float*)(lds + P_CS + w * 128 + r * 4) = csa[0];
    PRE_SIG(); PRE_WAIT(w ^ 2);
    ln_fix_col(acc, lds + P_ST + rt2 * 128, lds + P_ST + 256 + rt2 * 128, h, csa[0] + *(LAS float*)(lds + P_CS + (w ^ 2) * 128 + r * 4));
    o0 = pack8(acc, 0); o1 = pack8(acc, 1);
    if (r >= 28) { const unsigned f = r == 28 ? ONES2 : 0u; o0 = u32x4{f, f, f, f}; o1 = o0; }
  }
  const int dst = OFF_PAIR + (tile < 14 ? R_QA + (rt * 14 + tile) * 2 * KB : R_KVA + rt * 4 * KB + (tile - 14) * 2 * KB);
  st16<16>(o0, ws, lane * 16, dst);
  st16<16>(o1, ws, lane * 16 + KB, dst);
  asm volatile("s_waitcnt vmcnt(0)" ::: "memory");
  if (lane == 8 || (lane < 8 && tile >= 14))
    st_tag(p.ws + (lane == 8 ? OFF_P0F + (rt * 16 + tile) * 8 : OFF_PAIR + R_FKV + (lane * 64 + rt) * 128 + (tile - 13) * 8), tag);
  *(volatile LAS unsigned*)(lds + P_SG + w * 4) = 2u;
  STAMP(2); STAMP_DUMP();
  return tag;
}

constexpr int L_SCR = 0;
constexpr int L_WB = 64 * KB;
constexpr int L_QF = 148 * KB;
constexpr int L_MISC = L_QF + 8 * KB;
constexpr int M_SIG = L_MISC;
constexpr int M_ST = M_SIG + 512;
constexpr int M_CS = M_ST + 1280;
constexpr int L_BYTES = M_CS + 1024;
static_assert(L_BYTES <= 160 * KB && P_BYTES <= L_QF, "LDS");

DI void exp_pack(const f32x16& s, u32x4& p0, u32x4& p1) {
#pragma unroll
  for (int i = 0; i < 4; ++i) p0[i] = pk(__builtin_amdgcn_exp2f(s[2 * i]), __builtin_amdgcn_exp2f(s[2 * i + 1]));
#pragma unroll
  for (int i = 0; i < 4; ++i) p1[i] = pk(__builtin_amdgcn_exp2f(s[8 + 2 * i]), __builtin_amdgcn_exp2f(s[8 + 2 * i + 1]));
}
DI void set_shift(u32x4& q1, float m, int h, unsigned hi31) {
  const unsigned a = pk(m, m); const float r1 = m - bflo(a);
  const unsigned c = pk(r1, r1); const float r2 = r1 - bflo(c);
  const unsigned e = pk(r2, 0.f);
  const unsigned d2 = ((a & 0xFFFFu) | (c << 16)) ^ 0x80008000u, d3 = (e ^ 0x00008000u) | hi31;
  q1[2] = h ? d2 : q1[2]; q1[3] = h ? d3 : q1[3];
}
#define PIN(x) asm volatile("" : "+v"(x))
template <bool DOQK, bool DOPVP, class FA, class FB, class FC>
DI void unit(const f32x16& s, f32x16& sn, const f32x16& cn, f32x16& o, f32x16& op, u32x4& pp, const u32x4& kn0, const u32x4& kn1, const u32x4& qn0,
             const u32x4& qn1, const u32x4& v0, const u32x4& v1p, FA ha, FB hb, FC hc) {
  float e[16]; u32x4 p0;
#define E_(i) e[i] = __builtin_amdgcn_exp2f(s[i]); PIN(e[i]); SBAR()
#define C0_(i) p0[i] = pk(e[2 * i], e[2 * i + 1]); PIN(p0[i]); SBAR()
#define C1_(i) pp[i] = pk(e[8 + 2 * i], e[8 + 2 * i + 1]); PIN(pp[i]); SBAR()
  SBAR();
  if (DOQK) { sn = MFMA(kn0, qn0, cn); SBAR(); }
  E_(0); E_(1); E_(2); E_(3);
  if (DOPVP) { op = MFMA(v1p, pp, op); SBAR(); }
  ha(); SBAR();
  E_(4); C0_(0); E_(5); C0_(1); E_(6); E_(7);
  if (DOQK) { sn = MFMA(kn1, qn1, sn); SBAR(); }
  hb(); SBAR();
  E_(8); C0_(2); E_(9); C0_(3); E_(10); E_(11);
  o = MFMA(v0, p0, o); SBAR();
  hc(); SBAR();
  E_(12); C1_(0); E_(13); C1_(1); E_(14); E_(15); C1_(2); C1_(3);
}

struct Ctx { unsigned char* flg; ldsp sig; int lane; int w; };
DI unsigned now32() { return (unsigned)__builtin_amdgcn_s_memrealtime(); }
struct Clk { unsigned c, r; };
DI Clk clk_now() { Clk k; k.c = (unsigned)__builtin_amdgcn_s_memtime(); k.r = now32(); return k; }
DI unsigned eta(const Clk& k0, int dcyc) {
  const Clk k = clk_now();
  return (k.r + (unsigned)((float)dcyc * (float)(k.r - k0.r) * __builtin_amdgcn_rcpf((float)(k.c - k0.c)))) | 1u;
}
DI void post(const Ctx& c, int b, int slot, const Clk& k0, int dcyc) {
  const unsigned t = eta(k0, dcyc);
  if (c.lane == 0) __hip_atomic_store((GAS unsigned*)(c.flg + b * 128 + slot * 4), t, __ATOMIC_RELAXED, __HIP_MEMORY_SCOPE_AGENT);
}
DI unsigned flag_ld(const Ctx& c, int pb, int slot) { return __hip_atomic_load((GAS unsigned*)(c.flg + pb * 128 + slot * 4), __ATOMIC_RELAXED, __HIP_MEMORY_SCOPE_AGENT); }
template <int SLP = 1> DI void poll_wait(const Ctx& c, int pb, int slot) {
  unsigned f = 0;
#pragma unroll 1
  for (int spin = 0; spin < SPIN_CAP; ++spin) {
    f = __hip_atomic_load((GAS unsigned*)(c.flg + pb * 128 + slot * 4), __ATOMIC_RELAXED, __HIP_MEMORY_SCOPE_AGENT);
    if (__builtin_amdgcn_ballot_w64(f == 0u) == 0ull) break;
    __builtin_amdgcn_s_sleep(SLP);
  }
#pragma unroll 1
  for (int spin = 0; spin < 256; ++spin) {
    if (__builtin_amdgcn_ballot_w64((int)(f - now32()) > 0) == 0ull) break;
    __builtin_amdgcn_s_sleep(1);
  }
  CBAR();
}
DI void lds_sig(const Ctx& c, int idx) {
  CBAR();
  *(volatile LAS unsigned*)(c.sig + idx * 16 + c.w * 4) = 1u;
  CBAR();
}
template <int SLP> DI void lds_wait(const Ctx& c, int idx, unsigned n) {
#pragma unroll 1
  for (int spin = 0; spin < (SPIN_CAP << 6); ++spin) {
    const u32x4 v = *(volatile LAS u32x4*)(c.sig + idx * 16);
    if (__builtin_amdgcn_readfirstlane(v[0] + v[1] + v[2] + v[3]) >= n) break;
    if (SLP) __builtin_amdgcn_s_sleep(SLP);
  }
  CBAR();
}
template <int N, bool SWAP, class F> DI f32x16 gemm_lds(const u32x4 (&wr)[N], ldsp bb, F mid) {
  f32x16 acc = {};
  u32x4 bf[8];
#pragma unroll
  for (int i = 0; i < 8; ++i) bf[i] = lds_r16(bb + i * KB);
  SBAR();
#pragma unroll
  for (int i = 0; i < N; ++i) {
    acc = SWAP ? MFMA(bf[i & 7], wr[i], acc) : MFMA(wr[i], bf[i & 7], acc);
    SBAR();
    if (i + 8 < N) bf[i & 7] = lds_r16(bb + (i + 8) * KB);
    mid(i);
    SBAR();
  }
  return acc;
}
DI void add_res(const f32x16& a, const u32x4& x0, const u32x4& x1, u32x4& y0, u32x4& y1) {
#pragma unroll
  for (int i = 0; i < 4; ++i) {
    y0[i] = pk(a[2 * i] + bflo(x0[i]), a[2 * i + 1] + bfhi(x0[i]));
    y1[i] = pk(a[8 + 2 * i] + bflo(x1[i]), a[8 + 2 * i + 1] + bfhi(x1[i]));
  }
}
DI float row_stats(const u32x4& y0, const u32x4& y1, int h) {
  float s0 = 0.f, s1 = 0.f, q0 = 0.f, q1 = 0.f;
#pragma unroll
  for (int i = 0; i < 4; ++i) { s0 = dot2(y0[i], ONES2, s0); s1 = dot2(y1[i], ONES2, s1); q0 = dot2(y0[i], y0[i], q0); q1 = dot2(y1[i], y1[i], q1); }
  const float s = xhalf_add(s0 + s1), q = xhalf_add(q0 + q1);
  return h ? q : s;
}
DI void stats_finish(float v, float& mu, float& rs) {
  auto t = __builtin_amdgcn_permlane32_swap(__float_as_uint(v), __float_as_uint(v), false, false);
  mu = __uint_as_float(t[0]) * (1.f / D_);
  rs = __builtin_amdgcn_rsqf(__builtin_fmaxf(__uint_as_float(t[1]) * (1.f / D_) - mu * mu, 0.f) + EPS);
}
DI void stats_gather(rsrc_t ws, int so, ldsp dst, int lane) {
#pragma unroll 1
  for (int spin = 0; spin < SPIN_CAP; ++spin) {
    CBAR();
    unsigned sv[14], mx = 0;
#pragma unroll
    for (int i = 0; i < 14; ++i) sv[i] = ld4<16>(ws, lane * 4 + i * 256, so);
    float t0 = 0.f, t1 = 0.f, mu, rs;
#pragma unroll
    for (int i = 0; i < 14; i += 2) { t0 += __uint_as_float(sv[i]); t1 += __uint_as_float(sv[i + 1]); mx = umax3(mx, sv[i], sv[i + 1]); }
    stats_finish(t0 + t1, mu, rs);
    *(LAS float*)(dst + lane * 4) = lane >= 32 ? rs : mu;
    if (__builtin_amdgcn_ballot_w64(mx == SENT) == 0ull) break;
    RETRY(5);
  }
}
DI void norm_pack(const f32x16& o, int h, u32x4& y0, u32x4& y1) {
  const float inv = __builtin_amdgcn_rcpf(from_hi(o[12]));
#pragma unroll
  for (int i = 0; i < 4; ++i) { y0[i] = pk(o[2 * i] * inv, o[2 * i + 1] * inv); y1[i] = pk(o[8 + 2 * i] * inv, o[8 + 2 * i + 1] * inv); }
  if (h == 1) { y1[2] = 0; y1[3] = 0; }
}
constexpr float LBIG = 1e30f;
constexpr unsigned NEG31 = 0xF1490000u;

__global__ void __launch_bounds__(512, 2) k_main(Params p) {
  __shared__ __attribute__((aligned(16))) char smem[L_BYTES];
  const ldsp lds = (ldsp)smem;
  const int tid = threadIdx.x;
  const int w8 = __builtin_amdgcn_readfirstlane(tid >> 6);
  const rsrc_t wt = mkrs(p.ws, OFF_PAIR);
  auto nop = []() {};
  const f32x16 Z = {};
  if (tid < 128) *(LAS unsigned*)(lds + M_SIG + tid * 4) = 0u;
  if (CODE_KB && w8 == 7) {
    const unsigned char* pc = (const unsigned char*)(__builtin_amdgcn_s_getpc() & ~127ull);
    const int ln = (((blockIdx.x >> 3) * 64 + (tid & 63)) % (CODE_KB * 8)) * 128;
    glds16(pc + ln, (unsigned)(uintptr_t)(lds + L_QF));
  }
  const u64 tag = phase0(p, lds);
  STAMP_DECL;
#define PAIR_SETUP() \
    int lane = tid & 63, b = blockIdx.x, w = w8 & 3; \
    asm volatile("" : "+v"(lane), "+s"(b), "+s"(w)); \
    lane &= 63; b &= 255; w &= 3; \
    const int r = lane & 31, h = lane >> 5, l16 = lane * 16, xcd = b & 7, jj = b >> 3; \
    const int rt = 8 * xcd + (jj >> 2), sub = jj & 3;          \
    const int rb = 2 * xcd + (jj >> 4), hd = jj & 15;          \
    const ldsp scr = lds + L_SCR + l16; \
    unsigned char* const reg = p.ws + OFF_PAIR + pair * PAIR_SZ; \
    const rsrc_t ws = mkrs(reg, 2 * PAIR_SZ - pair * PAIR_SZ); \
    const Ctx cx = {reg + R_FLG, lds + M_SIG + pair * 256, lane, w}; \
    STAMP(0)

  if (w8 < 4) {
#pragma unroll 1
   for (int pair = 0; pair < 2; ++pair) {
    PAIR_SETUP();
    const unsigned char* const kpa = reg + R_FKV + (xcd * 64 + 4 * ((2 * xcd + 1 - (lane >> 1)) & 15) + w) * 128 + 8 + (lane & 1) * 8;
    unsigned rdy = 0xFFFFFFFFu, chk = pair ? 2u : 0u, pend = 3u;
    int kvi = 0;
    u64 kvf;
    auto kv_mask = [&]() { const unsigned bl = (unsigned)__builtin_amdgcn_ballot_w64(kvf == tag); rdy = bl & (bl >> 1); };
    auto kv_next = [&]() -> int {
      if (__builtin_expect(((rdy >> (2 * kvi)) & 1u) == 0u, 0)) {
#pragma unroll 1
        for (int spin = 0; spin < SPIN_CAP; ++spin) {
          kvf = ld_tag(kpa);
          kv_mask();
          if ((rdy >> (2 * kvi)) & 1u) break;
          __builtin_amdgcn_s_sleep(2);
        }
        CBAR();
      }
      const int o = R_KVA + (4 * ((2 * xcd + 1 - kvi) & 15) + w) * 4 * KB;
      ++kvi;
      return o;
    };
    {
      u32x4 q[4][2], K[4][2], V[4][2];
      int kb[4];
      if (pair) {
        poll_wait(cx, ((((4 * (lane & 1) + w) << 2) | 3) << 3) | xcd, 12);
      } else {
        __builtin_amdgcn_s_sleep(KV_DLY);
#pragma unroll 1
        for (int spin = 0; spin < SPIN_CAP; ++spin) {
          if (((unsigned)__builtin_amdgcn_ballot_w64(ld_tag(kpa) != tag) & 0xFu) == 0u) break;
          __builtin_amdgcn_s_sleep(2);
        }
        CBAR();
      }
      STAMP(23);
#pragma unroll
      for (int t = 0; t < 2; ++t) {
        kb[t] = kv_next();
        K[t][0] = ld16<16>(ws, l16, kb[t]); K[t][1] = ld16<16>(ws, l16 + KB, kb[t]);
        V[t][0] = ld16<16>(ws, l16 + 2 * KB, kb[t]); V[t][1] = ld16<16>(ws, l16 + 3 * KB, kb[t]);
        SBAR();
      }
#pragma unroll
      for (int t = 2; t < 4; ++t) { kb[t] = kb[0]; K[t][0] = K[t][1] = V[t][0] = V[t][1] = u32x4{}; }
      if (pair) { chk_handoff<2>(K[0], ws, l16, kb[0], 6); chk_handoff<2>(V[0], ws, l16 + 2 * KB, kb[0], 6); }
      f32x16 o0 = {}, o1 = {}, o2 = {}, o3 = {};
      PIN(o0); PIN(o1); PIN(o2); PIN(o3);
      kvf = ld_tag(kpa);
      STAMP(24);
      BAR();
#pragma unroll
      for (int a = 0; a < 4; ++a) { q[a][0] = lds_r16(lds + L_QF + l16 + 2 * a * KB); q[a][1] = lds_r16(lds + L_QF + l16 + (2 * a + 1) * KB); }
      STAMP(1);
      f32x16 sa = MFMA(K[0][0], q[0][0], Z), sb;
      sa = MFMA(K[0][1], q[0][1], sa);
      u32x4 pp = {};
#define TILE(c, LD, LDV1) { \
        constexpr int n = (c + 1) & 3, pv = (c + 3) & 3; \
        auto ldV1 = [&]() { if (LDV1) V[pv][1] = ld16<16>(ws, l16 + 3 * KB, kb[pv]); }; \
        auto ldK = [&]() { if (LD) { kb[c] = kv_next(); K[c][0] = ld16<16>(ws, l16, kb[c]); K[c][1] = ld16<16>(ws, l16 + KB, kb[c]); } }; \
        auto ldV0 = [&]() { if (LD) V[c][0] = ld16<16>(ws, l16 + 2 * KB, kb[c]); }; \
        unit<true, true>(sa, sb, Z, o0, o3, pp, K[c][0], K[c][1], q[1][0], q[1][1], V[c][0], V[pv][1], ldV1, nop, nop); \
        if (LD && c == 0 && __builtin_expect(pend != 0u, 0)) {        \
          pend = 0u; \
          _Pragma("unroll") for (int t = 2; t < 4; ++t) { kb[t] = kv_next(); K[t][0] = ld16<16>(ws, l16, kb[t]); K[t][1] = ld16<16>(ws, l16 + KB, kb[t]); \
            V[t][0] = ld16<16>(ws, l16 + 2 * KB, kb[t]); V[t][1] = ld16<16>(ws, l16 + 3 * KB, kb[t]); } } \
        unit<true, true>(sb, sa, Z, o1, o0, pp, K[c][0], K[c][1], q[2][0], q[2][1], V[c][0], V[c][1], nop, nop, nop); \
        unit<true, true>(sa, sb, Z, o2, o1, pp, K[c][0], K[c][1], q[3][0], q[3][1], V[c][0], V[c][1], nop, ldK, nop); \
        if (LD && c == 0 && __builtin_expect(chk != 0u, 0)) { chk = 0u; chk_handoff<2>(K[n], ws, l16, kb[n], 6); chk_handoff<2>(V[n], ws, l16 + 2 * KB, kb[n], 6); } \
        unit<true, true>(sb, sa, Z, o3, o2, pp, K[n][0], K[n][1], q[0][0], q[0][1], V[c][0], V[c][1], nop, nop, ldV0); }
#pragma unroll 1
      for (int tt = 0; tt < 12; tt += 4) {
        kv_mask(); kvf = ld_tag(kpa);
        TILE(0, true, true) TILE(1, true, true)
        if (tt == 0) { STAMP(27); }
        kv_mask(); kvf = ld_tag(kpa);
        TILE(2, true, true) TILE(3, true, true)
        if (tt == 0) { STAMP(28); } if (tt == 4) { STAMP(29); }
      }
      STAMP(2);
      TILE(0, false, true)
      lds_sig(cx, 11);
      TILE(1, false, false)
      STAMP(3);
      const ldsp red = scr + w * 4 * KB;
#define WR_LO(o, a) [&]() { lds_wf4(red + (a) * 16 * KB, f32x4{o[0], o[1], o[2], o[3]}); lds_wf4(red + (a) * 16 * KB + KB, f32x4{o[4], o[5], o[6], o[7]}); }
#define WR_HI(o, a) [&]() { lds_wf4(red + (a) * 16 * KB + 2 * KB, f32x4{o[8], o[9], o[10], o[11]}); lds_wf4(red + (a) * 16 * KB + 3 * KB, f32x4{o[12], o[13], o[14], o[15]}); lds_sig(cx, a); }
      unit<true, true>(sa, sb, Z, o0, o3, pp, K[3][0], K[3][1], q[0][0], q[0][1], V[2][0], V[1][1], nop, nop, nop);
      unit<true, true>(sb, sa, Z, o0, o0, pp, K[2][0], K[2][1], q[1][0], q[1][1], V[3][0], V[2][1], nop, nop, nop);
      unit<true, true>(sa, sb, Z, o1, o0, pp, K[3][0], K[3][1], q[1][0], q[1][1], V[2][0], V[3][1], nop, WR_LO(o0, 0), WR_HI(o0, 0));
      unit<true, true>(sb, sa, Z, o1, o1, pp, K[2][0], K[2][1], q[2][0], q[2][1], V[3][0], V[2][1], nop, nop, nop);
      unit<true, true>(sa, sb, Z, o2, o1, pp, K[3][0], K[3][1], q[2][0], q[2][1], V[2][0], V[3][1], nop, WR_LO(o1, 1), WR_HI(o1, 1));
      unit<true, true>(sb, sa, Z, o2, o2, pp, K[2][0], K[2][1], q[3][0], q[3][1], V[3][0], V[2][1], nop, nop, nop);
      unit<true, true>(sa, sb, Z, o3, o2, pp, K[3][0], K[3][1], q[3][0], q[3][1], V[2][0], V[3][1], nop, WR_LO(o2, 2), WR_HI(o2, 2));
      unit<false, true>(sb, sa, Z, o3, o3, pp, K[2][0], K[2][1], q[3][0], q[3][1], V[3][0], V[2][1], nop, nop, nop);
      o3 = MFMA(V[3][1], pp, o3);
      WR_LO(o3, 3)(); WR_HI(o3, 3)();
    }
    STAMP(4);
    {
      u32x4 of[8] = {};
      const int src = R_OFA + (rt * 32 + 8 * w) * KB;
      if (w != sub) {
        lds_wait<1>(cx, 8, 4u);
        poll_wait(cx, (((jj & ~3) | w) << 3) | xcd, 0);
        STAMP(5);
        if (DLY_OA) __builtin_amdgcn_s_sleep(DLY_OA);
        ld_handoff<8>(of, ws, l16, src);
      }
      BAR();
      STAMP(6);
      const unsigned bad = __builtin_amdgcn_readfirstlane(*(LAS unsigned*)(cx.sig + 10 * 16));
      if (__builtin_expect((bad >> w) & 1u, 0)) {
        const u32x4 q0 = lds_r16(lds + L_QF + l16 + 2 * w * KB);
        u32x4 q1 = lds_r16(lds + L_QF + l16 + (2 * w + 1) * KB);
        if (h == 1) { q1[2] = 0; q1[3] = 0; }
        float m = -INFINITY;
#pragma unroll 1
        for (int t = 0; t < 64; ++t) {
          f32x16 s = MFMA(ld16<16>(ws, l16, R_KVA + t * 4 * KB), q0, Z);
          s = MFMA(ld16<16>(ws, l16 + KB, R_KVA + t * 4 * KB), q1, s);
          m = max16(s, m);
        }
        set_shift(q1, xhalf_max(m), h, 0u);
        f32x16 o = {};
#pragma unroll 1
        for (int t = 0; t < 64; ++t) {
          f32x16 s = MFMA(ld16<16>(ws, l16, R_KVA + t * 4 * KB), q0, Z);
          s = MFMA(ld16<16>(ws, l16 + KB, R_KVA + t * 4 * KB), q1, s);
          u32x4 p0, p1;
          exp_pack(s, p0, p1);
          o = MFMA(ld16<16>(ws, l16 + 2 * KB, R_KVA + t * 4 * KB), p0, o);
          o = MFMA(ld16<16>(ws, l16 + 3 * KB, R_KVA + t * 4 * KB), p1, o);
        }
        u32x4 y0, y1;
        norm_pack(o, h, y0, y1);
        st16<16>(y0, ws, l16, R_OFA + (rt * 32 + sub * 8 + 2 * w) * KB);
        st16<16>(y1, ws, l16 + KB, R_OFA + (rt * 32 + sub * 8 + 2 * w) * KB);
        lds_w16(scr + (sub * 8 + 2 * w) * KB, y0); lds_w16(scr + (sub * 8 + 2 * w + 1) * KB, y1);
      }
      if (w != sub) {
        chk_handoff<8>(of, ws, l16, src, 0);
        STAMP(7);
#pragma unroll
        for (int i = 0; i < 8; ++i) lds_w16(scr + (8 * w + i) * KB, of[i]);
      }
      BAR();
    }
    STAMP(8);
    f32x4 ck[4], cq[4];
    const float cv = *(LAS float*)(lds + M_CS + 6 * 128 + r * 4);
#pragma unroll
    for (int g = 0; g < 4; ++g) { cq[g] = lds_rf4(lds + M_CS + 4 * 128 + (8 * g + 4 * h) * 4); ck[g] = lds_rf4(lds + M_CS + 5 * 128 + (8 * g + 4 * h) * 4); }
    BAR();
    STAMP(9);
    u32x4 q0, q1, k0, k1, v0, v1;
    const Clk ck0 = clk_now();
    {
      const int xo = R_XF1 + (rb * 4 + w) * 28 * KB;
      const ldsp wb0 = opaque(lds + L_WB + l16), wb1 = opaque(lds + L_WB + 28 * KB + l16);
      const ldsp st = lds + M_ST + w * 256;
      f32x4 m4[4], r4[4];
      poll_wait(cx, (((jj & ~15) | (w << 2) | (lane & 3)) << 3) | xcd, 1);
      STAMP(10);
      f32x16 aq, ak, av;
      float mu, rs;
      u32x4 xf[28], wk[3], wv[3], wq[4];
#define LDX(i) do { xf[i] = ld16<16>(ws, l16 + ((i) & 3) * KB, xo + ((i) >> 2) * 4 * KB); SBAR(); } while (0)
#define CHKX(i) do { if (__builtin_expect(__builtin_amdgcn_ballot_w64(smax(0u, xf[i]) == SENT) != 0ull, 0)) { \
          _Pragma("unroll 1") for (int spin = 0; spin < SPIN_CAP; ++spin) { CBAR(); RETRY(1); LDX(i); if (__builtin_amdgcn_ballot_w64(smax(0u, xf[i]) == SENT) == 0ull) break; } } SBAR(); } while (0)
#define QST(i) do { if ((i) < 28) { SBAR(); aq = MFMA(wq[(i) & 3], xf[(i) < 28 ? (i) : 0], (i) ? aq : Z); SBAR(); if ((i) + 3 < 28) wq[((i) + 3) & 3] = lds_r16(wb0 + ((i) + 3) * KB); SBAR(); } } while (0)
#pragma unroll
      for (int i = 0; i < X1_AHEAD; ++i) LDX(i);
#pragma unroll
      for (int i = 0; i < 2; ++i) { wk[i] = lds_r16(wb1 + i * KB); wv[i] = lds_r16(wb1 + (28 + i) * KB); }
#pragma unroll
      for (int i = 0; i < 3; ++i) wq[i] = lds_r16(wb0 + i * KB);
      SBAR();
      CHKX(0);
#pragma unroll
      for (int ks = 0; ks < 28; ++ks) {
        ak = MFMA(wk[ks % 3], xf[ks], ks ? ak : Z); SBAR();
        if (ks + 2 < 28) { wk[(ks + 2) % 3] = lds_r16(wb1 + (ks + 2) * KB); wv[(ks + 2) % 3] = lds_r16(wb1 + (28 + ks + 2) * KB); }
        if (X1_AHEAD + 2 * ks < 28) LDX(X1_AHEAD + 2 * ks);
        if (ks == 25) {
          lds_wait<0>(cx, 12 + w, 1u);
          mu = *(LAS float*)(st + r * 4); rs = *(LAS float*)(st + 128 + r * 4);
        }
        if (ks == 26) {
#pragma unroll
          for (int g = 0; g < 4; ++g) { m4[g] = lds_rf4(st + (8 * g + 4 * h) * 4); r4[g] = lds_rf4(st + 128 + (8 * g + 4 * h) * 4); }
        }
        SBAR();
        av = MFMA(xf[ks], wv[ks % 3], ks ? av : Z); SBAR();
        if (X1_AHEAD + 2 * ks + 1 < 28) LDX(X1_AHEAD + 2 * ks + 1);
        if (ks < QS) QST(ks);
        if (ks + 1 < 28) CHKX(ks + 1);
        if (ks == 0) { STAMP(11); } if (ks == 13) { STAMP(12); }
        if (ks == POST_KV) post(cx, b, 4 + w, ck0, DT_KV);
      }
      STAMP(13);
      const int ko = R_KVB + (hd * 64 + rb * 4 + w) * 4 * KB;
      QST(QS); QST(QS + 1);
#pragma unroll
      for (int g = 0; g < 4; ++g) {
#pragma unroll
        for (int i = 4 * g; i < 4 * g + 4; ++i) ak[i] = (ak[i] - mu * ck[g][i & 3]) * rs;
        unsigned a0 = pk(ak[4 * g], ak[4 * g + 1]), a1 = pk(ak[4 * g + 2], ak[4 * g + 3]);
        if (g == 3 && h == 1) { a0 = ONES2; a1 = 0x00003F80u; }
        PIN(a0); PIN(a1);
        if (g < 2) { k0[2 * g] = a0; k0[2 * g + 1] = a1; } else { k1[2 * g - 4] = a0; k1[2 * g - 3] = a1; }
        if (g == 1) st16<16>(k0, ws, l16, ko);
        if (g == 3) st16<16>(k1, ws, l16 + KB, ko);
        QST(QS + 2 + g);
      }
      const unsigned vpad = r == 28 ? ONES2 : 0u;
#pragma unroll
      for (int g = 0; g < 4; ++g) {
#pragma unroll
        for (int i = 4 * g; i < 4 * g + 4; ++i) av[i] = (av[i] - m4[g][i & 3] * cv) * r4[g][i & 3];
        unsigned a0 = pk(av[4 * g], av[4 * g + 1]), a1 = pk(av[4 * g + 2], av[4 * g + 3]);
        if (r >= 28) { a0 = vpad; a1 = vpad; }
        PIN(a0); PIN(a1);
        if (g < 2) { v0[2 * g] = a0; v0[2 * g + 1] = a1; } else { v1[2 * g - 4] = a0; v1[2 * g - 3] = a1; }
        if (g == 1) st16<16>(v0, ws, l16 + 2 * KB, ko);
        if (g == 3) st16<16>(v1, ws, l16 + 3 * KB, ko);
        QST(QS + 6 + g);
      }
      STAMP(26);
      lds_w16(scr + (32 + 4 * w) * KB, k0); QST(QS + 10); lds_w16(scr + (33 + 4 * w) * KB, k1); QST(QS + 11);
      lds_w16(scr + (34 + 4 * w) * KB, v0); QST(QS + 12); lds_w16(scr + (35 + 4 * w) * KB, v1);
      lds_sig(cx, 4);
#pragma unroll
      for (int i = QS + 13; i < 28; ++i) QST(i);
      const float rq = rs * C2;
#pragma unroll
      for (int i = 0; i < 16; ++i) aq[i] = (aq[i] - mu * cq[i >> 2][i & 3]) * rq;
      q0 = pack8(aq, 0); q1 = pack8(aq, 1);
      if (h == 1) { q1[2] = 0; q1[3] = NEG31; }
    }
    STAMP(14);
    {
      f32x16 cd, cf;
#pragma unroll
      for (int i = 0; i < 16; ++i) {
        const int kk = (i & 3) + 8 * (i >> 2) + 4 * h;
        cd[i] = kk <= r ? 0.f : -1e30f; cf[i] = kk <= r ? -1e30f : 0.f;
      }
      f32x16 sa = MFMA(k0, q0, cd), sb;
      sa = MFMA(k1, q1, sa);
      set_shift(q1, xhalf_max(max16(sa, sa[0])), h, NEG31);
      const ldsp kp = scr + (8 + w) * 4 * KB;
      auto need = [&](int u) {
        const int ti = 8 + w - u;
        if (u == 1 && w > 0) lds_wait<0>(cx, 4, 4u);
        if (__builtin_expect((ti & 3) == 3, 0)) lds_wait<1>(cx, 5 + (ti >> 2), 2u);
      };
      u32x4 K[2][2], V[2][2], pp;
      f32x16 o = {};
      sa = MFMA(k0, q0, cd); sa = MFMA(k1, q1, sa);
      V[0][0] = v0; V[0][1] = v1;
      need(1);
      STAMP(15);
      K[1][0] = lds_r16(kp - 4 * KB); K[1][1] = lds_r16(kp - 3 * KB); V[1][0] = lds_r16(kp - 2 * KB);
#pragma unroll
      for (int u = 0; u < 9; ++u) {
        const int c = u & 1, n = c ^ 1;
        auto ha = [&]() {
          if (u + 1 < 9) V[n][1] = lds_r16(kp - (u + 1) * 4 * KB + 3 * KB);
          if (u + 2 < 9) { need(u + 2); K[c][0] = lds_r16(kp - (u + 2) * 4 * KB); K[c][1] = lds_r16(kp - (u + 2) * 4 * KB + KB); }
        };
        auto hc = [&]() { if (u + 2 < 9) V[c][0] = lds_r16(kp - (u + 2) * 4 * KB + 2 * KB); if (u >= 4 && u <= 6 && __builtin_expect(u == (w < 2 ? 4 : 3 + w), 0)) post(cx, b, 8 + w, ck0, DT_OB + (8 - u) * DT_UNIT); };
        f32x16& s = c ? sb : sa; f32x16& sn = c ? sa : sb;
        if (u == 4) { STAMP(16); }
        if (u == 0) unit<true, false>(s, sn, Z, o, o, pp, K[n][0], K[n][1], q0, q1, V[c][0], V[n][1], ha, nop, hc);
        else if (u < 7) unit<true, true>(s, sn, Z, o, o, pp, K[n][0], K[n][1], q0, q1, V[c][0], V[n][1], ha, nop, hc);
        else if (u == 7) unit<true, true>(s, sn, cf, o, o, pp, K[n][0], K[n][1], q0, q1, V[c][0], V[n][1], ha, nop, hc);
        else unit<false, true>(s, sn, Z, o, o, pp, K[n][0], K[n][1], q0, q1, V[c][0], V[n][1], ha, nop, hc);
      }
      o = MFMA(V[0][1], pp, o);
      if (__builtin_expect(__builtin_amdgcn_ballot_w64(h == 1 && !(o[12] < LBIG)) != 0ull, 0)) {
        if (h == 1) { q1[2] = 0; q1[3] = NEG31; }
        float m = -INFINITY;
#pragma unroll 1
        for (int u = 0; u < 9; ++u) {
          f32x16 s;
#pragma unroll
          for (int i = 0; i < 16; ++i) s[i] = u == 0 ? cd[i] : u == 8 ? cf[i] : 0.f;
          s = MFMA(lds_r16(kp - u * 4 * KB), q0, s); s = MFMA(lds_r16(kp - u * 4 * KB + KB), q1, s);
          m = max16(s, m);
        }
        set_shift(q1, xhalf_max(m), h, NEG31);
        o = Z;
#pragma unroll 1
        for (int u = 0; u < 9; ++u) {
          f32x16 s;
#pragma unroll
          for (int i = 0; i < 16; ++i) s[i] = u == 0 ? cd[i] : u == 8 ? cf[i] : 0.f;
          s = MFMA(lds_r16(kp - u * 4 * KB), q0, s); s = MFMA(lds_r16(kp - u * 4 * KB + KB), q1, s);
          u32x4 p0, p1;
          exp_pack(s, p0, p1);
          o = MFMA(lds_r16(kp - u * 4 * KB + 2 * KB), p0, o); o = MFMA(lds_r16(kp - u * 4 * KB + 3 * KB), p1, o);
        }
      }
      u32x4 y0, y1;
      norm_pack(o, h, y0, y1);
      const int oo = R_OFB + ((rb * 4 + w) * 16 + hd) * 2 * KB;
      st16<16>(y0, ws, l16, oo); st16<16>(y1, ws, l16 + KB, oo);
      lds_sig(cx, 9);
    }
    STAMP(17);
    u32x4 wa1[WA1];
    {
      u32x4 of[8];
      const int src = R_OFB + (rt * 32 + 8 * w) * KB;
#pragma unroll
      for (int i = 0; i < WA1; ++i) { wa1[i] = ld16<16>(wt, l16 + (i & 3) * KB, pair ? OFF_PAIR : OFF_WAT + ((4 * sub + w) * 28 + (i & ~3)) * KB); SBAR(); }
      poll_wait(cx, (((jj & ~15) | (w << 2) | (lane & 3)) << 3) | xcd, 8 + (rt & 3));
      STAMP(18);
      if (DLY_OB) __builtin_amdgcn_s_sleep(DLY_OB);
      ld_handoff<8>(of, ws, l16, src);
      chk_handoff<8>(of, ws, l16, src, 3);
      STAMP(19);
      lds_wait<1>(cx, 9, 4u);
#pragma unroll
      for (int i = 0; i < 8; ++i) lds_w16(scr + (8 * w + i) * KB, of[i]);
      BAR();
      STAMP(20);
    }
    if (pair == 0) {
      u32x4 wa[28];
#pragma unroll
      for (int i = 0; i < WA1; ++i) wa[i] = wa1[i];
#pragma unroll
      for (int i = WA1; i < 28; ++i) { wa[i] = ld16<16>(wt, l16 + (i & 3) * KB, OFF_WAT + ((4 * sub + w) * 28 + (i & ~3)) * KB); SBAR(); }
      const int tile = 4 * sub + w;
      f32x4 cq[4];
#pragma unroll
      for (int g = 0; g < 4; ++g) cq[g] = lds_rf4(lds + M_CS + w * 128 + (8 * g + 4 * h) * 4);
      const float cs = *(LAS float*)(lds + M_CS + w * 128 + r * 4);
      asm volatile("s_waitcnt vmcnt(0)" ::: "memory");
      BAR();
      STAMP(21);
      const float mu = *(LAS float*)(lds + M_ST + 4 * 256 + r * 4), rs = *(LAS float*)(lds + M_ST + 4 * 256 + 128 + r * 4);
      u32x4 y0, y1;
      if (tile != 15) {
        f32x16 acc = gemm_lds<28, false>(wa, scr + 32 * KB, [](int) {});
        const float sc = tile < 14 ? rs * C2 : rs;
#pragma unroll
        for (int i = 0; i < 16; ++i) acc[i] = (acc[i] - mu * cq[i >> 2][i & 3]) * sc;
        y0 = pack8(acc, 0); y1 = pack8(acc, 1);
        if (tile == 14 && h == 1) { y1[2] = ONES2; y1[3] = 0x00003F80u; }
      } else {
        const ldsp st = lds + M_ST + 4 * 256;
        f32x4 r4[4];
#pragma unroll
        for (int g = 0; g < 4; ++g) { cq[g] = lds_rf4(st + (8 * g + 4 * h) * 4); r4[g] = lds_rf4(st + 128 + (8 * g + 4 * h) * 4); }
        f32x16 acc = gemm_lds<28, true>(wa, scr + 32 * KB, [](int) {});
#pragma unroll
        for (int i = 0; i < 16; ++i) acc[i] = (acc[i] - cq[i >> 2][i & 3] * cs) * r4[i >> 2][i & 3];
        y0 = pack8(acc, 0); y1 = pack8(acc, 1);
        if (r >= 28) { const unsigned f = r == 28 ? ONES2 : 0u; y0 = u32x4{f, f, f, f}; y1 = y0; }
      }
      const int dst = PAIR_SZ + (tile < 14 ? R_QA + (rt * 14 + tile) * 2 * KB : R_KVA + rt * 4 * KB + (tile - 14) * 2 * KB);
      st16<16>(y0, ws, l16, dst); st16<16>(y1, ws, l16 + KB, dst);
      STAMP(25);
      if (tile >= 14) {
        asm volatile("s_waitcnt vmcnt(0)" ::: "memory");
        if (lane < 8) st_tag(cx.flg + PAIR_SZ + (256 + lane * 64 + rt) * 128 + (tile - 13) * 8, tag);
      }
    }
    STAMP(22); STAMP_DUMP();
   }
   return;
  }

  u32x4 x0 = {}, x1 = {};
#pragma unroll 1
 for (int pair = 0; pair < 2; ++pair) {
  PAIR_SETUP();
  __builtin_amdgcn_s_setprio(2);
  const int ct0 = sub < 2 ? 4 * sub : 3 * sub + 2, nct = sub < 2 ? 4 : 3;
  const bool ctok = w < nct;
  const int ct = ct0 + (ctok ? w : 0);
  const int xo1 = R_XF1 + (rt * 28 + ct * 2) * KB, xo2 = R_XF2 + (rt * 28 + ct * 2) * KB;
  const int so1 = R_ST1 + (rt * 14 + ct) * 256, so2 = R_ST2 + (rt * 14 + ct) * 256;
  {
    const int qa = R_QA + rt * 28 * KB;
    if (pair) poll_wait<2>(cx, (((jj & ~3) | (lane & 3)) << 3) | xcd, 3);
    else {
#pragma unroll 1
      for (int spin = 0; spin < (SPIN_CAP << 4); ++spin) {
        const u32x4 v = *(volatile LAS u32x4*)(lds + P_SG);
        if (__builtin_amdgcn_readfirstlane(v[0] + v[1] + v[2] + v[3]) == 8u) break;
        __builtin_amdgcn_s_sleep(2);
      }
      __builtin_amdgcn_s_sleep(Q_DLY);
      const int g0 = 7 * (4 * sub + w), tl = (lane & 3) == 0 ? g0 >> 3 : (lane & 3) == 1 ? (g0 + 6) >> 3 : 14;
#pragma unroll 1
      for (int spin = 0; spin < SPIN_CAP; ++spin) {
        if (__builtin_amdgcn_ballot_w64(ld_tag(p.ws + OFF_P0F + (rt * 16 + tl) * 8) != tag) == 0ull) break;
        __builtin_amdgcn_s_sleep(1);
      }
      CBAR();
    }
    STAMP(16);
    u32x4 q[2], k0, k1;
#pragma unroll 1
    for (int spin = 0; spin < SPIN_CAP; ++spin) {
      CBAR();
#pragma unroll
      for (int s = 0; s < 2; ++s)
#pragma unroll
        for (int jh = 0; jh < 2; ++jh) {
          const int dg = 4 * s + 2 * jh + h, G = 7 * (4 * sub + w) + (dg < 7 ? dg : 6);
          const int g8 = G & 7;
          u32x2 v = ld8<16>(ws, ((G >> 3) * 2 + (g8 >> 2)) * KB + (r + 32 * (g8 & 1)) * 16 + ((g8 >> 1) & 1) * 8, qa);
          if (dg == 7) { v[0] = 0; v[1] = 0; }
          q[s][2 * jh] = v[0]; q[s][2 * jh + 1] = v[1];
        }
      k0 = ld16<16>(ws, l16, R_KVA + rt * 4 * KB); k1 = ld16<16>(ws, l16 + KB, R_KVA + rt * 4 * KB);
      if (pair == 0 || __builtin_amdgcn_ballot_w64(smax(smax(smax(smax(0u, q[0]), q[1]), k0), k1) == SENT) == 0ull) break;
      RETRY(7);
    }
    f32x16 s0 = MFMA(k0, q[0], Z);
    s0 = MFMA(k1, q[1], s0);
    lds_w16(lds + L_QF + l16 + 2 * w * KB, q[0]);
    set_shift(q[1], xhalf_max(max16(s0, s0[0])), h, 0u);
    lds_w16(lds + L_QF + l16 + (2 * w + 1) * KB, q[1]);
    STAMP(17);
    BAR();
  }
  STAMP(1);
  __builtin_amdgcn_s_setprio(HPRIO);
  u32x4 wf[32];
  Clk ck0 = clk_now();
  {
    if (pair == 0) {
#pragma unroll 1
      for (int spin = 0; spin < SPIN_CAP; ++spin) {
        bool ok = true;
#pragma unroll
        for (int i = 0; i < 4; ++i) ok &= ld_tag(p.ws + OFF_INI + (xcd * 256 + i * 64 + lane) * 8) == tag;
        if (__builtin_amdgcn_ballot_w64(!ok) == 0ull) break;
        __builtin_amdgcn_s_sleep(8);
      }
      CBAR();
    }
    STAMP(18);
    const u32x4 sv = {SENT, SENT, SENT, SENT};
#define PC() do { SBAR(); if (PACE) __builtin_amdgcn_s_sleep(PACE); SBAR(); } while (0)
    if (NOH) { for (int i = 0; i < NOH; ++i) __builtin_amdgcn_s_sleep(127); }
    auto fill = [&](int i) {
      const int hi = (i & 1) * KB;
      if (i < 2) st16<16>(sv, ws, l16 + hi, R_OFA + (rt * 32 + sub * 8 + 2 * w) * KB);
      else if (i < 4) { if (ctok) { st16<16>(sv, ws, l16 + hi, xo1); if (i == 2) st4<16>(SENT, ws, lane * 4, so1); } }
      else if (i < 8) st16<16>(sv, ws, l16 + (i - 4) * KB, R_KVB + (hd * 64 + rb * 4 + w) * 4 * KB);
      else if (i < 10) st16<16>(sv, ws, l16 + hi, R_OFB + ((rb * 4 + w) * 16 + hd) * 2 * KB);
      else if (i < 12) { if (ctok && pair == 0) { st16<16>(sv, ws, l16 + hi, xo2); if (i == 10) st4<16>(SENT, ws, lane * 4, so2); } }
      else if (pair == 0) st16<16>(sv, ws, l16 + hi, PAIR_SZ + (4 * sub + w < 14 ? R_QA + (rt * 14 + 4 * sub + w) * 2 * KB : R_KVA + rt * 4 * KB + (4 * sub + w - 14) * 2 * KB));
      SBAR();
    };
    unsigned c4[4] = {};
    if (pair == 0) {
      const unsigned short* xr = p.x + (size_t)(rt * 32 + r) * D_ + ct * 32 + 4 * h;
      const u32x2 a0 = *(const u32x2*)xr, a1 = *(const u32x2*)(xr + 8), a2 = *(const u32x2*)(xr + 16), a3 = *(const u32x2*)(xr + 24);
      x0 = u32x4{a0[0], a0[1], a1[0], a1[1]}; x1 = u32x4{a2[0], a2[1], a3[0], a3[1]};
      const int co = (h ? 16 + hd * 3 + (w < 3 ? w : 0) : 4 * sub + w) * 512 + r * 4;
#pragma unroll
      for (int i = 0; i < 4; ++i) c4[i] = ld4<16>(wt, co + i * 128, OFF_CS);
      if (WARM) glds16_sc1(p.ws + (lane < 28 ? OFF_WOB + lane * 128 : OFF_WAT + (lane < 56 ? lane - 28 : 0) * 128) + (jj * 4 + w) * 3584, (unsigned)(uintptr_t)(lds + M_ST));
      SBAR();
    }
    const unsigned char* wbsrc = p.ws + OFF_WBT + (hd * 84 + w * 21) * KB + l16;
    const unsigned wbdst = (unsigned)(uintptr_t)(lds + L_WB) + w * 21 * KB;
#pragma unroll
    for (int i = 0; i < 32; ++i) {
      wf[i] = ld16<16>(wt, l16 + (i & 3) * KB, OFF_WOA + (ct * 32 + (i & ~3)) * KB); SBAR();
      if (i % 3 != 2 || i > 29) { const int g = i - (i + 1) / 3; if (g < 21 && pair == 0) glds16_sc1(wbsrc + g * KB, wbdst + g * KB); }
      if ((i & 1) == 0 && i < 28) fill(i >> 1);
      if (i == 10) { asm volatile("s_waitcnt vmcnt(5)" ::: "memory"); lds_sig(cx, 8); }
      if (i % PGRP == PGRP - 1) { SBAR(); if (pair) __builtin_amdgcn_s_sleep(PACE3); else __builtin_amdgcn_s_sleep(PACE); SBAR(); }
    }
    if (pair == 0) *(LAS float*)(lds + M_CS + (4 * h + w) * 128 + r * 4) = (__uint_as_float(c4[0]) + __uint_as_float(c4[1])) + (__uint_as_float(c4[2]) + __uint_as_float(c4[3]));
    asm volatile("s_waitcnt vmcnt(0)" ::: "memory");
  }
  STAMP(2);
  {
    u32x4 keep[2];
    const int s = w & 1;
    __builtin_amdgcn_s_setprio(2);
    lds_wait<2>(cx, 8, 4u);
#pragma unroll 1
    for (int spin = 0; spin < SPIN_CAP; ++spin) {
      if (__builtin_amdgcn_readfirstlane(*(volatile LAS unsigned*)(cx.sig + 11 * 16 + w * 4))) break;
      __builtin_amdgcn_s_sleep(DOZE);
    }
#pragma unroll
    for (int it = 0; it < 2; ++it) {
      const int T = 2 * it + (w >> 1);
      if (it == 0) lds_wait<2>(cx, T, 4u); else lds_wait<1>(cx, T, 4u);
      if (it == 0) { if (w == POST_OA) post(cx, b, 0, ck0, DT_OA); ck0 = clk_now(); }

      const ldsp red = scr + T * 16 * KB;
      f32x4 a0[4], a1[4]; float l4[4];
#pragma unroll
      for (int k = 0; k < 4; ++k) {
        l4[k] = *(LAS float*)(lds + L_SCR + T * 16 * KB + k * 4 * KB + 3 * KB + (32 + r) * 16);
        a0[k] = lds_rf4(red + k * 4 * KB + (2 * s) * KB);
        a1[k] = lds_rf4(red + k * 4 * KB + (2 * s + 1) * KB);
      }
      const float l = (l4[0] + l4[1]) + (l4[2] + l4[3]);
      const f32x4 b0 = (a0[0] + a0[1]) + (a0[2] + a0[3]), b1 = (a1[0] + a1[1]) + (a1[2] + a1[3]);
      const float inv = __builtin_amdgcn_rcpf(l);
      u32x4 o = {pk(b0[0] * inv, b0[1] * inv), pk(b0[2] * inv, b0[3] * inv), pk(b1[0] * inv, b1[1] * inv), pk(b1[2] * inv, b1[3] * inv)};
      if (s == 1 && h == 1) { o[2] = 0; o[3] = 0; }
      if (__builtin_expect(__builtin_amdgcn_ballot_w64(!(l < LBIG)) == 0ull, 1)) st16<16>(o, ws, l16, R_OFA + (rt * 32 + sub * 8 + 2 * T + s) * KB);
      else if (lane == 0) __hip_atomic_fetch_or((LAS unsigned*)(cx.sig + 10 * 16), 1u << T, __ATOMIC_RELAXED, __HIP_MEMORY_SCOPE_WORKGROUP);
      keep[it] = o;
      if (it == 0) { STAMP(3); } else { STAMP(4); }
    }
    BAR();
    STAMP(5);
    const unsigned bad = __builtin_amdgcn_readfirstlane(*(LAS unsigned*)(cx.sig + 10 * 16));
#pragma unroll
    for (int it = 0; it < 2; ++it) {
      const int T = 2 * it + (w >> 1);
      if (!((bad >> T) & 1u)) lds_w16(scr + (sub * 8 + 2 * T + s) * KB, keep[it]);
    }
    BAR();
  }
  STAMP(6);
  {
    const f32x16 acc = gemm_lds<32, false>(wf, scr, [&](int i) { if (i == 0 && w == 0) post(cx, b, 1, ck0, DT_X); if (i == 24) BAR();   });
    add_res(acc, x0, x1, x0, x1);
    if (ctok) { st16<16>(x0, ws, l16, xo1); st16<16>(x1, ws, l16 + KB, xo1); SBAR(); st4<16>(__float_as_uint(row_stats(x0, x1, h)), ws, lane * 4, so1); }
  }
  STAMP(7);
  {
    __builtin_amdgcn_s_setprio(0);
    poll_wait<2>(cx, (((jj & ~15) | (w << 2) | (lane & 3)) << 3) | xcd, 1);
    __builtin_amdgcn_s_sleep(ST_DLY);
    stats_gather(ws, R_ST1 + (rb * 4 + w) * 14 * 256, lds + M_ST + w * 256, lane);
    lds_sig(cx, 12 + w);
    const int kt = 4 * rb - 8 + 2 * w;
    u32x4 hv[8];
    STAMP(8);
    if (kt >= 0) {
      const int prb = kt >> 2;
      __builtin_amdgcn_s_setprio(0);
      poll_wait<4>(cx, (((prb & 1) * 16 + hd) << 3) | (prb >> 1), 4 + (kt & 3) + (lane & 1));
      __builtin_amdgcn_s_setprio(2);
      STAMP(9);
      if (DLY_KV) __builtin_amdgcn_s_sleep(DLY_KV);
      ld_handoff<8>(hv, ws, l16, R_KVB + (hd * 64 + kt) * 4 * KB);
      chk_handoff<8>(hv, ws, l16, R_KVB + (hd * 64 + kt) * 4 * KB, 2);
    } else {
#pragma unroll
      for (int i = 0; i < 8; ++i) hv[i] = u32x4{0u, 0u, 0u, ((i & 3) == 1 && h == 1) ? 0x3F800000u : 0u};
    }
#pragma unroll
    for (int i = 0; i < 8; ++i) lds_w16(scr + (8 * w + i) * KB, hv[i]);
    lds_sig(cx, 5 + (w >> 1));
    STAMP(10);
    __builtin_amdgcn_s_setprio(0);
#pragma unroll
    for (int i = 0; i < 32; ++i) { wf[i] = ld16<16>(wt, l16 + (i & 3) * KB, OFF_WOB + (ct * 32 + (i & ~3)) * KB); SBAR(); if (PACE2) __builtin_amdgcn_s_sleep(PACE2); SBAR(); }
    asm volatile("s_waitcnt vmcnt(0)" ::: "memory");
    BAR();
  }
  STAMP(11);
  __builtin_amdgcn_s_setprio(2);
  {
    const f32x16 acc = gemm_lds<32, false>(wf, scr, [&](int i) { if (i == 0 && w == 0 && pair == 0) post(cx, b, 2, ck0, DT_X); });
    add_res(acc, x0, x1, x0, x1);
    if (pair == 1) {
      if (ctok) {
        unsigned short* orow = p.out + (size_t)(rt * 32 + r) * D_ + ct * 32 + 4 * h;
        *(u32x2*)orow = u32x2{x0[0], x0[1]}; *(u32x2*)(orow + 8) = u32x2{x0[2], x0[3]};
        *(u32x2*)(orow + 16) = u32x2{x1[0], x1[1]}; *(u32x2*)(orow + 24) = u32x2{x1[2], x1[3]};
      }
      if (b == 0 && w == 0 && lane == 0) st_tag(p.ws + OFF_EPO, tag);
      STAMP(12); STAMP_DUMP(); SPAN_T(1);
      return;
    }
    if (ctok) { st16<16>(x0, ws, l16, xo2); st16<16>(x1, ws, l16 + KB, xo2); SBAR(); st4<16>(__float_as_uint(row_stats(x0, x1, h)), ws, lane * 4, so2); }
  }
  STAMP(12);
  {
    u32x4 xf[7];
    const int src = R_XF2 + (rt * 28 + 7 * w) * KB;
    poll_wait(cx, (((jj & ~3) | (lane & 3)) << 3) | xcd, 2);
    STAMP(13);
    if (DLY_X2) __builtin_amdgcn_s_sleep(DLY_X2);
    ld_handoff<7>(xf, ws, l16, src);
    if (w == 3) stats_gather(ws, R_ST2 + rt * 14 * 256, lds + M_ST + 4 * 256, lane);
    chk_handoff<7>(xf, ws, l16, src, 4);
    STAMP(14);
#pragma unroll
    for (int i = 0; i < 7; ++i) lds_w16(scr + (32 + 7 * w + i) * KB, xf[i]);
    BAR();
    if (w < 2) {
      const unsigned t = eta(ck0, w ? DT_Q + DT_V : DT_Q);
      if (lane == 0) __hip_atomic_store((GAS unsigned*)(cx.flg + PAIR_SZ + b * 128 + (w ? 12 : 3) * 4), t, __ATOMIC_RELAXED, __HIP_MEMORY_SCOPE_AGENT);
    }
  }
  STAMP(15); STAMP_DUMP();
 }
}

extern "C" void kernel_launch(void* const* d_in, const int* in_sizes, int n_in, void* d_out, int out_size, void* d_ws, size_t ws_size,
                              hipStream_t stream) {
  Params p;
  p.x = (const unsigned short*)d_in[0];
  p.wq = (const unsigned short*)d_in[1];
  p.wk = (const unsigned short*)d_in[2];
  p.wv = (const unsigned short*)d_in[3];
  p.woa = (const unsigned short*)d_in[4];
  p.wqkv = (const unsigned short*)d_in[6];
  p.wob = (const unsigned short*)d_in[7];
  p.out = (unsigned short*)d_out;
  p.ws = (unsigned char*)d_ws;
  k_main<<<256, 512, 0, stream>>>(p);
}
```

```cpp
#include <hip/hip_runtime.h>
#include <hip/hip_bf16.h>
#include <stdint.h>

typedef short bf16x8 __attribute__((ext_vector_type(8)));
typedef short s16x4 __attribute__((ext_vector_type(4)));
typedef float f32x16 __attribute__((ext_vector_type(16)));
typedef float f32x4 __attribute__((ext_vector_type(4)));
typedef float f32x2 __attribute__((ext_vector_type(2)));
typedef unsigned u32x4 __attribute__((ext_vector_type(4)));
typedef unsigned u32x2 __attribute__((ext_vector_type(2)));
typedef __bf16 bf16x2_t __attribute__((ext_vector_type(2)));
#define DI __device__ __forceinline__
#define LAS __attribute__((address_space(3)))
#define GAS __attribute__((address_space(1)))
typedef LAS char* ldsp;

#define MFMA(a, b, c) __builtin_amdgcn_mfma_f32_32x32x16_bf16(__builtin_bit_cast(bf16x8, (a)), __builtin_bit_cast(bf16x8, (b)), (c), 0, 0, 0)
#define BAR() asm volatile("s_waitcnt lgkmcnt(0)\n\ts_barrier" ::: "memory")
#define CBAR() asm volatile("" ::: "memory")
#define SBAR() __builtin_amdgcn_sched_barrier(0)

constexpr int S_ = 2048, D_ = 448;
constexpr float C2 = 0.18898223650461362f * 1.4426950408889634f;
constexpr float EPS = 1e-5f;
constexpr unsigned SENT = 0xFFFFFFFFu;
constexpr unsigned ONES2 = 0x3F803F80u;
constexpr int SPIN_CAP = 1 << 14;

constexpr int KB = 1024;
constexpr int OFF_WAT = 0;
constexpr int OFF_WBT = OFF_WAT + 448 * KB;
constexpr int OFF_WKV = OFF_WBT + 1344 * KB;
constexpr int OFF_WOA = OFF_WKV + 56 * KB;
constexpr int OFF_WOB = OFF_WOA + 448 * KB;
constexpr int OFF_CS = OFF_WOB + 448 * KB;
constexpr int OFF_EPO = OFF_CS + 132 * KB;
constexpr int OFF_INI = OFF_EPO + KB;
constexpr int OFF_PAIR = OFF_INI + 16 * KB;
constexpr int QA_SZ = 64 * 28 * KB;
constexpr int R_KVA = 0;
constexpr int R_OFA = R_KVA + 256 * KB;
constexpr int R_XF1 = R_OFA + 2048 * KB;
constexpr int R_KVB = R_XF1 + QA_SZ;
constexpr int R_OFB = R_KVB + 4096 * KB;
constexpr int R_XF2 = R_OFB + 2048 * KB;
constexpr int R_ST1 = R_XF2 + QA_SZ;
constexpr int R_ST2 = R_ST1 + 64 * 14 * 256;
constexpr int R_FLG = R_ST2 + 64 * 14 * 256;
constexpr int R_FKV = R_FLG + 32 * KB;
constexpr int PAIR_SZ = R_FKV + 64 * KB;
constexpr int OFF_DBG = OFF_PAIR + 2 * PAIR_SZ;
constexpr int WS_BYTES = OFF_DBG + 2048 * KB;

#define NOSIDE 0
#define NOKV 0
#define HPRIO 0
#define NOH 0
#define SIG_X1 14
#define WOB1 0
#define DT_OA 2200
#define DT_X 1850
#define DT_KV 3700
#define DT_OB 1000
#define DT_UNIT 240
#define X1_AHEAD 8
#define WA1 8
#define PRE_AUX 16
#define PRO_DLY 2
#define WARM 0
#define KV_DLY 6
#define Q_DLY 4
#define PACE3 8
#define DT_Q 3000
#define ETA_AT 26
#define RSLP 0
#define QS 14
#define ST_DLY 20
#define CODE_KB 44
#define DT_V 300
#define NOFILL 0
#define NOWB 0
#define NOWO 0
#define PGRP 2
#define DOZE 14
#define NFILL0 6
#define PACEF 2
#define DLY_OA 0
#define DLY_X1 0
#define DLY_KV 0
#define DLY_OB 0
#define DLY_X2 0
#define DLY_WOB 24
#define PACE 3
#define PACE2 0
#define POST_OA 2
#define POST_OB 6
#define POST_KV 12
#define POST_GEMM 8

DI unsigned pk(float a, float b) { f32x2 v = {a, b}; bf16x2_t r = __builtin_convertvector(v, bf16x2_t); return __builtin_bit_cast(unsigned, r); }
DI float dot2(unsigned a, unsigned b, float c) { return __builtin_amdgcn_fdot2_f32_bf16(__builtin_bit_cast(bf16x2_t, a), __builtin_bit_cast(bf16x2_t, b), c, false); }
DI float bflo(unsigned u) { return __uint_as_float(u << 16); }
DI float bfhi(unsigned u) { return __uint_as_float(u & 0xFFFF0000u); }
DI float xhalf_add(float v) { auto r = __builtin_amdgcn_permlane32_swap(__float_as_uint(v), __float_as_uint(v), false, false); return __uint_as_float(r[0]) + __uint_as_float(r[1]); }
DI float xhalf_max(float v) { auto r = __builtin_amdgcn_permlane32_swap(__float_as_uint(v), __float_as_uint(v), false, false); return __builtin_fmaxf(__uint_as_float(r[0]), __uint_as_float(r[1])); }
DI float from_hi(float v) { auto r = __builtin_amdgcn_permlane32_swap(__float_as_uint(v), __float_as_uint(v), false, false); return __uint_as_float(r[1]); }
DI float max3f(float a, float b, float c) { return __builtin_fmaxf(__builtin_fmaxf(a, b), c); }
DI float max16(const f32x16& s, float m) {
#pragma unroll
  for (int i = 0; i < 16; i += 2) m = max3f(m, s[i], s[i + 1]);
  return m;
}
typedef __amdgpu_buffer_rsrc_t rsrc_t;
DI rsrc_t mkrs(const void* p, unsigned n) { return __builtin_amdgcn_make_buffer_rsrc((void*)p, 0, n, 0x00020000); }
template <int AUX> DI u32x4 ld16(rsrc_t rs, int voff, int soff) { return __builtin_amdgcn_raw_buffer_load_b128(rs, voff, soff, AUX); }
template <int AUX> DI u32x2 ld8(rsrc_t rs, int voff, int soff) { return __builtin_amdgcn_raw_buffer_load_b64(rs, voff, soff, AUX); }
template <int AUX> DI void st16(u32x4 v, rsrc_t rs, int voff, int soff) { __builtin_amdgcn_raw_buffer_store_b128(v, rs, voff, soff, AUX); }
template <int AUX> DI void st8(u32x2 v, rsrc_t rs, int voff, int soff) { __builtin_amdgcn_raw_buffer_store_b64(v, rs, voff, soff, AUX); }
template <int AUX> DI unsigned ld4(rsrc_t rs, int voff, int soff) { return __builtin_amdgcn_raw_buffer_load_b32(rs, voff, soff, AUX); }
template <int AUX> DI void st4(unsigned v, rsrc_t rs, int voff, int soff) { __builtin_amdgcn_raw_buffer_store_b32(v, rs, voff, soff, AUX); }
DI ldsp opaque(ldsp p) { unsigned a = (unsigned)(uintptr_t)p; asm volatile("" : "+v"(a)); return (ldsp)(uintptr_t)a; }
typedef unsigned long long u64;
DI u64 ld_tag(const unsigned char* a) { return __hip_atomic_load((GAS u64*)a, __ATOMIC_RELAXED, __HIP_MEMORY_SCOPE_AGENT); }
DI void st_tag(unsigned char* a, u64 v) { __hip_atomic_store((GAS u64*)a, v, __ATOMIC_RELAXED, __HIP_MEMORY_SCOPE_AGENT); }
DI u32x4 lds_r16(ldsp p) { return *(LAS u32x4*)p; }
DI f32x4 lds_rf4(ldsp p) { return *(LAS f32x4*)p; }
DI void lds_w16(ldsp p, u32x4 v) { *(LAS u32x4*)p = v; }
DI void lds_wf4(ldsp p, f32x4 v) { *(LAS f32x4*)p = v; }
DI unsigned umax3(unsigned a, unsigned b, unsigned c) { return max(max(a, b), c); }
DI unsigned smax(unsigned m, u32x4 v) { return umax3(umax3(m, v[0], v[1]), v[2], v[3]); }
DI void glds16_sc1(const void* g, unsigned lds_base) {
  unsigned sv;
  asm volatile("s_mov_b32 %0, m0\n\ts_mov_b32 m0, %2\n\ts_nop 0\n\tglobal_load_lds_dwordx4 %1, off sc1\n\ts_mov_b32 m0, %0" : "=&s"(sv) : "v"(g), "s"(lds_base) : "memory");
}
DI void glds16(const void* g, unsigned lds_base) {
  unsigned sv;
  asm volatile("s_mov_b32 %0, m0\n\ts_mov_b32 m0, %2\n\ts_nop 0\n\tglobal_load_lds_dwordx4 %1, off\n\ts_mov_b32 m0, %0" : "=&s"(sv) : "v"(g), "s"(lds_base) : "memory");
}
DI u32x4 pack8(const f32x16& a, int s) {
  u32x4 r;
#pragma unroll
  for (int i = 0; i < 4; ++i) r[i] = pk(a[8 * s + 2 * i], a[8 * s + 2 * i + 1]);
  return r;
}
#define RETRY(tag) do { } while (0)
template <int N> DI void ld_handoff(u32x4 (&v)[N], rsrc_t rs, int voff, int soff) {
#pragma unroll
  for (int i = 0; i < N; ++i) { v[i] = ld16<16>(rs, voff + (i & 3) * KB, soff + (i >> 2) * 4 * KB); SBAR(); }
}
template <int N> DI bool any_sent(const u32x4 (&v)[N]) {
  unsigned m = 0;
#pragma unroll
  for (int i = 0; i < N; ++i) m = smax(m, v[i]);
  return __builtin_amdgcn_ballot_w64(m == SENT) != 0ull;
}
template <int N> DI void chk_handoff(u32x4 (&v)[N], rsrc_t rs, int voff, int soff, int tag) {
  if (__builtin_expect(any_sent<N>(v), 0)) {
#pragma unroll 1
    for (int spin = 0; spin < SPIN_CAP; ++spin) {
      CBAR();
      RETRY(tag);
      if (RSLP) __builtin_amdgcn_s_sleep(RSLP);
#pragma unroll
      for (int i = 0; i < N; ++i)
        if (__builtin_amdgcn_ballot_w64(smax(0u, v[i]) == SENT) != 0ull) v[i] = ld16<16>(rs, voff + (i & 3) * KB, soff + (i >> 2) * 4 * KB);
      if (!any_sent<N>(v)) break;
    }
  }
}

#define STAMP_DECL
#define STAMP_DUMP() do { } while (0)
#define STAMP(i) do { } while (0)

#define SPAN_T(i) do { } while (0)
struct Params {
  const unsigned short *x, *wq, *wk, *wv, *woa, *wqkv, *wob;
  unsigned short* out;
  unsigned char* ws;
};

constexpr int L_SCR = 0;
constexpr int L_WB = 64 * KB;
constexpr int L_QF = 148 * KB;
constexpr int L_MISC = L_QF + 8 * KB;
constexpr int M_SIG = L_MISC;
constexpr int M_ST = M_SIG + 768;
constexpr int M_CS = M_ST + 1280;
constexpr int L_BYTES = M_CS + 1024;
constexpr int XI_KS = 1056;
constexpr int P_KV = 0, P_KVW = 112 * 56, P_XI = 25600, P_WQ = P_XI + 28 * XI_KS;
static_assert(L_BYTES <= 160 * KB && P_WQ + 448 * 224 + 64 <= L_MISC, "LDS");

DI void exp_pack(const f32x16& s, u32x4& p0, u32x4& p1) {
#pragma unroll
  for (int i = 0; i < 4; ++i) p0[i] = pk(__builtin_amdgcn_exp2f(s[2 * i]), __builtin_amdgcn_exp2f(s[2 * i + 1]));
#pragma unroll
  for (int i = 0; i < 4; ++i) p1[i] = pk(__builtin_amdgcn_exp2f(s[8 + 2 * i]), __builtin_amdgcn_exp2f(s[8 + 2 * i + 1]));
}
DI void set_shift(u32x4& q1, float m, int h, unsigned hi31) {
  const unsigned a = pk(m, m); const float r1 = m - bflo(a);
  const unsigned c = pk(r1, r1); const float r2 = r1 - bflo(c);
  const unsigned e = pk(r2, 0.f);
  const unsigned d2 = ((a & 0xFFFFu) | (c << 16)) ^ 0x80008000u, d3 = (e ^ 0x00008000u) | hi31;
  q1[2] = h ? d2 : q1[2]; q1[3] = h ? d3 : q1[3];
}
#define PIN(x) asm volatile("" : "+v"(x))
template <bool DOQK, bool DOPVP, class FA, class FB, class FC>
DI void unit(const f32x16& s, f32x16& sn, const f32x16& cn, f32x16& o, f32x16& op, u32x4& pp, const u32x4& kn0, const u32x4& kn1, const u32x4& qn0,
             const u32x4& qn1, const u32x4& v0, const u32x4& v1p, FA ha, FB hb, FC hc) {
  float e[16]; u32x4 p0;
#define E_(i) e[i] = __builtin_amdgcn_exp2f(s[i]); PIN(e[i]); SBAR()
#define C0_(i) p0[i] = pk(e[2 * i], e[2 * i + 1]); PIN(p0[i]); SBAR()
#define C1_(i) pp[i] = pk(e[8 + 2 * i], e[8 + 2 * i + 1]); PIN(pp[i]); SBAR()
  SBAR();
  if (DOQK) { sn = MFMA(kn0, qn0, cn); SBAR(); }
  E_(0); E_(1); E_(2); E_(3);
  if (DOPVP) { op = MFMA(v1p, pp, op); SBAR(); }
  ha(); SBAR();
  E_(4); C0_(0); E_(5); C0_(1); E_(6); E_(7);
  if (DOQK) { sn = MFMA(kn1, qn1, sn); SBAR(); }
  hb(); SBAR();
  E_(8); C0_(2); E_(9); C0_(3); E_(10); E_(11);
  o = MFMA(v0, p0, o); SBAR();
  hc(); SBAR();
  E_(12); C1_(0); E_(13); C1_(1); E_(14); E_(15); C1_(2); C1_(3);
}

struct Ctx { unsigned char* flg; ldsp sig; int lane; int w; };
DI unsigned now32() { return (unsigned)__builtin_amdgcn_s_memrealtime(); }
struct Clk { unsigned c, r; };
DI Clk clk_now() { Clk k; k.c = (unsigned)__builtin_amdgcn_s_memtime(); k.r = now32(); return k; }
DI unsigned eta(const Clk& k0, int dcyc) {
  const Clk k = clk_now();
  return (k.r + (unsigned)((float)dcyc * (float)(k.r - k0.r) * __builtin_amdgcn_rcpf((float)(k.c - k0.c)))) | 1u;
}
DI void post(const Ctx& c, int b, int slot, const Clk& k0, int dcyc) {
  const unsigned t = eta(k0, dcyc);
  if (c.lane == 0) __hip_atomic_store((GAS unsigned*)(c.flg + b * 128 + slot * 4), t, __ATOMIC_RELAXED, __HIP_MEMORY_SCOPE_AGENT);
}
DI unsigned flag_ld(const Ctx& c, int pb, int slot) { return __hip_atomic_load((GAS unsigned*)(c.flg + pb * 128 + slot * 4), __ATOMIC_RELAXED, __HIP_MEMORY_SCOPE_AGENT); }
template <int SLP = 1> DI void poll_wait(const Ctx& c, int pb, int slot, unsigned f = 0u) {
#pragma unroll 1
  for (int spin = 0; spin < SPIN_CAP; ++spin) {
    if (__builtin_amdgcn_ballot_w64(f == 0u) == 0ull) break;
    if (spin) __builtin_amdgcn_s_sleep(SLP);
    f = __hip_atomic_load((GAS unsigned*)(c.flg + pb * 128 + slot * 4), __ATOMIC_RELAXED, __HIP_MEMORY_SCOPE_AGENT);
  }
#pragma unroll 1
  for (int spin = 0; spin < 256; ++spin) {
    if (__builtin_amdgcn_ballot_w64((int)(f - now32()) > 0) == 0ull) break;
    __builtin_amdgcn_s_sleep(1);
  }
  CBAR();
}
DI void lds_sig(const Ctx& c, int idx) {
  CBAR();
  *(volatile LAS unsigned*)(c.sig + idx * 16 + c.w * 4) = 1u;
  CBAR();
}
template <int SLP> DI void lds_wait(const Ctx& c, int idx, unsigned n) {
#pragma unroll 1
  for (int spin = 0; spin < (SPIN_CAP << 6); ++spin) {
    const u32x4 v = *(volatile LAS u32x4*)(c.sig + idx * 16);
    if (__builtin_amdgcn_readfirstlane(v[0] + v[1] + v[2] + v[3]) >= n) break;
    if (SLP) __builtin_amdgcn_s_sleep(SLP);
  }
  CBAR();
}
template <int N, bool SWAP, class F> DI f32x16 gemm_lds(const u32x4 (&wr)[N], ldsp bb, F mid) {
  f32x16 acc = {};
  u32x4 bf[8];
#pragma unroll
  for (int i = 0; i < 8; ++i) bf[i] = lds_r16(bb + i * KB);
  SBAR();
#pragma unroll
  for (int i = 0; i < N; ++i) {
    acc = SWAP ? MFMA(bf[i & 7], wr[i], acc) : MFMA(wr[i], bf[i & 7], acc);
    SBAR();
    if (i + 8 < N) bf[i & 7] = lds_r16(bb + (i + 8) * KB);
    mid(i);
    SBAR();
  }
  return acc;
}
DI void add_res(const f32x16& a, const u32x4& x0, const u32x4& x1, u32x4& y0, u32x4& y1) {
#pragma unroll
  for (int i = 0; i < 4; ++i) {
    y0[i] = pk(a[2 * i] + bflo(x0[i]), a[2 * i + 1] + bfhi(x0[i]));
    y1[i] = pk(a[8 + 2 * i] + bflo(x1[i]), a[8 + 2 * i + 1] + bfhi(x1[i]));
  }
}
DI float row_stats(const u32x4& y0, const u32x4& y1, int h) {
  float s0 = 0.f, s1 = 0.f, q0 = 0.f, q1 = 0.f;
#pragma unroll
  for (int i = 0; i < 4; ++i) { s0 = dot2(y0[i], ONES2, s0); s1 = dot2(y1[i], ONES2, s1); q0 = dot2(y0[i], y0[i], q0); q1 = dot2(y1[i], y1[i], q1); }
  const float s = xhalf_add(s0 + s1), q = xhalf_add(q0 + q1);
  return h ? q : s;
}
DI void stats_finish(float v, float& mu, float& rs) {
  auto t = __builtin_amdgcn_permlane32_swap(__float_as_uint(v), __float_as_uint(v), false, false);
  mu = __uint_as_float(t[0]) * (1.f / D_);
  rs = __builtin_amdgcn_rsqf(__builtin_fmaxf(__uint_as_float(t[1]) * (1.f / D_) - mu * mu, 0.f) + EPS);
}
DI void stats_gather(rsrc_t ws, int so, ldsp dst, int lane) {
#pragma unroll 1
  for (int spin = 0; spin < SPIN_CAP; ++spin) {
    CBAR();
    unsigned sv[14], mx = 0;
#pragma unroll
    for (int i = 0; i < 14; ++i) sv[i] = ld4<16>(ws, lane * 4 + i * 256, so);
    float t0 = 0.f, t1 = 0.f, mu, rs;
#pragma unroll
    for (int i = 0; i < 14; i += 2) { t0 += __uint_as_float(sv[i]); t1 += __uint_as_float(sv[i + 1]); mx = umax3(mx, sv[i], sv[i + 1]); }
    stats_finish(t0 + t1, mu, rs);
    *(LAS float*)(dst + lane * 4) = lane >= 32 ? rs : mu;
    if (__builtin_amdgcn_ballot_w64(mx == SENT) == 0ull) break;
    RETRY(5);
  }
}
DI void norm_pack(const f32x16& o, int h, u32x4& y0, u32x4& y1) {
  const float inv = __builtin_amdgcn_rcpf(from_hi(o[12]));
#pragma unroll
  for (int i = 0; i < 4; ++i) { y0[i] = pk(o[2 * i] * inv, o[2 * i + 1] * inv); y1[i] = pk(o[8 + 2 * i] * inv, o[8 + 2 * i + 1] * inv); }
  if (h == 1) { y1[2] = 0; y1[3] = 0; }
}
constexpr float LBIG = 1e30f;
constexpr unsigned NEG31 = 0xF1490000u;

DI u32x4 tr_frag(ldsp a, int hi) {
  const s16x4 lo_ = __builtin_amdgcn_ds_read_tr16_b64_v4i16((LAS s16x4*)a), hi_ = __builtin_amdgcn_ds_read_tr16_b64_v4i16((LAS s16x4*)(a + hi));
  return __builtin_bit_cast(u32x4, __builtin_shufflevector(lo_, hi_, 0, 1, 2, 3, 4, 5, 6, 7));
}
DI void part_write(ldsp d, const f32x16& a) {
#pragma unroll
  for (int g = 0; g < 4; ++g) lds_wf4(d + g * KB, f32x4{a[4 * g], a[4 * g + 1], a[4 * g + 2], a[4 * g + 3]});
}
template <bool LN, class F>
DI void kv_finish(ldsp part, int pstr, int isV, int s, ldsp st, ldsp cs, int lane, rsrc_t ws, int kvo, unsigned char* fkv, u64 tag, F hook) {
  const int r = lane & 31, h = lane >> 5;
  f32x4 a[2][4], c[2] = {}, m[2] = {};
  float l0 = 0.f, l1 = 0.f;
#pragma unroll
  for (int k = 0; k < 4; ++k) { a[0][k] = lds_rf4(part + k * pstr + 2 * s * KB + lane * 16); a[1][k] = lds_rf4(part + k * pstr + (2 * s + 1) * KB + lane * 16); }
  if (LN) {
    l0 = *(LAS float*)((isV ? cs : st) + r * 4); l1 = *(LAS float*)(st + 128 + r * 4);
#pragma unroll
    for (int g = 0; g < 2; ++g) { c[g] = lds_rf4((isV ? st : cs) + (16 * s + 8 * g + 4 * h) * 4); m[g] = lds_rf4(st + 128 + (16 * s + 8 * g + 4 * h) * 4); }
  }
  SBAR();
  float v[8];
#pragma unroll
  for (int i = 0; i < 8; ++i) v[i] = (a[i >> 2][0][i & 3] + a[i >> 2][1][i & 3]) + (a[i >> 2][2][i & 3] + a[i >> 2][3][i & 3]);
  if (LN) {
#pragma unroll
    for (int i = 0; i < 8; ++i) v[i] = (v[i] - l0 * c[i >> 2][i & 3]) * (isV ? m[i >> 2][i & 3] : l1);
  }
  u32x4 o = {pk(v[0], v[1]), pk(v[2], v[3]), pk(v[4], v[5]), pk(v[6], v[7])};
  if (isV) { if (r >= 28) { const unsigned f = r == 28 ? ONES2 : 0u; o = u32x4{f, f, f, f}; } }
  else if (s == 1 && h == 1) { o[2] = ONES2; o[3] = 0x00003F80u; }
  st16<16>(o, ws, lane * 16, kvo + (2 * isV + s) * KB);
  hook();
  asm volatile("s_waitcnt vmcnt(0)" ::: "memory");
  if (lane < 8) st_tag(fkv + lane * 64 * 128 + (2 * isV + s) * 8, tag);
}

DI void phase0_h(const Params& p, const ldsp lds, u64 epo, u64& tag) {
  const int tid = threadIdx.x & 255, lane = tid & 63, r = lane & 31, h = lane >> 5;
  const int w = __builtin_amdgcn_readfirstlane(tid >> 6);
  const int b = blockIdx.x, xcd = b & 7, jj = b >> 3, rt = 8 * xcd + (jj >> 2), sub = jj & 3;
  const rsrc_t ws = mkrs(p.ws, WS_BYTES);
  const Ctx cx = {p.ws, lds + M_SIG + 512, lane, w};
  const int pair = -1; (void)pair;
  STAMP_DECL; STAMP(0);
  const ldsp priv = lds + P_KV + w * P_KVW;
  if (sub < 2) {
    const unsigned char* src = (const unsigned char*)(sub ? p.wv : p.wk) + w * P_KVW + lane * 16;
#pragma unroll
    for (int i = 0; i < 7; ++i) if (i < 6 || lane < 8) glds16(src + i * KB, (unsigned)(uintptr_t)priv + i * KB);
  }
  const int q8 = tid & 7, row8 = tid >> 3;
  u32x4 xv[7];
  {
    const unsigned short* xr = p.x + (size_t)(rt * 32 + row8) * D_ + q8 * 8;
#pragma unroll
    for (int i = 0; i < 7; ++i) xv[i] = *(const u32x4*)(xr + i * 64);
  }
  SBAR();
  const int tt = b >> 2, tks = (b & 3) * 7 + 2 * w, wog = b * 4 + w;
  u32x2 tv[4];
  unsigned te[8];
  const bool dead = (lane & 7) == 7;
  {
    const int pitch = tt < 16 ? 448 : 1344;
    const unsigned short* src = (tt < 16 ? p.wq + 28 * tt : p.wqkv + ((tt - 16) % 3) * 448 + ((tt - 16) / 3) * 28) + (size_t)(tks * 16 + (lane >> 3)) * pitch + (dead ? 0 : (lane & 7) * 4);
#pragma unroll
    for (int i = 0; i < 4; ++i) tv[i] = *(const u32x2*)(src + (size_t)((w == 3 && i >= 2) ? 0 : i * 8) * pitch);
    int g = wog < 896 ? wog : 0;
    const unsigned short* wo = g >= 448 ? p.wob : p.woa;
    if (g >= 448) g -= 448;
    const int ct = g >> 5, ks = g & 31, head = ks >> 1;
    const int k0 = head * 28 + 16 * (ks & 1) + 4 * h, kmax = head * 28 + 28;
#pragma unroll
    for (int j = 0; j < 8; ++j) {
      const int k = k0 + 8 * (j >> 2) + (j & 3);
      te[j] = wo[(k < kmax ? k : 0) * 448 + 32 * ct + r];
    }
    st4<16>(0u, ws, w == 0 ? h * PAIR_SZ + r * 4 : WS_BYTES, OFF_PAIR + R_FLG + b * 128);
  }
  SBAR();
  {
    float sm[2] = {}, sq[2] = {};
#pragma unroll
    for (int i = 0; i < 7; ++i)
#pragma unroll
      for (int d = 0; d < 4; ++d) { sm[d & 1] = dot2(xv[i][d], ONES2, sm[d & 1]); sq[d & 1] = dot2(xv[i][d], xv[i][d], sq[d & 1]); }
    float a = sm[0] + sm[1], c = sq[0] + sq[1];
#define DPP_ADD(v, ctl) v += __uint_as_float(__builtin_amdgcn_update_dpp(0u, __float_as_uint(v), ctl, 0xF, 0xF, true))
    DPP_ADD(a, 0xB1); DPP_ADD(c, 0xB1); DPP_ADD(a, 0x4E); DPP_ADD(c, 0x4E); DPP_ADD(a, 0x141); DPP_ADD(c, 0x141);
    const float mu = a * (1.f / D_), rs = __builtin_amdgcn_rsqf(__builtin_fmaxf(c * (1.f / D_) - mu * mu, 0.f) + EPS), nb = -mu * rs;
#pragma unroll
    for (int i = 0; i < 7; ++i) {
      unsigned y[4];
#pragma unroll
      for (int d = 0; d < 4; ++d) y[d] = pk(__builtin_fmaf(bflo(xv[i][d]), rs, nb), __builtin_fmaf(bfhi(xv[i][d]), rs, nb));
      const ldsp dst = lds + P_XI + (4 * i + (q8 >> 1)) * XI_KS + row8 * 16 + (q8 & 1) * 8;
      *(LAS u32x2*)dst = u32x2{y[0], y[1]};
      *(LAS u32x2*)(dst + 512) = u32x2{y[2], y[3]};
    }
  }
  lds_sig(cx, 0);
  tag = (((u64)__builtin_amdgcn_readfirstlane((unsigned)(epo >> 32)) << 32) | __builtin_amdgcn_readfirstlane((unsigned)epo)) + 1ull;
  const int i16 = lane & 15, trow = 4 * h + (i16 >> 2), tcol = ((lane >> 4) & 1) * 32 + (i16 & 3) * 8;
  STAMP(1);
  if (sub < 2) {
    u32x4 wf[7];
#pragma unroll
    for (int i = 0; i < 7; ++i) wf[i] = tr_frag(priv + (16 * i + trow) * 56 + tcol, 8 * 56);
    lds_wait<0>(cx, 0, 4u);
    f32x16 acc = {};
#pragma unroll
    for (int i = 0; i < 7; ++i) { const u32x4 xf = lds_r16(lds + P_XI + (7 * w + i) * XI_KS + lane * 16); acc = sub ? MFMA(xf, wf[i], acc) : MFMA(wf[i], xf, acc); }
    part_write(priv + lane * 16, acc);
    lds_sig(cx, 1);
    if (w < 2) {
      lds_wait<0>(cx, 1, 4u);
      kv_finish<false>(lds + P_KV, P_KVW, sub, w, (ldsp)0, (ldsp)0, lane, ws, OFF_PAIR + R_KVA + rt * 4 * KB, p.ws + OFF_PAIR + R_FKV + rt * 128, tag, [&]() { STAMP(4); });
    }
    if (rt == 0) {
      float cs = 0.f;
#pragma unroll
      for (int i = 0; i < 7; ++i) {
        st16<16>(wf[i], ws, lane * 16, OFF_WKV + (sub * 28 + 7 * w + i) * KB);
#pragma unroll
        for (int d = 0; d < 4; ++d) cs = dot2(wf[i][d], ONES2, cs);
      }
      cs = xhalf_add(cs);
      st4<16>(h ? 0u : __float_as_uint(cs), ws, lane * 4, OFF_CS + ((64 + sub) * 16 + 4 * w) * 128);
      st4<16>(0u, ws, lane * 4 + 256, OFF_CS + ((64 + sub) * 16 + 4 * w) * 128);
    }
  }
  STAMP(2);
  {
    const ldsp img = priv + 4 * KB;
#pragma unroll
    for (int i = 0; i < 4; ++i) *(LAS u32x2*)(img + i * 512 + lane * 8) = dead ? u32x2{0u, 0u} : tv[i];
    CBAR();
    float cs = 0.f;
#pragma unroll
    for (int ks = 0; ks < 2; ++ks) {
      const u32x4 o = tr_frag(img + ks * KB + trow * 64 + tcol, 512);
      if (ks == 0 || w < 3) {
        st16<16>(o, ws, lane * 16, (tt * 28 + tks + ks) * KB);
#pragma unroll
        for (int i = 0; i < 4; ++i) cs = dot2(o[i], ONES2, cs);
      }
    }
    u32x4 o;
#pragma unroll
    for (int i = 0; i < 4; ++i) { asm volatile("" : "+v"(te[2 * i]), "+v"(te[2 * i + 1])); o[i] = te[2 * i] | (te[2 * i + 1] << 16); }
    if ((wog & 1) && h == 1) { o[2] = 0u; o[3] = 0u; }
    if (wog < 896) st16<16>(o, ws, lane * 16, OFF_WOA + wog * KB);
    cs = xhalf_add(cs);
    if (h == 0) st4<16>(__float_as_uint(cs), ws, r * 4, OFF_CS + wog * 128);
    asm volatile("s_waitcnt vmcnt(0)" ::: "memory");
    unsigned k = 0;
    if (lane == 0) k = __hip_atomic_fetch_add((LAS unsigned*)(cx.sig + 2 * 16), 1u, __ATOMIC_RELAXED, __HIP_MEMORY_SCOPE_WORKGROUP);
    if (__builtin_amdgcn_readfirstlane(k) == 3u && lane < 8) st_tag(p.ws + OFF_INI + (lane * 256 + b) * 8, tag);
  }
  STAMP(3); STAMP_DUMP();
}
DI void phase0_c(const Params& p, const ldsp lds, u64 epo, u64& tag, u32x4& q0, u32x4& q1) {
  const int lane = threadIdx.x & 63, h = lane >> 5;
  const int w = __builtin_amdgcn_readfirstlane(threadIdx.x >> 6), sub = (blockIdx.x >> 3) & 3;
  const Ctx cx = {p.ws, lds + M_SIG + 512, lane, w};
  const int pair = -1; (void)pair;
  STAMP_DECL; STAMP(0);
  __builtin_amdgcn_s_sleep(PRO_DLY);
  if (lane < 56) {
    const unsigned short* src = p.wq + (size_t)(4 * w + lane / 14) * 448 + sub * 112 + (lane % 14) * 8;
#pragma unroll 4
    for (int i = 0; i < 28; ++i) glds16(src + (size_t)i * 16 * 448, (unsigned)(uintptr_t)(lds + P_WQ) + (i * 16 + 4 * w) * 224);
  }
  asm volatile("s_waitcnt vmcnt(0)" ::: "memory");
  tag = (((u64)__builtin_amdgcn_readfirstlane((unsigned)(epo >> 32)) << 32) | __builtin_amdgcn_readfirstlane((unsigned)epo)) + 1ull;
  lds_sig(cx, 3);
  lds_wait<1>(cx, 3, 4u); lds_wait<1>(cx, 0, 4u);
  STAMP(1);
  const int i16 = lane & 15;
  const ldsp wb0 = opaque(lds + P_WQ + (4 * h + (i16 >> 2)) * 224 + w * 56 + ((lane >> 4) & 1) * 32 + (i16 & 3) * 8), wb1 = opaque(wb0 + 14 * 16 * 224);
  const ldsp xb = lds + P_XI + lane * 16;
  u32x4 wr[4], xr[4];
#define PRE_LD(i) do { wr[(i) & 3] = tr_frag(((i) < 14 ? wb0 : wb1) + ((i) % 14) * 16 * 224, 8 * 224); xr[(i) & 3] = lds_r16(xb + (i) * XI_KS); } while (0)
  PRE_LD(0); PRE_LD(1); PRE_LD(2); SBAR();
  f32x16 acc = {};
#pragma unroll
  for (int i = 0; i < 28; ++i) {
    if (i + 3 < 28) PRE_LD(i + 3);
    SBAR();
    acc = MFMA(wr[i & 3], xr[i & 3], acc);
    SBAR();
  }
  lds_sig(cx, 4);
#pragma unroll
  for (int i = 0; i < 16; ++i) acc[i] *= C2;
  q0 = pack8(acc, 0); q1 = pack8(acc, 1);
  lds_wait<0>(cx, 4, 4u);
  STAMP(2); STAMP_DUMP();
}

__global__ void __launch_bounds__(512, 2) k_main(Params p) {
  __shared__ __attribute__((aligned(16))) char smem[L_BYTES];
  const ldsp lds = (ldsp)smem;
  const int tid = threadIdx.x;
  const int w8 = __builtin_amdgcn_readfirstlane(tid >> 6);
  const rsrc_t wt = mkrs(p.ws, OFF_PAIR);
  auto nop = []() {};
  const f32x16 Z = {};
  SPAN_T(0);
  const u64 epo = ld_tag(p.ws + OFF_EPO);
  u64 tag;
  if (tid < 192) *(LAS unsigned*)(lds + M_SIG + tid * 4) = 0u;
  if (CODE_KB && w8 == 3) {
    const unsigned char* pc = (const unsigned char*)(__builtin_amdgcn_s_getpc() & ~127ull);
    const int ln = (((blockIdx.x >> 3) * 64 + (tid & 63)) % (CODE_KB * 8)) * 128;
    glds16(pc + ln, (unsigned)(uintptr_t)(lds + M_ST));
  }
  BAR();
  STAMP_DECL;
#define PAIR_SETUP() \
    int lane = tid & 63, b = blockIdx.x, w = w8 & 3; \
    asm volatile("" : "+v"(lane), "+s"(b), "+s"(w)); \
    lane &= 63; b &= 255; w &= 3; \
    const int r = lane & 31, h = lane >> 5, l16 = lane * 16, xcd = b & 7, jj = b >> 3; \
    const int rt = 8 * xcd + (jj >> 2), sub = jj & 3;          \
    const int rb = 2 * xcd + (jj >> 4), hd = jj & 15;          \
    const ldsp scr = lds + L_SCR + l16; \
    unsigned char* const reg = p.ws + OFF_PAIR + pair * PAIR_SZ; \
    const rsrc_t ws = mkrs(reg, 2 * PAIR_SZ - pair * PAIR_SZ); \
    const Ctx cx = {reg + R_FLG, lds + M_SIG + pair * 256, lane, w}; \
    STAMP(0)

  if (w8 < 4) {
    u32x4 qn0, qn1;
    unsigned kve = 0u;
    phase0_c(p, lds, epo, tag, qn0, qn1);
#pragma unroll 1
   for (int pair = 0; pair < 2; ++pair) {
    PAIR_SETUP();
    const unsigned char* const kpa = reg + R_FKV + (xcd * 64 + 4 * ((2 * xcd + 1 - (lane >> 2)) & 15) + w) * 128 + (lane & 3) * 8;
    u64 miss = 0ull, kvf;
    unsigned chk = pair, pend = 1u;
    int kvi = 0;
    auto kv_mask = [&]() { miss = __builtin_amdgcn_ballot_w64(kvf != tag); };
    auto kv_next = [&]() -> int {
      if (__builtin_expect(((unsigned)(miss >> (4 * kvi)) & 15u) != 0u, 0)) {
#pragma unroll 1
        for (int spin = 0; spin < SPIN_CAP; ++spin) {
          kvf = ld_tag(kpa);
          kv_mask();
          if (((unsigned)(miss >> (4 * kvi)) & 15u) == 0u) break;
          __builtin_amdgcn_s_sleep(2);
        }
        CBAR();
      }
      const int o = R_KVA + (4 * ((2 * xcd + 1 - kvi) & 15) + w) * 4 * KB;
      ++kvi;
      return o;
    };
    {
      u32x4 q[4][2], K[4][2], V[4][2];
      int kb[4];
      if (pair) {
        poll_wait(cx, ((((4 * (lane & 1) + w) << 2) | ((lane >> 1) & 1)) << 3) | xcd, 12, kve);
      } else {
#pragma unroll 1
        for (int spin = 0; spin < SPIN_CAP; ++spin) {
          if (((unsigned)__builtin_amdgcn_ballot_w64(ld_tag(kpa) != tag) & 0xFFu) == 0u) break;
          __builtin_amdgcn_s_sleep(2);
        }
        CBAR();
      }
      STAMP(23);
#pragma unroll
      for (int t = 0; t < 2; ++t) {
        kb[t] = kv_next();
        K[t][0] = ld16<16>(ws, l16, kb[t]); K[t][1] = ld16<16>(ws, l16 + KB, kb[t]);
        V[t][0] = ld16<16>(ws, l16 + 2 * KB, kb[t]); V[t][1] = ld16<16>(ws, l16 + 3 * KB, kb[t]);
        SBAR();
      }
#pragma unroll
      for (int t = 2; t < 4; ++t) { kb[t] = kb[0]; K[t][0] = K[t][1] = V[t][0] = V[t][1] = u32x4{}; }
      kvf = ld_tag(kpa);
      f32x16 o0 = {}, o1 = {}, o2 = {}, o3 = {};
      PIN(o0); PIN(o1); PIN(o2); PIN(o3);
      if (h == 1) { qn1[2] = 0u; qn1[3] = 0u; }
      lds_w16(lds + L_QF + l16 + 2 * w * KB, qn0);
      if (pair) chk_handoff<2>(K[0], ws, l16, kb[0], 6);
      STAMP(24);
      {
        f32x16 s0 = MFMA(K[0][0], qn0, Z);
        s0 = MFMA(K[0][1], qn1, s0);
        set_shift(qn1, xhalf_max(max16(s0, s0[0])), h, 0u);
        lds_w16(lds + L_QF + l16 + (2 * w + 1) * KB, qn1);
        lds_sig(cx, 7);
      }
      if (pair) chk_handoff<2>(V[0], ws, l16 + 2 * KB, kb[0], 6);
      lds_wait<0>(cx, 7, 4u);
#pragma unroll
      for (int a = 0; a < 4; ++a) { q[a][0] = lds_r16(lds + L_QF + l16 + 2 * a * KB); q[a][1] = lds_r16(lds + L_QF + l16 + (2 * a + 1) * KB); }
      STAMP(1);
      f32x16 sa = MFMA(K[0][0], q[0][0], Z), sb;
      sa = MFMA(K[0][1], q[0][1], sa);
      u32x4 pp = {};
#define TILE(c, LD, LDV1) { \
        constexpr int n = (c + 1) & 3, pv = (c + 3) & 3; \
        auto ldV1 = [&]() { if (LDV1 && !NOKV) V[pv][1] = ld16<16>(ws, l16 + 3 * KB, kb[pv]); }; \
        auto ldK = [&]() { if (NOKV) { PIN(K[c][0]); PIN(K[c][1]); } else if (LD) { kb[c] = kv_next(); K[c][0] = ld16<16>(ws, l16, kb[c]); K[c][1] = ld16<16>(ws, l16 + KB, kb[c]); } }; \
        auto ldV0 = [&]() { if (LD && !NOKV) V[c][0] = ld16<16>(ws, l16 + 2 * KB, kb[c]); }; \
        unit<true, true>(sa, sb, Z, o0, o3, pp, K[c][0], K[c][1], q[1][0], q[1][1], V[c][0], V[pv][1], ldV1, nop, nop); \
        unit<true, true>(sb, sa, Z, o1, o0, pp, K[c][0], K[c][1], q[2][0], q[2][1], V[c][0], V[c][1], nop, nop, nop); \
        if (LD && c == 0 && __builtin_expect(pend != 0u, 0)) {        \
          pend = 0u; kv_mask(); \
          _Pragma("unroll") for (int t = 2; t < 4; ++t) { kb[t] = kv_next(); K[t][0] = ld16<16>(ws, l16, kb[t]); K[t][1] = ld16<16>(ws, l16 + KB, kb[t]); \
            V[t][0] = ld16<16>(ws, l16 + 2 * KB, kb[t]); V[t][1] = ld16<16>(ws, l16 + 3 * KB, kb[t]); } } \
        unit<true, true>(sa, sb, Z, o2, o1, pp, K[c][0], K[c][1], q[3][0], q[3][1], V[c][0], V[c][1], nop, ldK, nop); \
        if (LD && c == 0 && __builtin_expect(chk != 0u, 0)) { chk = 0u; chk_handoff<2>(K[n], ws, l16, kb[n], 6); chk_handoff<2>(V[n], ws, l16 + 2 * KB, kb[n], 6); } \
        unit<true, true>(sb, sa, Z, o3, o2, pp, K[n][0], K[n][1], q[0][0], q[0][1], V[c][0], V[c][1], nop, nop, ldV0); }
#pragma unroll 1
      for (int tt = 0; tt < 12; tt += 4) {
        kv_mask(); kvf = ld_tag(kpa);
        TILE(0, true, true) TILE(1, true, true)
        if (tt == 0) { STAMP(27); }
        kv_mask(); kvf = ld_tag(kpa);
        TILE(2, true, true) TILE(3, true, true)
        if (tt == 0) { STAMP(28); } if (tt == 4) { STAMP(29); }
      }
      STAMP(2);
      TILE(0, false, true)
      lds_sig(cx, 11);
      lds_wait<1>(cx, 8, 4u);
      TILE(1, false, false)
      STAMP(3);
      const ldsp red = scr + w * 4 * KB;
#define WR_LO(o, a) [&]() { lds_wf4(red + (a) * 16 * KB, f32x4{o[0], o[1], o[2], o[3]}); lds_wf4(red + (a) * 16 * KB + KB, f32x4{o[4], o[5], o[6], o[7]}); }
#define WR_HI(o, a) [&]() { lds_wf4(red + (a) * 16 * KB + 2 * KB, f32x4{o[8], o[9], o[10], o[11]}); lds_wf4(red + (a) * 16 * KB + 3 * KB, f32x4{o[12], o[13], o[14], o[15]}); lds_sig(cx, a); }
      unit<true, true>(sa, sb, Z, o0, o3, pp, K[3][0], K[3][1], q[0][0], q[0][1], V[2][0], V[1][1], nop, nop, nop);
      unit<true, true>(sb, sa, Z, o0, o0, pp, K[2][0], K[2][1], q[1][0], q[1][1], V[3][0], V[2][1], nop, nop, nop);
      unit<true, true>(sa, sb, Z, o1, o0, pp, K[3][0], K[3][1], q[1][0], q[1][1], V[2][0], V[3][1], nop, WR_LO(o0, 0), WR_HI(o0, 0));
      unit<true, true>(sb, sa, Z, o1, o1, pp, K[2][0], K[2][1], q[2][0], q[2][1], V[3][0], V[2][1], nop, nop, nop);
      unit<true, true>(sa, sb, Z, o2, o1, pp, K[3][0], K[3][1], q[2][0], q[2][1], V[2][0], V[3][1], nop, WR_LO(o1, 1), WR_HI(o1, 1));
      unit<true, true>(sb, sa, Z, o2, o2, pp, K[2][0], K[2][1], q[3][0], q[3][1], V[3][0], V[2][1], nop, nop, nop);
      unit<true, true>(sa, sb, Z, o3, o2, pp, K[3][0], K[3][1], q[3][0], q[3][1], V[2][0], V[3][1], nop, WR_LO(o2, 2), WR_HI(o2, 2));
      unit<false, true>(sb, sa, Z, o3, o3, pp, K[2][0], K[2][1], q[3][0], q[3][1], V[3][0], V[2][1], nop, nop, nop);
      o3 = MFMA(V[3][1], pp, o3);
      WR_LO(o3, 3)(); WR_HI(o3, 3)();
    }
    STAMP(4);
    {
      u32x4 of[8] = {};
      const int src = R_OFA + (rt * 32 + 8 * w) * KB;
      if (w != sub) {
        poll_wait(cx, (((jj & ~3) | w) << 3) | xcd, 0);
        STAMP(5);
        if (DLY_OA) __builtin_amdgcn_s_sleep(DLY_OA);
        ld_handoff<8>(of, ws, l16, src);
      }
      BAR();
      STAMP(6);
      const unsigned bad = __builtin_amdgcn_readfirstlane(*(LAS unsigned*)(cx.sig + 10 * 16));
      if (__builtin_expect((bad >> w) & 1u, 0)) {
        const u32x4 q0 = lds_r16(lds + L_QF + l16 + 2 * w * KB);
        u32x4 q1 = lds_r16(lds + L_QF + l16 + (2 * w + 1) * KB);
        if (h == 1) { q1[2] = 0; q1[3] = 0; }
        float m = -INFINITY;
#pragma unroll 1
        for (int t = 0; t < 64; ++t) {
          f32x16 s = MFMA(ld16<16>(ws, l16, R_KVA + t * 4 * KB), q0, Z);
          s = MFMA(ld16<16>(ws, l16 + KB, R_KVA + t * 4 * KB), q1, s);
          m = max16(s, m);
        }
        set_shift(q1, xhalf_max(m), h, 0u);
        f32x16 o = {};
#pragma unroll 1
        for (int t = 0; t < 64; ++t) {
          f32x16 s = MFMA(ld16<16>(ws, l16, R_KVA + t * 4 * KB), q0, Z);
          s = MFMA(ld16<16>(ws, l16 + KB, R_KVA + t * 4 * KB), q1, s);
          u32x4 p0, p1;
          exp_pack(s, p0, p1);
          o = MFMA(ld16<16>(ws, l16 + 2 * KB, R_KVA + t * 4 * KB), p0, o);
          o = MFMA(ld16<16>(ws, l16 + 3 * KB, R_KVA + t * 4 * KB), p1, o);
        }
        u32x4 y0, y1;
        norm_pack(o, h, y0, y1);
        st16<16>(y0, ws, l16, R_OFA + (rt * 32 + sub * 8 + 2 * w) * KB);
        st16<16>(y1, ws, l16 + KB, R_OFA + (rt * 32 + sub * 8 + 2 * w) * KB);
        lds_w16(scr + (sub * 8 + 2 * w) * KB, y0); lds_w16(scr + (sub * 8 + 2 * w + 1) * KB, y1);
      }
      if (w != sub) {
        chk_handoff<8>(of, ws, l16, src, 0);
        STAMP(7);
#pragma unroll
        for (int i = 0; i < 8; ++i) lds_w16(scr + (8 * w + i) * KB, of[i]);
      }
      BAR();
    }
    STAMP(8);
    if (WARM == 2 && pair == 0)
      glds16_sc1(p.ws + (lane < 28 ? OFF_WOB + lane * 128 : OFF_WAT + (lane < 56 ? lane - 28 : 0) * 128) + (jj * 4 + w) * 3584, (unsigned)(uintptr_t)(lds + L_QF));
    f32x4 ck[4], cq[4];
    const float cv = *(LAS float*)(lds + M_CS + 6 * 128 + r * 4);
#pragma unroll
    for (int g = 0; g < 4; ++g) { cq[g] = lds_rf4(lds + M_CS + 4 * 128 + (8 * g + 4 * h) * 4); ck[g] = lds_rf4(lds + M_CS + 5 * 128 + (8 * g + 4 * h) * 4); }
    BAR();
    STAMP(9);
    u32x4 q0, q1, k0, k1, v0, v1;
    const Clk ck0 = clk_now();
    {
      const int xo = R_XF1 + (rb * 4 + w) * 28 * KB;
      const ldsp wb0 = opaque(lds + L_WB + l16), wb1 = opaque(lds + L_WB + 28 * KB + l16);
      const ldsp st = lds + M_ST + w * 256;
      f32x4 m4[4], r4[4];
      poll_wait(cx, (((jj & ~15) | (w << 2) | (lane & 3)) << 3) | xcd, 1);
      STAMP(10);
      f32x16 aq, ak, av;
      float mu, rs;
      u32x4 xf[28], wk[3], wv[3], wq[4];
#define LDX(i) do { xf[i] = ld16<16>(ws, l16 + ((i) & 3) * KB, xo + ((i) >> 2) * 4 * KB); SBAR(); } while (0)
#define CHKX(i) do { if (__builtin_expect(__builtin_amdgcn_ballot_w64(smax(0u, xf[i]) == SENT) != 0ull, 0)) { \
          _Pragma("unroll 1") for (int spin = 0; spin < SPIN_CAP; ++spin) { CBAR(); RETRY(1); LDX(i); if (__builtin_amdgcn_ballot_w64(smax(0u, xf[i]) == SENT) == 0ull) break; } } SBAR(); } while (0)
#define QST(i) do { if ((i) < 28) { SBAR(); aq = MFMA(wq[(i) & 3], xf[(i) < 28 ? (i) : 0], (i) ? aq : Z); SBAR(); if ((i) + 3 < 28) wq[((i) + 3) & 3] = lds_r16(wb0 + ((i) + 3) * KB); SBAR(); } } while (0)
#pragma unroll
      for (int i = 0; i < X1_AHEAD; ++i) LDX(i);
#pragma unroll
      for (int i = 0; i < 2; ++i) { wk[i] = lds_r16(wb1 + i * KB); wv[i] = lds_r16(wb1 + (28 + i) * KB); }
#pragma unroll
      for (int i = 0; i < 3; ++i) wq[i] = lds_r16(wb0 + i * KB);
      SBAR();
      CHKX(0);
#pragma unroll
      for (int ks = 0; ks < 28; ++ks) {
        ak = MFMA(wk[ks % 3], xf[ks], ks ? ak : Z); SBAR();
        if (ks + 2 < 28) { wk[(ks + 2) % 3] = lds_r16(wb1 + (ks + 2) * KB); wv[(ks + 2) % 3] = lds_r16(wb1 + (28 + ks + 2) * KB); }
        if (X1_AHEAD + 2 * ks < 28) LDX(X1_AHEAD + 2 * ks);
        if (ks == 25) {
          lds_wait<0>(cx, 12 + w, 1u);
          mu = *(LAS float*)(st + r * 4); rs = *(LAS float*)(st + 128 + r * 4);
        }
        if (ks == 26) {
#pragma unroll
          for (int g = 0; g < 4; ++g) { m4[g] = lds_rf4(st + (8 * g + 4 * h) * 4); r4[g] = lds_rf4(st + 128 + (8 * g + 4 * h) * 4); }
        }
        SBAR();
        av = MFMA(xf[ks], wv[ks % 3], ks ? av : Z); SBAR();
        if (X1_AHEAD + 2 * ks + 1 < 28) LDX(X1_AHEAD + 2 * ks + 1);
        if (ks < QS) QST(ks);
        if (ks + 1 < 28) CHKX(ks + 1);
        if (ks == 0) { STAMP(11); } if (ks == 13) { STAMP(12); }
        if (ks == POST_KV) post(cx, b, 4 + w, ck0, DT_KV);
      }
      STAMP(13);
      const int ko = R_KVB + (hd * 64 + rb * 4 + w) * 4 * KB;
      QST(QS); QST(QS + 1);
#pragma unroll
      for (int g = 0; g < 4; ++g) {
#pragma unroll
        for (int i = 4 * g; i < 4 * g + 4; ++i) ak[i] = (ak[i] - mu * ck[g][i & 3]) * rs;
        unsigned a0 = pk(ak[4 * g], ak[4 * g + 1]), a1 = pk(ak[4 * g + 2], ak[4 * g + 3]);
        if (g == 3 && h == 1) { a0 = ONES2; a1 = 0x00003F80u; }
        PIN(a0); PIN(a1);
        if (g < 2) { k0[2 * g] = a0; k0[2 * g + 1] = a1; } else { k1[2 * g - 4] = a0; k1[2 * g - 3] = a1; }
        if (g == 1) st16<16>(k0, ws, l16, ko);
        if (g == 3) st16<16>(k1, ws, l16 + KB, ko);
        QST(QS + 2 + g);
      }
      const unsigned vpad = r == 28 ? ONES2 : 0u;
#pragma unroll
      for (int g = 0; g < 4; ++g) {
#pragma unroll
        for (int i = 4 * g; i < 4 * g + 4; ++i) av[i] = (av[i] - m4[g][i & 3] * cv) * r4[g][i & 3];
        unsigned a0 = pk(av[4 * g], av[4 * g + 1]), a1 = pk(av[4 * g + 2], av[4 * g + 3]);
        if (r >= 28) { a0 = vpad; a1 = vpad; }
        PIN(a0); PIN(a1);
        if (g < 2) { v0[2 * g] = a0; v0[2 * g + 1] = a1; } else { v1[2 * g - 4] = a0; v1[2 * g - 3] = a1; }
        if (g == 1) st16<16>(v0, ws, l16 + 2 * KB, ko);
        if (g == 3) st16<16>(v1, ws, l16 + 3 * KB, ko);
        QST(QS + 6 + g);
      }
      STAMP(26);
      lds_w16(scr + (32 + 4 * w) * KB, k0); QST(QS + 10); lds_w16(scr + (33 + 4 * w) * KB, k1); QST(QS + 11);
      lds_w16(scr + (34 + 4 * w) * KB, v0); QST(QS + 12); lds_w16(scr + (35 + 4 * w) * KB, v1);
      lds_sig(cx, 4);
#pragma unroll
      for (int i = QS + 13; i < 28; ++i) QST(i);
      const float rq = rs * C2;
#pragma unroll
      for (int i = 0; i < 16; ++i) aq[i] = (aq[i] - mu * cq[i >> 2][i & 3]) * rq;
      q0 = pack8(aq, 0); q1 = pack8(aq, 1);
      if (h == 1) { q1[2] = 0; q1[3] = NEG31; }
    }
    if (WARM == 3 && pair == 0)
      glds16_sc1(p.ws + OFF_WAT + (lane < 28 ? lane : 0) * 128 + (jj * 4 + w) * 3584, (unsigned)(uintptr_t)(lds + L_QF));
    STAMP(14);
    {
      f32x16 cd, cf;
#pragma unroll
      for (int i = 0; i < 16; ++i) {
        const int kk = (i & 3) + 8 * (i >> 2) + 4 * h;
        cd[i] = kk <= r ? 0.f : -1e30f; cf[i] = kk <= r ? -1e30f : 0.f;
      }
      f32x16 sa = MFMA(k0, q0, cd), sb;
      sa = MFMA(k1, q1, sa);
      set_shift(q1, xhalf_max(max16(sa, sa[0])), h, NEG31);
      const ldsp kp = scr + (8 + w) * 4 * KB;
      auto need = [&](int u) {
        const int ti = 8 + w - u;
        if (u == 1 && w > 0) lds_wait<0>(cx, 4, 4u);
        if (__builtin_expect((ti & 3) == 3, 0)) lds_wait<1>(cx, 5 + (ti >> 2), 2u);
      };
      u32x4 K[2][2], V[2][2], pp;
      f32x16 o = {};
      sa = MFMA(k0, q0, cd); sa = MFMA(k1, q1, sa);
      V[0][0] = v0; V[0][1] = v1;
      need(1);
      STAMP(15);
      K[1][0] = lds_r16(kp - 4 * KB); K[1][1] = lds_r16(kp - 3 * KB); V[1][0] = lds_r16(kp - 2 * KB);
#pragma unroll
      for (int u = 0; u < 9; ++u) {
        const int c = u & 1, n = c ^ 1;
        auto ha = [&]() {
          if (u + 1 < 9) V[n][1] = lds_r16(kp - (u + 1) * 4 * KB + 3 * KB);
          if (u + 2 < 9) { need(u + 2); K[c][0] = lds_r16(kp - (u + 2) * 4 * KB); K[c][1] = lds_r16(kp - (u + 2) * 4 * KB + KB); }
        };
        auto hc = [&]() { if (u + 2 < 9) V[c][0] = lds_r16(kp - (u + 2) * 4 * KB + 2 * KB); if (u >= 4 && u <= 6 && __builtin_expect(u == (w < 2 ? 4 : 3 + w), 0)) post(cx, b, 8 + w, ck0, DT_OB + (8 - u) * DT_UNIT); };
        f32x16& s = c ? sb : sa; f32x16& sn = c ? sa : sb;
        if (u == 4) { STAMP(16); }
        if (u == 0) unit<true, false>(s, sn, Z, o, o, pp, K[n][0], K[n][1], q0, q1, V[c][0], V[n][1], ha, nop, hc);
        else if (u < 7) unit<true, true>(s, sn, Z, o, o, pp, K[n][0], K[n][1], q0, q1, V[c][0], V[n][1], ha, nop, hc);
        else if (u == 7) unit<true, true>(s, sn, cf, o, o, pp, K[n][0], K[n][1], q0, q1, V[c][0], V[n][1], ha, nop, hc);
        else unit<false, true>(s, sn, Z, o, o, pp, K[n][0], K[n][1], q0, q1, V[c][0], V[n][1], ha, nop, hc);
      }
      o = MFMA(V[0][1], pp, o);
      if (__builtin_expect(__builtin_amdgcn_ballot_w64(h == 1 && !(o[12] < LBIG)) != 0ull, 0)) {
        if (h == 1) { q1[2] = 0; q1[3] = NEG31; }
        float m = -INFINITY;
#pragma unroll 1
        for (int u = 0; u < 9; ++u) {
          f32x16 s;
#pragma unroll
          for (int i = 0; i < 16; ++i) s[i] = u == 0 ? cd[i] : u == 8 ? cf[i] : 0.f;
          s = MFMA(lds_r16(kp - u * 4 * KB), q0, s); s = MFMA(lds_r16(kp - u * 4 * KB + KB), q1, s);
          m = max16(s, m);
        }
        set_shift(q1, xhalf_max(m), h, NEG31);
        o = Z;
#pragma unroll 1
        for (int u = 0; u < 9; ++u) {
          f32x16 s;
#pragma unroll
          for (int i = 0; i < 16; ++i) s[i] = u == 0 ? cd[i] : u == 8 ? cf[i] : 0.f;
          s = MFMA(lds_r16(kp - u * 4 * KB), q0, s); s = MFMA(lds_r16(kp - u * 4 * KB + KB), q1, s);
          u32x4 p0, p1;
          exp_pack(s, p0, p1);
          o = MFMA(lds_r16(kp - u * 4 * KB + 2 * KB), p0, o); o = MFMA(lds_r16(kp - u * 4 * KB + 3 * KB), p1, o);
        }
      }
      u32x4 y0, y1;
      norm_pack(o, h, y0, y1);
      const int oo = R_OFB + ((rb * 4 + w) * 16 + hd) * 2 * KB;
      st16<16>(y0, ws, l16, oo); st16<16>(y1, ws, l16 + KB, oo);
      lds_sig(cx, 9);
    }
    STAMP(17);
    u32x4 wa1[WA1];
    {
      u32x4 of[8];
      const int src = R_OFB + (rt * 32 + 8 * w) * KB;
#pragma unroll
      for (int i = 0; i < WA1; ++i) { wa1[i] = ld16<16>(wt, l16 + (i & 3) * KB, pair ? OFF_PAIR : OFF_WAT + ((4 * sub + w) * 28 + (i & ~3)) * KB); SBAR(); }
      poll_wait(cx, (((jj & ~15) | (w << 2) | (lane & 3)) << 3) | xcd, 8 + (rt & 3));
      STAMP(18);
      if (DLY_OB) __builtin_amdgcn_s_sleep(DLY_OB);
      ld_handoff<8>(of, ws, l16, src);
      chk_handoff<8>(of, ws, l16, src, 3);
      STAMP(19);
      lds_wait<1>(cx, 9, 4u);
#pragma unroll
      for (int i = 0; i < 8; ++i) lds_w16(scr + (8 * w + i) * KB, of[i]);
      BAR();
      STAMP(20);
    }
    if (pair == 0) {
      u32x4 wa[28];
#pragma unroll
      for (int i = 0; i < WA1; ++i) wa[i] = wa1[i];
#pragma unroll
      for (int i = WA1; i < 28; ++i) { wa[i] = ld16<16>(wt, l16 + (i & 3) * KB, OFF_WAT + ((4 * sub + w) * 28 + (i & ~3)) * KB); SBAR(); }
      f32x4 cq[4];
#pragma unroll
      for (int g = 0; g < 4; ++g) cq[g] = lds_rf4(lds + M_CS + w * 128 + (8 * g + 4 * h) * 4);
      if (w >= 2) poll_wait<2>(cx, (((jj & ~3) | (lane & 3)) << 3) | xcd, 2);
      if (w == 3) stats_gather(ws, R_ST2 + rt * 14 * 256, lds + M_ST + 4 * 256, lane);
      if (w == 2 && sub < 2) {
        const unsigned t = eta(ck0, DT_Q);
        if (lane == 0) __hip_atomic_store((GAS unsigned*)(cx.flg + PAIR_SZ + b * 128 + 12 * 4), t, __ATOMIC_RELAXED, __HIP_MEMORY_SCOPE_AGENT);
      }
      BAR();
      if (sub < 2 && w < 2) __builtin_amdgcn_s_sleep(4);
      STAMP(21);
      const float mu = *(LAS float*)(lds + M_ST + 4 * 256 + r * 4), rq = *(LAS float*)(lds + M_ST + 4 * 256 + 128 + r * 4) * C2;
      f32x16 acc = gemm_lds<28, false>(wa, scr + 32 * KB, [&](int i) { if (i == 8) kve = flag_ld(cx, PAIR_SZ / 128 + (((((4 * (lane & 1) + w) << 2) | ((lane >> 1) & 1)) << 3) | xcd), 12); });
#pragma unroll
      for (int i = 0; i < 16; ++i) acc[i] = (acc[i] - mu * cq[i >> 2][i & 3]) * rq;
      qn0 = pack8(acc, 0); qn1 = pack8(acc, 1);
    }
    STAMP(22); STAMP_DUMP();
   }
   return;
  }

  __builtin_amdgcn_s_setprio(2);
  phase0_h(p, lds, epo, tag);
  u32x4 x0 = {}, x1 = {};
#pragma unroll 1
 for (int pair = 0; pair < 2; ++pair) {
  PAIR_SETUP();
  __builtin_amdgcn_s_setprio(HPRIO);
  const int ct0 = sub < 2 ? 4 * sub : 3 * sub + 2, nct = sub < 2 ? 4 : 3;
  const bool ctok = w < nct;
  const int ct = ct0 + (ctok ? w : 0);
  const int xo1 = R_XF1 + (rt * 28 + ct * 2) * KB, xo2 = R_XF2 + (rt * 28 + ct * 2) * KB;
  const int so1 = R_ST1 + (rt * 14 + ct) * 256, so2 = R_ST2 + (rt * 14 + ct) * 256;
  lds_wait<4>(cx, 7, 4u);
  STAMP(1);
  __builtin_amdgcn_s_setprio(HPRIO);
  u32x4 wf[32];
  Clk ck0 = clk_now();
  {
    const u32x4 sv = {SENT, SENT, SENT, SENT};
#define PC() do { SBAR(); if (PACE) __builtin_amdgcn_s_sleep(PACE); SBAR(); } while (0)
    if (NOH) { for (int i = 0; i < NOH; ++i) __builtin_amdgcn_s_sleep(127); }
    auto fill = [&](int i) {
      const int hi = (i & 1) * KB;
      if (i < 2) st16<16>(sv, ws, l16 + hi, R_OFA + (rt * 32 + sub * 8 + 2 * w) * KB);
      else if (i < 4) { if (ctok) { st16<16>(sv, ws, l16 + hi, xo1); if (i == 2) st4<16>(SENT, ws, lane * 4, so1); } }
      else if (i < 8) st16<16>(sv, ws, l16 + (i - 4) * KB, R_KVB + (hd * 64 + rb * 4 + w) * 4 * KB);
      else if (i < 10) st16<16>(sv, ws, l16 + hi, R_OFB + ((rb * 4 + w) * 16 + hd) * 2 * KB);
      else if (i < 12) { if (ctok && pair == 0) { st16<16>(sv, ws, l16 + hi, xo2); if (i == 10) st4<16>(SENT, ws, lane * 4, so2); } }
      else if (i == 12 && pair == 0 && sub < 2 && w < 2) st16<16>(sv, ws, l16, PAIR_SZ + R_KVA + (rt * 4 + 2 * sub + w) * KB);
      SBAR();
    };
#pragma unroll
    for (int i = 0; i < NFILL0; ++i) { fill(i); __builtin_amdgcn_s_sleep(PACEF); }
    if (pair == 0) {
#pragma unroll 1
      for (int spin = 0; spin < SPIN_CAP; ++spin) {
        bool ok = true;
#pragma unroll
        for (int i = 0; i < 4; ++i) ok &= ld_tag(p.ws + OFF_INI + (xcd * 256 + i * 64 + lane) * 8) == tag;
        if (__builtin_amdgcn_ballot_w64(!ok) == 0ull) break;
        __builtin_amdgcn_s_sleep(8);
      }
      CBAR();
    }
    STAMP(18);
    unsigned c4[16] = {};
    if (pair == 0) {
      const unsigned short* xr = p.x + (size_t)(rt * 32 + r) * D_ + ct * 32 + 4 * h;
      const u32x2 a0 = *(const u32x2*)xr, a1 = *(const u32x2*)(xr + 8), a2 = *(const u32x2*)(xr + 16), a3 = *(const u32x2*)(xr + 24);
      x0 = u32x4{a0[0], a0[1], a1[0], a1[1]}; x1 = u32x4{a2[0], a2[1], a3[0], a3[1]};
      const int co = (h ? (w < 3 ? 16 + hd * 3 + w : 64 + (sub & 1)) : 4 * sub + w) * 2048 + r * 4;
#pragma unroll
      for (int i = 0; i < 16; ++i) c4[i] = ld4<16>(wt, co + i * 128, OFF_CS);
      if (WARM == 1) glds16_sc1(p.ws + (lane < 28 ? OFF_WOB + lane * 128 : OFF_WAT + (lane < 56 ? lane - 28 : 0) * 128) + (jj * 4 + w) * 3584, (unsigned)(uintptr_t)(lds + M_ST));
      SBAR();
    }
    const unsigned char* wbsrc = p.ws + OFF_WBT + (hd * 84 + w * 21) * KB + l16;
    const unsigned wbdst = (unsigned)(uintptr_t)(lds + L_WB) + w * 21 * KB;
#pragma unroll
    for (int i = 0; i < 32; ++i) {
      wf[i] = ld16<16>(wt, l16 + (i & 3) * KB, OFF_WOA + (ct * 32 + (i & ~3)) * KB); SBAR();
      if (i % 3 != 2 || i > 29) { const int g = i - (i + 1) / 3; if (g < 21 && pair == 0) glds16_sc1(wbsrc + g * KB, wbdst + g * KB); }
      if ((i & 1) == 0 && NFILL0 + (i >> 1) < 13) fill(NFILL0 + (i >> 1));
      if (i == 10) { asm volatile("s_waitcnt vmcnt(5)" ::: "memory"); lds_sig(cx, 8); }
      if (i % PGRP == PGRP - 1) { SBAR(); if (pair) __builtin_amdgcn_s_sleep(PACE3); else __builtin_amdgcn_s_sleep(PACE); SBAR(); }
    }
    if (pair == 0) {
      float t = 0.f;
#pragma unroll
      for (int i = 0; i < 16; ++i) t += __uint_as_float(c4[i]);
      *(LAS float*)(lds + M_CS + (4 * h + w) * 128 + r * 4) = t;
    }
    asm volatile("s_waitcnt vmcnt(0)" ::: "memory");
  }
  STAMP(2);
  {
    u32x4 keep[2];
    const int s = w & 1;
    __builtin_amdgcn_s_setprio(2);
    lds_wait<2>(cx, 8, 4u);
#pragma unroll 1
    for (int spin = 0; spin < SPIN_CAP; ++spin) {
      if (__builtin_amdgcn_readfirstlane(*(volatile LAS unsigned*)(cx.sig + 11 * 16 + w * 4))) break;
      __builtin_amdgcn_s_sleep(DOZE);
    }
#pragma unroll
    for (int it = 0; it < 2; ++it) {
      const int T = 2 * it + (w >> 1);
      if (it == 0) lds_wait<2>(cx, T, 4u); else lds_wait<1>(cx, T, 4u);
      if (it == 0) { if (w == POST_OA) post(cx, b, 0, ck0, DT_OA); ck0 = clk_now(); }

      const ldsp red = scr + T * 16 * KB;
      f32x4 a0[4], a1[4]; float l4[4];
#pragma unroll
      for (int k = 0; k < 4; ++k) {
        l4[k] = *(LAS float*)(lds + L_SCR + T * 16 * KB + k * 4 * KB + 3 * KB + (32 + r) * 16);
        a0[k] = lds_rf4(red + k * 4 * KB + (2 * s) * KB);
        a1[k] = lds_rf4(red + k * 4 * KB + (2 * s + 1) * KB);
      }
      const float l = (l4[0] + l4[1]) + (l4[2] + l4[3]);
      const f32x4 b0 = (a0[0] + a0[1]) + (a0[2] + a0[3]), b1 = (a1[0] + a1[1]) + (a1[2] + a1[3]);
      const float inv = __builtin_amdgcn_rcpf(l);
      u32x4 o = {pk(b0[0] * inv, b0[1] * inv), pk(b0[2] * inv, b0[3] * inv), pk(b1[0] * inv, b1[1] * inv), pk(b1[2] * inv, b1[3] * inv)};
      if (s == 1 && h == 1) { o[2] = 0; o[3] = 0; }
      if (__builtin_expect(__builtin_amdgcn_ballot_w64(!(l < LBIG)) == 0ull, 1)) st16<16>(o, ws, l16, R_OFA + (rt * 32 + sub * 8 + 2 * T + s) * KB);
      else if (lane == 0) __hip_atomic_fetch_or((LAS unsigned*)(cx.sig + 10 * 16), 1u << T, __ATOMIC_RELAXED, __HIP_MEMORY_SCOPE_WORKGROUP);
      keep[it] = o;
      if (it == 0) { STAMP(3); } else { STAMP(4); }
    }
    BAR();
    STAMP(5);
    const unsigned bad = __builtin_amdgcn_readfirstlane(*(LAS unsigned*)(cx.sig + 10 * 16));
#pragma unroll
    for (int it = 0; it < 2; ++it) {
      const int T = 2 * it + (w >> 1);
      if (!((bad >> T) & 1u)) lds_w16(scr + (sub * 8 + 2 * T + s) * KB, keep[it]);
    }
    BAR();
  }
  STAMP(6);
  {
    const f32x16 acc = gemm_lds<32, false>(wf, scr, [&](int i) { if (i == 0 && w == 0) post(cx, b, 1, ck0, DT_X); if (i == 24) BAR();   });
    add_res(acc, x0, x1, x0, x1);
    if (ctok) { st16<16>(x0, ws, l16, xo1); st16<16>(x1, ws, l16 + KB, xo1); SBAR(); st4<16>(__float_as_uint(row_stats(x0, x1, h)), ws, lane * 4, so1); }
  }
  STAMP(7);
  u32x4 wkv[7];
  {
    __builtin_amdgcn_s_setprio(0);
    poll_wait<2>(cx, (((jj & ~15) | (w << 2) | (lane & 3)) << 3) | xcd, 1);
    __builtin_amdgcn_s_sleep(ST_DLY);
    stats_gather(ws, R_ST1 + (rb * 4 + w) * 14 * 256, lds + M_ST + w * 256, lane);
    lds_sig(cx, 12 + w);
    const int kt = 4 * rb - 8 + 2 * w;
    u32x4 hv[8];
    STAMP(8);
    if (kt >= 0) {
      const int prb = kt >> 2;
      __builtin_amdgcn_s_setprio(0);
      poll_wait<4>(cx, (((prb & 1) * 16 + hd) << 3) | (prb >> 1), 4 + (kt & 3) + (lane & 1));
      __builtin_amdgcn_s_setprio(2);
      STAMP(9);
      if (DLY_KV) __builtin_amdgcn_s_sleep(DLY_KV);
      ld_handoff<8>(hv, ws, l16, R_KVB + (hd * 64 + kt) * 4 * KB);
      chk_handoff<8>(hv, ws, l16, R_KVB + (hd * 64 + kt) * 4 * KB, 2);
    } else {
#pragma unroll
      for (int i = 0; i < 8; ++i) hv[i] = u32x4{0u, 0u, 0u, ((i & 3) == 1 && h == 1) ? 0x3F800000u : 0u};
    }
#pragma unroll
    for (int i = 0; i < 8; ++i) lds_w16(scr + (8 * w + i) * KB, hv[i]);
    lds_sig(cx, 5 + (w >> 1));
    STAMP(10);
    __builtin_amdgcn_s_setprio(0);
#pragma unroll
    for (int i = 0; i < 32; ++i) { wf[i] = ld16<16>(wt, l16 + (i & 3) * KB, OFF_WOB + (ct * 32 + (i & ~3)) * KB); SBAR(); if (PACE2) __builtin_amdgcn_s_sleep(PACE2); SBAR(); }
#pragma unroll
    for (int i = 0; i < 7; ++i) wkv[i] = ld16<16>(wt, l16 + (i & 3) * KB, OFF_WKV + ((sub & 1) * 28 + 7 * w + (i & 4)) * KB);
    asm volatile("s_waitcnt vmcnt(0)" ::: "memory");
    BAR();
  }
  STAMP(11);
  __builtin_amdgcn_s_setprio(2);
  {
    const f32x16 acc = gemm_lds<32, false>(wf, scr, [&](int i) { if (i == 0 && w == 0 && pair == 0) post(cx, b, 2, ck0, DT_X); if (i == 31 && pair == 0) lds_sig(cx, 32 + 5); });
    add_res(acc, x0, x1, x0, x1);
    if (pair == 1) {
      if (ctok) {
        unsigned short* orow = p.out + (size_t)(rt * 32 + r) * D_ + ct * 32 + 4 * h;
        *(u32x2*)orow = u32x2{x0[0], x0[1]}; *(u32x2*)(orow + 8) = u32x2{x0[2], x0[3]};
        *(u32x2*)(orow + 16) = u32x2{x1[0], x1[1]}; *(u32x2*)(orow + 24) = u32x2{x1[2], x1[3]};
      }
      if (b == 0 && w == 0 && lane == 0) st_tag(p.ws + OFF_EPO, tag);
      STAMP(12); STAMP_DUMP(); SPAN_T(1);
      return;
    }
    if (ctok) { st16<16>(x0, ws, l16, xo2); st16<16>(x1, ws, l16 + KB, xo2); SBAR(); st4<16>(__float_as_uint(row_stats(x0, x1, h)), ws, lane * 4, so2); }
  }
  STAMP(12);
  {
    u32x4 xf[7];
    const int src = R_XF2 + (rt * 28 + 7 * w) * KB;
    lds_wait<0>(cx, 32 + 5, 4u);
    poll_wait(cx, (((jj & ~3) | (lane & 3)) << 3) | xcd, 2);
    STAMP(13);
    ld_handoff<7>(xf, ws, l16, src);
    chk_handoff<7>(xf, ws, l16, src, 4);
    STAMP(14);
    if (sub < 2) {
      f32x16 acc = {};
      if (sub) {
#pragma unroll
        for (int i = 0; i < 7; ++i) { acc = MFMA(xf[i], wkv[i], acc); SBAR(); lds_w16(scr + (32 + 7 * w + i) * KB, xf[i]); SBAR(); }
      } else {
#pragma unroll
        for (int i = 0; i < 7; ++i) { acc = MFMA(wkv[i], xf[i], acc); SBAR(); lds_w16(scr + (32 + 7 * w + i) * KB, xf[i]); SBAR(); }
      }
      part_write(scr + w * 4 * KB, acc);
      STAMP(16);
    } else {
#pragma unroll
      for (int i = 0; i < 7; ++i) lds_w16(scr + (32 + 7 * w + i) * KB, xf[i]);
    }
    BAR();
    if (sub < 2 && w < 2) kv_finish<true>(lds + L_SCR, 4 * KB, sub, w, lds + M_ST + 4 * 256, lds + M_CS + 7 * 128, lane, ws, PAIR_SZ + R_KVA + rt * 4 * KB, reg + PAIR_SZ + R_FKV + rt * 128, tag, [&]() { STAMP(17); });
  }
  STAMP(15); STAMP_DUMP();
 }
}

extern "C" void kernel_launch(void* const* d_in, const int* in_sizes, int n_in, void* d_out, int out_size, void* d_ws, size_t ws_size,
                              hipStream_t stream) {
  Params p;
  p.x = (const unsigned short*)d_in[0];
  p.wq = (const unsigned short*)d_in[1];
  p.wk = (const unsigned short*)d_in[2];
  p.wv = (const unsigned short*)d_in[3];
  p.woa = (const unsigned short*)d_in[4];
  p.wqkv = (const unsigned short*)d_in[6];
  p.wob = (const unsigned short*)d_in[7];
  p.out = (unsigned short*)d_out;
  p.ws = (unsigned char*)d_ws;
  k_main<<<256, 512, 0, stream>>>(p);
}
```

```cpp
#include <hip/hip_runtime.h>
#include <hip/hip_bf16.h>
#include <stdint.h>

typedef short bf16x8 __attribute__((ext_vector_type(8)));
typedef short s16x4 __attribute__((ext_vector_type(4)));
typedef float f32x16 __attribute__((ext_vector_type(16)));
typedef float f32x4 __attribute__((ext_vector_type(4)));
typedef float f32x2 __attribute__((ext_vector_type(2)));
typedef unsigned u32x4 __attribute__((ext_vector_type(4)));
typedef unsigned u32x2 __attribute__((ext_vector_type(2)));
typedef __bf16 bf16x2_t __attribute__((ext_vector_type(2)));
#define DI __device__ __forceinline__
#define LAS __attribute__((address_space(3)))
#define GAS __attribute__((address_space(1)))
typedef LAS char* ldsp;

#define MFMA(a, b, c) __builtin_amdgcn_mfma_f32_32x32x16_bf16(__builtin_bit_cast(bf16x8, (a)), __builtin_bit_cast(bf16x8, (b)), (c), 0, 0, 0)
#define BAR() asm volatile("s_waitcnt lgkmcnt(0)\n\ts_barrier" ::: "memory")
#define CBAR() asm volatile("" ::: "memory")
#define SBAR() __builtin_amdgcn_sched_barrier(0)

constexpr int D_ = 448;
constexpr float C2 = 0.18898223650461362f * 1.4426950408889634f;
constexpr float EPS = 1e-5f;
constexpr unsigned SENT = 0xFFFFFFFFu;
constexpr unsigned ONES2 = 0x3F803F80u;
constexpr int SPIN_CAP = 1 << 16;

constexpr int KB = 1024;
constexpr int OFF_WAT = 0;
constexpr int OFF_WBT = OFF_WAT + 448 * KB;
constexpr int OFF_WKV = OFF_WBT + 1344 * KB;
constexpr int OFF_WOA = OFF_WKV + 56 * KB;
constexpr int OFF_WOB = OFF_WOA + 448 * KB;
constexpr int OFF_CS = OFF_WOB + 448 * KB;
constexpr int OFF_EPO = OFF_CS + 132 * KB;
constexpr int OFF_INI = OFF_EPO + KB;
constexpr int OFF_PAIR = OFF_INI + 16 * KB;
constexpr int QA_SZ = 64 * 28 * KB;
constexpr int R_KVA = 0;
constexpr int R_OFA = R_KVA + 256 * KB;
constexpr int R_XF1 = R_OFA + 2048 * KB;
constexpr int R_KVB = R_XF1 + QA_SZ;
constexpr int R_OFB = R_KVB + 4096 * KB;
constexpr int R_XF2 = R_OFB + 2048 * KB;
constexpr int R_ST1 = R_XF2 + QA_SZ;
constexpr int R_ST2 = R_ST1 + 64 * 14 * 256;
constexpr int R_FLG = R_ST2 + 64 * 14 * 256;
constexpr int R_FKV = R_FLG + 32 * KB;
constexpr int PAIR_SZ = R_FKV + 64 * KB;
constexpr int WS_BYTES = OFF_PAIR + 2 * PAIR_SZ;

constexpr int HPRIO = 0, DT_OA = 1900, DT_X = 1850, DT_KV = 3700, DT_OB = 850, DT_UNIT = 240, X1_AHEAD = 8, WA1 = 8;
constexpr int PRO_DLY = 2, PACE3 = 8, DT_Q = 3000, QS = 14, ST_DLY = 20, CODE_KB = 44, PGRP = 2, DOZE = 14;
constexpr int NFILL0 = 6, PACEF = 2, HALO_SLP = 8, PACE = 3, POST_OA = 2, POST_KV = 12;

DI unsigned pk(float a, float b) { f32x2 v = {a, b}; bf16x2_t r = __builtin_convertvector(v, bf16x2_t); return __builtin_bit_cast(unsigned, r); }
DI float dot2(unsigned a, unsigned b, float c) { return __builtin_amdgcn_fdot2_f32_bf16(__builtin_bit_cast(bf16x2_t, a), __builtin_bit_cast(bf16x2_t, b), c, false); }
DI float bflo(unsigned u) { return __uint_as_float(u << 16); }
DI float bfhi(unsigned u) { return __uint_as_float(u & 0xFFFF0000u); }
DI float xhalf_add(float v) { auto r = __builtin_amdgcn_permlane32_swap(__float_as_uint(v), __float_as_uint(v), false, false); return __uint_as_float(r[0]) + __uint_as_float(r[1]); }
DI float xhalf_max(float v) { auto r = __builtin_amdgcn_permlane32_swap(__float_as_uint(v), __float_as_uint(v), false, false); return __builtin_fmaxf(__uint_as_float(r[0]), __uint_as_float(r[1])); }
DI float from_hi(float v) { auto r = __builtin_amdgcn_permlane32_swap(__float_as_uint(v), __float_as_uint(v), false, false); return __uint_as_float(r[1]); }
DI float max3f(float a, float b, float c) { return __builtin_fmaxf(__builtin_fmaxf(a, b), c); }
DI float max16(const f32x16& s, float m) {
#pragma unroll
  for (int i = 0; i < 16; i += 2) m = max3f(m, s[i], s[i + 1]);
  return m;
}
typedef __amdgpu_buffer_rsrc_t rsrc_t;
DI rsrc_t mkrs(const void* p, unsigned n) { return __builtin_amdgcn_make_buffer_rsrc((void*)p, 0, n, 0x00020000); }
template <int AUX> DI u32x4 ld16(rsrc_t rs, int voff, int soff) { return __builtin_amdgcn_raw_buffer_load_b128(rs, voff, soff, AUX); }
template <int AUX> DI void st16(u32x4 v, rsrc_t rs, int voff, int soff) { __builtin_amdgcn_raw_buffer_store_b128(v, rs, voff, soff, AUX); }
template <int AUX> DI unsigned ld4(rsrc_t rs, int voff, int soff) { return __builtin_amdgcn_raw_buffer_load_b32(rs, voff, soff, AUX); }
template <int AUX> DI void st4(unsigned v, rsrc_t rs, int voff, int soff) { __builtin_amdgcn_raw_buffer_store_b32(v, rs, voff, soff, AUX); }
DI ldsp opaque(ldsp p) { unsigned a = (unsigned)(uintptr_t)p; asm volatile("" : "+v"(a)); return (ldsp)(uintptr_t)a; }
typedef unsigned long long u64;
DI u64 ld_tag(const unsigned char* a) { return __hip_atomic_load((GAS u64*)a, __ATOMIC_RELAXED, __HIP_MEMORY_SCOPE_AGENT); }
DI void st_tag(unsigned char* a, u64 v) { __hip_atomic_store((GAS u64*)a, v, __ATOMIC_RELAXED, __HIP_MEMORY_SCOPE_AGENT); }
DI u32x4 lds_r16(ldsp p) { return *(LAS u32x4*)p; }
DI f32x4 lds_rf4(ldsp p) { return *(LAS f32x4*)p; }
DI void lds_w16(ldsp p, u32x4 v) { *(LAS u32x4*)p = v; }
DI void lds_wf4(ldsp p, f32x4 v) { *(LAS f32x4*)p = v; }
DI unsigned umax3(unsigned a, unsigned b, unsigned c) { return max(max(a, b), c); }
DI unsigned smax(unsigned m, u32x4 v) { return umax3(umax3(m, v[0], v[1]), v[2], v[3]); }
DI void glds16_sc1(const void* g, unsigned lds_base) {
  unsigned sv;
  asm volatile("s_mov_b32 %0, m0\n\ts_mov_b32 m0, %2\n\ts_nop 0\n\tglobal_load_lds_dwordx4 %1, off sc1\n\ts_mov_b32 m0, %0" : "=&s"(sv) : "v"(g), "s"(lds_base) : "memory");
}
DI void glds16(const void* g, unsigned lds_base) {
  unsigned sv;
  asm volatile("s_mov_b32 %0, m0\n\ts_mov_b32 m0, %2\n\ts_nop 0\n\tglobal_load_lds_dwordx4 %1, off\n\ts_mov_b32 m0, %0" : "=&s"(sv) : "v"(g), "s"(lds_base) : "memory");
}
DI u32x4 pack8(const f32x16& a, int s) {
  u32x4 r;
#pragma unroll
  for (int i = 0; i < 4; ++i) r[i] = pk(a[8 * s + 2 * i], a[8 * s + 2 * i + 1]);
  return r;
}
template <int N> DI void ld_handoff(u32x4 (&v)[N], rsrc_t rs, int voff, int soff) {
#pragma unroll
  for (int i = 0; i < N; ++i) { v[i] = ld16<16>(rs, voff + (i & 3) * KB, soff + (i >> 2) * 4 * KB); SBAR(); }
}
template <int N> DI bool any_sent(const u32x4 (&v)[N]) {
  unsigned m = 0;
#pragma unroll
  for (int i = 0; i < N; ++i) m = smax(m, v[i]);
  return __builtin_amdgcn_ballot_w64(m == SENT) != 0ull;
}
template <int N> DI void chk_handoff(u32x4 (&v)[N], rsrc_t rs, int voff, int soff) {
  if (__builtin_expect(any_sent<N>(v), 0)) {
#pragma unroll 1
    for (int spin = 0; spin < SPIN_CAP; ++spin) {
      CBAR();
#pragma unroll
      for (int i = 0; i < N; ++i)
        if (__builtin_amdgcn_ballot_w64(smax(0u, v[i]) == SENT) != 0ull) v[i] = ld16<16>(rs, voff + (i & 3) * KB, soff + (i >> 2) * 4 * KB);
      if (!any_sent<N>(v)) break;
    }
  }
}

struct Params {
  const unsigned short *x, *wq, *wk, *wv, *woa, *wqkv, *wob;
  unsigned short* out;
  unsigned char* ws;
};

constexpr int L_SCR = 0;
constexpr int L_WB = 64 * KB;
constexpr int L_QF = 148 * KB;
constexpr int L_MISC = L_QF + 8 * KB;
constexpr int M_SIG = L_MISC;
constexpr int M_ST = M_SIG + 768;
constexpr int M_CS = M_ST + 1280;
constexpr int L_BYTES = M_CS + 1024;
constexpr int XI_KS = 1056;
constexpr int P_KV = 0, P_KVW = 112 * 56, P_XI = 25600, P_WQ = P_XI + 28 * XI_KS;
static_assert(L_BYTES <= 160 * KB && P_WQ + 448 * 224 + 64 <= L_MISC, "LDS");

DI void exp_pack(const f32x16& s, u32x4& p0, u32x4& p1) {
#pragma unroll
  for (int i = 0; i < 4; ++i) p0[i] = pk(__builtin_amdgcn_exp2f(s[2 * i]), __builtin_amdgcn_exp2f(s[2 * i + 1]));
#pragma unroll
  for (int i = 0; i < 4; ++i) p1[i] = pk(__builtin_amdgcn_exp2f(s[8 + 2 * i]), __builtin_amdgcn_exp2f(s[8 + 2 * i + 1]));
}
DI void set_shift(u32x4& q1, float m, int h, unsigned hi31) {
  const unsigned a = pk(m, m); const float r1 = m - bflo(a);
  const unsigned c = pk(r1, r1); const float r2 = r1 - bflo(c);
  const unsigned e = pk(r2, 0.f);
  const unsigned d2 = ((a & 0xFFFFu) | (c << 16)) ^ 0x80008000u, d3 = (e ^ 0x00008000u) | hi31;
  q1[2] = h ? d2 : q1[2]; q1[3] = h ? d3 : q1[3];
}
#define PIN(x) asm volatile("" : "+v"(x))
template <bool DOQK, bool DOPVP, class FA, class FB, class FC>
DI void unit(const f32x16& s, f32x16& sn, const f32x16& cn, f32x16& o, f32x16& op, u32x4& pp, const u32x4& kn0, const u32x4& kn1, const u32x4& qn0,
             const u32x4& qn1, const u32x4& v0, const u32x4& v1p, FA ha, FB hb, FC hc) {
  float e[16]; u32x4 p0;
#define E_(i) e[i] = __builtin_amdgcn_exp2f(s[i]); PIN(e[i]); SBAR()
#define C0_(i) p0[i] = pk(e[2 * i], e[2 * i + 1]); PIN(p0[i]); SBAR()
#define C1_(i) pp[i] = pk(e[8 + 2 * i], e[8 + 2 * i + 1]); PIN(pp[i]); SBAR()
  SBAR();
  if (DOQK) { sn = MFMA(kn0, qn0, cn); SBAR(); }
  E_(0); E_(1); E_(2); E_(3);
  if (DOPVP) { op = MFMA(v1p, pp, op); SBAR(); }
  ha(); SBAR();
  E_(4); C0_(0); E_(5); C0_(1); E_(6); E_(7);
  if (DOQK) { sn = MFMA(kn1, qn1, sn); SBAR(); }
  hb(); SBAR();
  E_(8); C0_(2); E_(9); C0_(3); E_(10); E_(11);
  o = MFMA(v0, p0, o); SBAR();
  hc(); SBAR();
  E_(12); C1_(0); E_(13); C1_(1); E_(14); E_(15); C1_(2); C1_(3);
}

struct Ctx { unsigned char* flg; ldsp sig; int lane; int w; };
DI unsigned now32() { return (unsigned)__builtin_amdgcn_s_memrealtime(); }
struct Clk { unsigned c, r; };
DI Clk clk_now() { Clk k; k.c = (unsigned)__builtin_amdgcn_s_memtime(); k.r = now32(); return k; }
DI unsigned eta(const Clk& k0, int dcyc) {
  const Clk k = clk_now();
  return (k.r + (unsigned)((float)dcyc * (float)(k.r - k0.r) * __builtin_amdgcn_rcpf((float)(k.c - k0.c)))) | 1u;
}
DI void post(const Ctx& c, int b, int slot, const Clk& k0, int dcyc) {
  const unsigned t = eta(k0, dcyc);
  if (c.lane == 0) __hip_atomic_store((GAS unsigned*)(c.flg + b * 128 + slot * 4), t, __ATOMIC_RELAXED, __HIP_MEMORY_SCOPE_AGENT);
}
DI unsigned flag_ld(const Ctx& c, int pb, int slot) { return __hip_atomic_load((GAS unsigned*)(c.flg + pb * 128 + slot * 4), __ATOMIC_RELAXED, __HIP_MEMORY_SCOPE_AGENT); }
template <int SLP = 1> DI void poll_wait(const Ctx& c, int pb, int slot, unsigned f = 0u) {
#pragma unroll 1
  for (int spin = 0; spin < SPIN_CAP; ++spin) {
    if (__builtin_amdgcn_ballot_w64(f == 0u) == 0ull) break;
    if (spin) __builtin_amdgcn_s_sleep(SLP);
    f = __hip_atomic_load((GAS unsigned*)(c.flg + pb * 128 + slot * 4), __ATOMIC_RELAXED, __HIP_MEMORY_SCOPE_AGENT);
  }
  unsigned t = __builtin_amdgcn_readlane(f, 0);
#pragma unroll
  for (int i = 1; i < 4; ++i) { const unsigned u = __builtin_amdgcn_readlane(f, i); t = (int)(u - t) > 0 ? u : t; }
#pragma unroll 1
  for (int spin = 0; spin < 256; ++spin) {
    const int d = (int)(t - now32());
    if (d <= 0) break;
    if (d > 30) __builtin_amdgcn_s_sleep(6); else __builtin_amdgcn_s_sleep(1);
  }
  CBAR();
}
DI void lds_sig(const Ctx& c, int idx) {
  CBAR();
  *(volatile LAS unsigned*)(c.sig + idx * 16 + c.w * 4) = 1u;
  CBAR();
}
template <int SLP> DI void lds_wait(const Ctx& c, int idx, unsigned n) {
#pragma unroll 1
  for (int spin = 0; spin < (SPIN_CAP << 6); ++spin) {
    const u32x4 v = *(volatile LAS u32x4*)(c.sig + idx * 16);
    if (__builtin_amdgcn_readfirstlane(v[0] + v[1] + v[2] + v[3]) >= n) break;
    if (SLP) __builtin_amdgcn_s_sleep(SLP);
  }
  CBAR();
}
template <int N, bool SWAP, class F> DI f32x16 gemm_lds(const u32x4 (&wr)[N], ldsp bb, F mid) {
  f32x16 acc = {};
  u32x4 bf[8];
#pragma unroll
  for (int i = 0; i < 8; ++i) bf[i] = lds_r16(bb + i * KB);
  SBAR();
#pragma unroll
  for (int i = 0; i < N; ++i) {
    acc = SWAP ? MFMA(bf[i & 7], wr[i], acc) : MFMA(wr[i], bf[i & 7], acc);
    SBAR();
    if (i + 8 < N) bf[i & 7] = lds_r16(bb + (i + 8) * KB);
    mid(i);
    SBAR();
  }
  return acc;
}
DI void add_res(const f32x16& a, const u32x4& x0, const u32x4& x1, u32x4& y0, u32x4& y1) {
#pragma unroll
  for (int i = 0; i < 4; ++i) {
    y0[i] = pk(a[2 * i] + bflo(x0[i]), a[2 * i + 1] + bfhi(x0[i]));
    y1[i] = pk(a[8 + 2 * i] + bflo(x1[i]), a[8 + 2 * i + 1] + bfhi(x1[i]));
  }
}
DI float row_stats(const u32x4& y0, const u32x4& y1, int h) {
  float s0 = 0.f, s1 = 0.f, q0 = 0.f, q1 = 0.f;
#pragma unroll
  for (int i = 0; i < 4; ++i) { s0 = dot2(y0[i], ONES2, s0); s1 = dot2(y1[i], ONES2, s1); q0 = dot2(y0[i], y0[i], q0); q1 = dot2(y1[i], y1[i], q1); }
  const float s = xhalf_add(s0 + s1), q = xhalf_add(q0 + q1);
  return h ? q : s;
}
DI void stats_finish(float v, float& mu, float& rs) {
  auto t = __builtin_amdgcn_permlane32_swap(__float_as_uint(v), __float_as_uint(v), false, false);
  mu = __uint_as_float(t[0]) * (1.f / D_);
  rs = __builtin_amdgcn_rsqf(__builtin_fmaxf(__uint_as_float(t[1]) * (1.f / D_) - mu * mu, 0.f) + EPS);
}
DI void stats_gather(rsrc_t ws, int so, ldsp dst, int lane) {
#pragma unroll 1
  for (int spin = 0; spin < SPIN_CAP; ++spin) {
    CBAR();
    unsigned sv[14], mx = 0;
#pragma unroll
    for (int i = 0; i < 14; ++i) sv[i] = ld4<16>(ws, lane * 4 + i * 256, so);
    float t0 = 0.f, t1 = 0.f, mu, rs;
#pragma unroll
    for (int i = 0; i < 14; i += 2) { t0 += __uint_as_float(sv[i]); t1 += __uint_as_float(sv[i + 1]); mx = umax3(mx, sv[i], sv[i + 1]); }
    stats_finish(t0 + t1, mu, rs);
    *(LAS float*)(dst + lane * 4) = lane >= 32 ? rs : mu;
    if (__builtin_amdgcn_ballot_w64(mx == SENT) == 0ull) break;
  }
}
DI void norm_pack(const f32x16& o, int h, u32x4& y0, u32x4& y1) {
  const float inv = __builtin_amdgcn_rcpf(from_hi(o[12]));
#pragma unroll
  for (int i = 0; i < 4; ++i) { y0[i] = pk(o[2 * i] * inv, o[2 * i + 1] * inv); y1[i] = pk(o[8 + 2 * i] * inv, o[8 + 2 * i + 1] * inv); }
  if (h == 1) { y1[2] = 0; y1[3] = 0; }
}
constexpr float LBIG = 1e30f;
constexpr unsigned NEG31 = 0xF1490000u;

DI u32x4 tr_frag(ldsp a, int hi) {
  const s16x4 lo_ = __builtin_amdgcn_ds_read_tr16_b64_v4i16((LAS s16x4*)a), hi_ = __builtin_amdgcn_ds_read_tr16_b64_v4i16((LAS s16x4*)(a + hi));
  return __builtin_bit_cast(u32x4, __builtin_shufflevector(lo_, hi_, 0, 1, 2, 3, 4, 5, 6, 7));
}
DI void part_write(ldsp d, const f32x16& a) {
#pragma unroll
  for (int g = 0; g < 4; ++g) lds_wf4(d + g * KB, f32x4{a[4 * g], a[4 * g + 1], a[4 * g + 2], a[4 * g + 3]});
}
template <bool LN>
DI void kv_finish(ldsp part, int pstr, int isV, int s, ldsp st, ldsp cs, int lane, rsrc_t ws, int kvo, unsigned char* fkv, u64 tag) {
  const int r = lane & 31, h = lane >> 5;
  f32x4 a[2][4], c[2] = {}, m[2] = {};
  float l0 = 0.f, l1 = 0.f;
#pragma unroll
  for (int k = 0; k < 4; ++k) { a[0][k] = lds_rf4(part + k * pstr + 2 * s * KB + lane * 16); a[1][k] = lds_rf4(part + k * pstr + (2 * s + 1) * KB + lane * 16); }
  if (LN) {
    l0 = *(LAS float*)((isV ? cs : st) + r * 4); l1 = *(LAS float*)(st + 128 + r * 4);
#pragma unroll
    for (int g = 0; g < 2; ++g) { c[g] = lds_rf4((isV ? st : cs) + (16 * s + 8 * g + 4 * h) * 4); m[g] = lds_rf4(st + 128 + (16 * s + 8 * g + 4 * h) * 4); }
  }
  SBAR();
  float v[8];
#pragma unroll
  for (int i = 0; i < 8; ++i) v[i] = (a[i >> 2][0][i & 3] + a[i >> 2][1][i & 3]) + (a[i >> 2][2][i & 3] + a[i >> 2][3][i & 3]);
  if (LN) {
#pragma unroll
    for (int i = 0; i < 8; ++i) v[i] = (v[i] - l0 * c[i >> 2][i & 3]) * (isV ? m[i >> 2][i & 3] : l1);
  }
  u32x4 o = {pk(v[0], v[1]), pk(v[2], v[3]), pk(v[4], v[5]), pk(v[6], v[7])};
  if (isV) { if (r >= 28) { const unsigned f = r == 28 ? ONES2 : 0u; o = u32x4{f, f, f, f}; } }
  else if (s == 1 && h == 1) { o[2] = ONES2; o[3] = 0x00003F80u; }
  st16<16>(o, ws, lane * 16, kvo + (2 * isV + s) * KB);
  asm volatile("s_waitcnt vmcnt(0)" ::: "memory");
  if (lane < 8) st_tag(fkv + lane * 64 * 128 + (2 * isV + s) * 8, tag);
}

DI void phase0_h(const Params& p, const ldsp lds, u64 epo, u64& tag) {
  const int tid = threadIdx.x & 255, lane = tid & 63, r = lane & 31, h = lane >> 5;
  const int w = __builtin_amdgcn_readfirstlane(tid >> 6);
  const int b = blockIdx.x, xcd = b & 7, jj = b >> 3, rt = 8 * xcd + (jj >> 2), sub = jj & 3;
  const rsrc_t ws = mkrs(p.ws, WS_BYTES);
  const Ctx cx = {p.ws, lds + M_SIG + 512, lane, w};
  const ldsp priv = lds + P_KV + w * P_KVW;
  if (sub < 2) {
    const unsigned char* src = (const unsigned char*)(sub ? p.wv : p.wk) + w * P_KVW + lane * 16;
#pragma unroll
    for (int i = 0; i < 7; ++i) if (i < 6 || lane < 8) glds16(src + i * KB, (unsigned)(uintptr_t)priv + i * KB);
  }
  const int q8 = tid & 7, row8 = tid >> 3;
  u32x4 xv[7];
  {
    const unsigned short* xr = p.x + (size_t)(rt * 32 + row8) * D_ + q8 * 8;
#pragma unroll
    for (int i = 0; i < 7; ++i) xv[i] = *(const u32x4*)(xr + i * 64);
  }
  SBAR();
  const int tt = b >> 2, tks = (b & 3) * 7 + 2 * w, wog = b * 4 + w;
  u32x2 tv[4];
  unsigned te[8];
  const bool dead = (lane & 7) == 7;
  {
    const int pitch = tt < 16 ? 448 : 1344;
    const unsigned short* src = (tt < 16 ? p.wq + 28 * tt : p.wqkv + ((tt - 16) % 3) * 448 + ((tt - 16) / 3) * 28) + (size_t)(tks * 16 + (lane >> 3)) * pitch + (dead ? 0 : (lane & 7) * 4);
#pragma unroll
    for (int i = 0; i < 4; ++i) tv[i] = *(const u32x2*)(src + (size_t)((w == 3 && i >= 2) ? 0 : i * 8) * pitch);
    int g = wog < 896 ? wog : 0;
    const unsigned short* wo = g >= 448 ? p.wob : p.woa;
    if (g >= 448) g -= 448;
    const int ct = g >> 5, ks = g & 31, head = ks >> 1;
    const int k0 = head * 28 + 16 * (ks & 1) + 4 * h, kmax = head * 28 + 28;
#pragma unroll
    for (int j = 0; j < 8; ++j) {
      const int k = k0 + 8 * (j >> 2) + (j & 3);
      te[j] = wo[(k < kmax ? k : 0) * 448 + 32 * ct + r];
    }
  }
  SBAR();
  {
    float sm[2] = {}, sq[2] = {};
#pragma unroll
    for (int i = 0; i < 7; ++i)
#pragma unroll
      for (int d = 0; d < 4; ++d) { sm[d & 1] = dot2(xv[i][d], ONES2, sm[d & 1]); sq[d & 1] = dot2(xv[i][d], xv[i][d], sq[d & 1]); }
    float a = sm[0] + sm[1], c = sq[0] + sq[1];
#define DPP_ADD(v, ctl) v += __uint_as_float(__builtin_amdgcn_update_dpp(0u, __float_as_uint(v), ctl, 0xF, 0xF, true))
    DPP_ADD(a, 0xB1); DPP_ADD(c, 0xB1); DPP_ADD(a, 0x4E); DPP_ADD(c, 0x4E); DPP_ADD(a, 0x141); DPP_ADD(c, 0x141);
    const float mu = a * (1.f / D_), rs = __builtin_amdgcn_rsqf(__builtin_fmaxf(c * (1.f / D_) - mu * mu, 0.f) + EPS), nb = -mu * rs;
#pragma unroll
    for (int i = 0; i < 7; ++i) {
      unsigned y[4];
#pragma unroll
      for (int d = 0; d < 4; ++d) y[d] = pk(__builtin_fmaf(bflo(xv[i][d]), rs, nb), __builtin_fmaf(bfhi(xv[i][d]), rs, nb));
      const ldsp dst = lds + P_XI + (4 * i + (q8 >> 1)) * XI_KS + row8 * 16 + (q8 & 1) * 8;
      *(LAS u32x2*)dst = u32x2{y[0], y[1]};
      *(LAS u32x2*)(dst + 512) = u32x2{y[2], y[3]};
    }
  }
  lds_sig(cx, 0);
  tag = (((u64)__builtin_amdgcn_readfirstlane((unsigned)(epo >> 32)) << 32) | __builtin_amdgcn_readfirstlane((unsigned)epo)) + 1ull;
  const int i16 = lane & 15, trow = 4 * h + (i16 >> 2), tcol = ((lane >> 4) & 1) * 32 + (i16 & 3) * 8;
  if (sub < 2) {
    u32x4 wf[7];
#pragma unroll
    for (int i = 0; i < 7; ++i) wf[i] = tr_frag(priv + (16 * i + trow) * 56 + tcol, 8 * 56);
    lds_wait<0>(cx, 0, 4u);
    f32x16 acc = {};
#pragma unroll
    for (int i = 0; i < 7; ++i) { const u32x4 xf = lds_r16(lds + P_XI + (7 * w + i) * XI_KS + lane * 16); acc = sub ? MFMA(xf, wf[i], acc) : MFMA(wf[i], xf, acc); }
    part_write(priv + lane * 16, acc);
    lds_sig(cx, 1);
    if (w < 2) {
      lds_wait<0>(cx, 1, 4u);
      kv_finish<false>(lds + P_KV, P_KVW, sub, w, (ldsp)0, (ldsp)0, lane, ws, OFF_PAIR + R_KVA + rt * 4 * KB, p.ws + OFF_PAIR + R_FKV + rt * 128, tag);
    }
    if (rt == 0) {
      float cs = 0.f;
#pragma unroll
      for (int i = 0; i < 7; ++i) {
        st16<16>(wf[i], ws, lane * 16, OFF_WKV + (sub * 28 + 7 * w + i) * KB);
#pragma unroll
        for (int d = 0; d < 4; ++d) cs = dot2(wf[i][d], ONES2, cs);
      }
      cs = xhalf_add(cs);
      st4<16>(h ? 0u : __float_as_uint(cs), ws, lane * 4, OFF_CS + ((64 + sub) * 16 + 4 * w) * 128);
      st4<16>(0u, ws, lane * 4 + 256, OFF_CS + ((64 + sub) * 16 + 4 * w) * 128);
    }
  }
  {
    st4<16>(0u, ws, w == 0 ? h * PAIR_SZ + r * 4 : WS_BYTES, OFF_PAIR + R_FLG + b * 128);
    const ldsp img = priv + 4 * KB;
#pragma unroll
    for (int i = 0; i < 4; ++i) *(LAS u32x2*)(img + i * 512 + lane * 8) = dead ? u32x2{0u, 0u} : tv[i];
    CBAR();
    float cs = 0.f;
#pragma unroll
    for (int ks = 0; ks < 2; ++ks) {
      const u32x4 o = tr_frag(img + ks * KB + trow * 64 + tcol, 512);
      if (ks == 0 || w < 3) {
        st16<16>(o, ws, lane * 16, (tt * 28 + tks + ks) * KB);
#pragma unroll
        for (int i = 0; i < 4; ++i) cs = dot2(o[i], ONES2, cs);
      }
    }
    u32x4 o;
#pragma unroll
    for (int i = 0; i < 4; ++i) { asm volatile("" : "+v"(te[2 * i]), "+v"(te[2 * i + 1])); o[i] = te[2 * i] | (te[2 * i + 1] << 16); }
    if ((wog & 1) && h == 1) { o[2] = 0u; o[3] = 0u; }
    if (wog < 896) st16<16>(o, ws, lane * 16, OFF_WOA + wog * KB);
    cs = xhalf_add(cs);
    if (h == 0) st4<16>(__float_as_uint(cs), ws, r * 4, OFF_CS + wog * 128);
    asm volatile("s_waitcnt vmcnt(0)" ::: "memory");
    unsigned k = 0;
    if (lane == 0) k = __hip_atomic_fetch_add((LAS unsigned*)(cx.sig + 2 * 16), 1u, __ATOMIC_RELAXED, __HIP_MEMORY_SCOPE_WORKGROUP);
    if (__builtin_amdgcn_readfirstlane(k) == 3u && lane < 8) st_tag(p.ws + OFF_INI + (lane * 256 + b) * 8, tag);
  }
}
DI void phase0_c(const Params& p, const ldsp lds, u64 epo, u64& tag, u32x4& q0, u32x4& q1) {
  const int lane = threadIdx.x & 63, h = lane >> 5;
  const int w = __builtin_amdgcn_readfirstlane(threadIdx.x >> 6), sub = (blockIdx.x >> 3) & 3;
  const Ctx cx = {p.ws, lds + M_SIG + 512, lane, w};
  __builtin_amdgcn_s_sleep(PRO_DLY);
  if (lane < 56) {
    const unsigned short* src = p.wq + (size_t)(4 * w + lane / 14) * 448 + sub * 112 + (lane % 14) * 8;
#pragma unroll 4
    for (int i = 0; i < 28; ++i) glds16(src + (size_t)i * 16 * 448, (unsigned)(uintptr_t)(lds + P_WQ) + (i * 16 + 4 * w) * 224);
  }
  asm volatile("s_waitcnt vmcnt(0)" ::: "memory");
  tag = (((u64)__builtin_amdgcn_readfirstlane((unsigned)(epo >> 32)) << 32) | __builtin_amdgcn_readfirstlane((unsigned)epo)) + 1ull;
  lds_sig(cx, 3);
  lds_wait<1>(cx, 3, 4u); lds_wait<1>(cx, 0, 4u);
  const int i16 = lane & 15;
  const ldsp wb0 = opaque(lds + P_WQ + (4 * h + (i16 >> 2)) * 224 + w * 56 + ((lane >> 4) & 1) * 32 + (i16 & 3) * 8), wb1 = opaque(wb0 + 14 * 16 * 224);
  const ldsp xb = lds + P_XI + lane * 16;
  u32x4 wr[4], xr[4];
#define PRE_LD(i) do { wr[(i) & 3] = tr_frag(((i) < 14 ? wb0 : wb1) + ((i) % 14) * 16 * 224, 8 * 224); xr[(i) & 3] = lds_r16(xb + (i) * XI_KS); } while (0)
  PRE_LD(0); PRE_LD(1); PRE_LD(2); SBAR();
  f32x16 acc = {};
#pragma unroll
  for (int i = 0; i < 28; ++i) {
    if (i + 3 < 28) PRE_LD(i + 3);
    SBAR();
    acc = MFMA(wr[i & 3], xr[i & 3], acc);
    SBAR();
  }
  lds_sig(cx, 4);
#pragma unroll
  for (int i = 0; i < 16; ++i) acc[i] *= C2;
  q0 = pack8(acc, 0); q1 = pack8(acc, 1);
  lds_wait<0>(cx, 4, 4u);
}

__global__ void __launch_bounds__(512, 2) k_main(Params p) {
  __shared__ __attribute__((aligned(16))) char smem[L_BYTES];
  const ldsp lds = (ldsp)smem;
  const int tid = threadIdx.x;
  const int w8 = __builtin_amdgcn_readfirstlane(tid >> 6);
  const rsrc_t wt = mkrs(p.ws, OFF_PAIR);
  auto nop = []() {};
  const f32x16 Z = {};
  const u64 epo = ld_tag(p.ws + OFF_EPO);
  u64 tag;
  if (tid < 192) *(LAS unsigned*)(lds + M_SIG + tid * 4) = 0u;
  if (CODE_KB && w8 == 3) {
    const unsigned char* pc = (const unsigned char*)(__builtin_amdgcn_s_getpc() & ~127ull);
    const int ln = (((blockIdx.x >> 3) * 64 + (tid & 63)) % (CODE_KB * 8)) * 128;
    glds16(pc + ln, (unsigned)(uintptr_t)(lds + M_ST));
  }
  BAR();
#define PAIR_SETUP() \
    int lane = tid & 63, b = blockIdx.x, w = w8 & 3; \
    asm volatile("" : "+v"(lane), "+s"(b), "+s"(w)); \
    lane &= 63; b &= 255; w &= 3; \
    const int r = lane & 31, h = lane >> 5, l16 = lane * 16, xcd = b & 7, jj = b >> 3; \
    const int rt = 8 * xcd + (jj >> 2), sub = jj & 3;          \
    const int rb = 2 * xcd + (jj >> 4), hd = jj & 15;          \
    const ldsp scr = lds + L_SCR + l16; \
    unsigned char* const reg = p.ws + OFF_PAIR + pair * PAIR_SZ; \
    const rsrc_t ws = mkrs(reg, 2 * PAIR_SZ - pair * PAIR_SZ); \
    const Ctx cx = {reg + R_FLG, lds + M_SIG + pair * 256, lane, w}

  if (w8 < 4) {
    u32x4 qn0, qn1;
    unsigned kve = 0u;
    phase0_c(p, lds, epo, tag, qn0, qn1);
#pragma unroll 1
   for (int pair = 0; pair < 2; ++pair) {
    PAIR_SETUP();
    const unsigned char* const kpa = reg + R_FKV + (xcd * 64 + 4 * ((2 * xcd + 1 - (lane >> 2)) & 15) + w) * 128 + (lane & 3) * 8;
    u64 miss = 0ull, kvf;
    unsigned chk = pair, pend = 1u;
    int kvi = 0;
    auto kv_mask = [&]() { miss = __builtin_amdgcn_ballot_w64(kvf != tag); };
    auto kv_next = [&]() -> int {
      if (__builtin_expect(((unsigned)(miss >> (4 * kvi)) & 15u) != 0u, 0)) {
#pragma unroll 1
        for (int spin = 0; spin < SPIN_CAP; ++spin) {
          kvf = ld_tag(kpa);
          kv_mask();
          if (((unsigned)(miss >> (4 * kvi)) & 15u) == 0u) break;
          __builtin_amdgcn_s_sleep(2);
        }
        CBAR();
      }
      const int o = R_KVA + (4 * ((2 * xcd + 1 - kvi) & 15) + w) * 4 * KB;
      ++kvi;
      return o;
    };
    {
      u32x4 q[4][2], K[4][2], V[4][2];
      int kb[4];
      if (pair) {
        poll_wait(cx, ((((4 * (lane & 1) + w) << 2) | ((lane >> 1) & 1)) << 3) | xcd, 12, kve);
      } else {
#pragma unroll 1
        for (int spin = 0; spin < SPIN_CAP; ++spin) {
          if (((unsigned)__builtin_amdgcn_ballot_w64(ld_tag(kpa) != tag) & 0xFFu) == 0u) break;
          __builtin_amdgcn_s_sleep(2);
        }
        CBAR();
      }
#pragma unroll
      for (int t = 0; t < 2; ++t) {
        kb[t] = kv_next();
        K[t][0] = ld16<16>(ws, l16, kb[t]); K[t][1] = ld16<16>(ws, l16 + KB, kb[t]);
        V[t][0] = ld16<16>(ws, l16 + 2 * KB, kb[t]); V[t][1] = ld16<16>(ws, l16 + 3 * KB, kb[t]);
        SBAR();
      }
#pragma unroll
      for (int t = 2; t < 4; ++t) { kb[t] = kb[0]; K[t][0] = K[t][1] = V[t][0] = V[t][1] = u32x4{}; }
      kvf = ld_tag(kpa);
      f32x16 o0 = {}, o1 = {}, o2 = {}, o3 = {};
      PIN(o0); PIN(o1); PIN(o2); PIN(o3);
      if (h == 1) { qn1[2] = 0u; qn1[3] = 0u; }
      lds_w16(lds + L_QF + l16 + 2 * w * KB, qn0);
      if (pair) chk_handoff<2>(K[0], ws, l16, kb[0]);
      {
        f32x16 s0 = MFMA(K[0][0], qn0, Z);
        s0 = MFMA(K[0][1], qn1, s0);
        set_shift(qn1, xhalf_max(max16(s0, s0[0])), h, 0u);
        lds_w16(lds + L_QF + l16 + (2 * w + 1) * KB, qn1);
        lds_sig(cx, 7);
      }
      if (pair) chk_handoff<2>(V[0], ws, l16 + 2 * KB, kb[0]);
      lds_wait<0>(cx, 7, 4u);
#pragma unroll
      for (int a = 0; a < 4; ++a) { q[a][0] = lds_r16(lds + L_QF + l16 + 2 * a * KB); q[a][1] = lds_r16(lds + L_QF + l16 + (2 * a + 1) * KB); }
      f32x16 sa = MFMA(K[0][0], q[0][0], Z), sb;
      sa = MFMA(K[0][1], q[0][1], sa);
      u32x4 pp = {};
#define TILE(c, LD, LDV1) { \
        constexpr int n = (c + 1) & 3, pv = (c + 3) & 3; \
        auto ldV1 = [&]() { if (LDV1) V[pv][1] = ld16<16>(ws, l16 + 3 * KB, kb[pv]); }; \
        auto ldK = [&]() { if (LD) { kb[c] = kv_next(); K[c][0] = ld16<16>(ws, l16, kb[c]); K[c][1] = ld16<16>(ws, l16 + KB, kb[c]); } }; \
        auto ldV0 = [&]() { if (LD) V[c][0] = ld16<16>(ws, l16 + 2 * KB, kb[c]); }; \
        unit<true, true>(sa, sb, Z, o0, o3, pp, K[c][0], K[c][1], q[1][0], q[1][1], V[c][0], V[pv][1], ldV1, nop, nop); \
        unit<true, true>(sb, sa, Z, o1, o0, pp, K[c][0], K[c][1], q[2][0], q[2][1], V[c][0], V[c][1], nop, nop, nop); \
        if (LD && c == 0 && __builtin_expect(pend != 0u, 0)) {        \
          pend = 0u; kv_mask(); \
          _Pragma("unroll") for (int t = 2; t < 4; ++t) { kb[t] = kv_next(); K[t][0] = ld16<16>(ws, l16, kb[t]); K[t][1] = ld16<16>(ws, l16 + KB, kb[t]); \
            V[t][0] = ld16<16>(ws, l16 + 2 * KB, kb[t]); V[t][1] = ld16<16>(ws, l16 + 3 * KB, kb[t]); } } \
        unit<true, true>(sa, sb, Z, o2, o1, pp, K[c][0], K[c][1], q[3][0], q[3][1], V[c][0], V[c][1], nop, ldK, nop); \
        if (LD && c == 0 && __builtin_expect(chk != 0u, 0)) { chk = 0u; chk_handoff<2>(K[n], ws, l16, kb[n]); chk_handoff<2>(V[n], ws, l16 + 2 * KB, kb[n]); } \
        unit<true, true>(sb, sa, Z, o3, o2, pp, K[n][0], K[n][1], q[0][0], q[0][1], V[c][0], V[c][1], nop, nop, ldV0); }
#pragma unroll 1
      for (int tt = 0; tt < 12; tt += 4) {
        kv_mask(); kvf = ld_tag(kpa);
        TILE(0, true, true) TILE(1, true, true)
        kv_mask(); kvf = ld_tag(kpa);
        TILE(2, true, true) TILE(3, true, true)
      }
      TILE(0, false, true)
      lds_sig(cx, 11);
      lds_wait<1>(cx, 8, 4u);
      TILE(1, false, false)
      const ldsp red = scr + w * 4 * KB;
#define WR_LO(o, a) [&]() { lds_wf4(red + (a) * 16 * KB, f32x4{o[0], o[1], o[2], o[3]}); lds_wf4(red + (a) * 16 * KB + KB, f32x4{o[4], o[5], o[6], o[7]}); }
#define WR_HI(o, a) [&]() { lds_wf4(red + (a) * 16 * KB + 2 * KB, f32x4{o[8], o[9], o[10], o[11]}); lds_wf4(red + (a) * 16 * KB + 3 * KB, f32x4{o[12], o[13], o[14], o[15]}); lds_sig(cx, a); }
      unit<true, true>(sa, sb, Z, o0, o3, pp, K[3][0], K[3][1], q[0][0], q[0][1], V[2][0], V[1][1], nop, nop, nop);
      unit<true, true>(sb, sa, Z, o0, o0, pp, K[2][0], K[2][1], q[1][0], q[1][1], V[3][0], V[2][1], nop, nop, nop);
      unit<true, true>(sa, sb, Z, o1, o0, pp, K[3][0], K[3][1], q[1][0], q[1][1], V[2][0], V[3][1], nop, WR_LO(o0, 0), WR_HI(o0, 0));
      unit<true, true>(sb, sa, Z, o1, o1, pp, K[2][0], K[2][1], q[2][0], q[2][1], V[3][0], V[2][1], nop, nop, nop);
      unit<true, true>(sa, sb, Z, o2, o1, pp, K[3][0], K[3][1], q[2][0], q[2][1], V[2][0], V[3][1], nop, WR_LO(o1, 1), WR_HI(o1, 1));
      unit<true, true>(sb, sa, Z, o2, o2, pp, K[2][0], K[2][1], q[3][0], q[3][1], V[3][0], V[2][1], nop, nop, nop);
      unit<true, true>(sa, sb, Z, o3, o2, pp, K[3][0], K[3][1], q[3][0], q[3][1], V[2][0], V[3][1], nop, WR_LO(o2, 2), WR_HI(o2, 2));
      unit<false, true>(sb, sa, Z, o3, o3, pp, K[2][0], K[2][1], q[3][0], q[3][1], V[3][0], V[2][1], nop, nop, nop);
      o3 = MFMA(V[3][1], pp, o3);
      WR_LO(o3, 3)(); WR_HI(o3, 3)();
    }
    {
      u32x4 of[8] = {};
      const int src = R_OFA + (rt * 32 + 8 * w) * KB;
      if (w != sub) {
        poll_wait(cx, (((jj & ~3) | w) << 3) | xcd, 0);
        ld_handoff<8>(of, ws, l16, src);
      }
      BAR();
      const unsigned bad = __builtin_amdgcn_readfirstlane(*(LAS unsigned*)(cx.sig + 10 * 16));
      if (__builtin_expect((bad >> w) & 1u, 0)) {
        const u32x4 q0 = lds_r16(lds + L_QF + l16 + 2 * w * KB);
        u32x4 q1 = lds_r16(lds + L_QF + l16 + (2 * w + 1) * KB);
        if (h == 1) { q1[2] = 0; q1[3] = 0; }
        float m = -INFINITY;
#pragma unroll 1
        for (int t = 0; t < 64; ++t) {
          f32x16 s = MFMA(ld16<16>(ws, l16, R_KVA + t * 4 * KB), q0, Z);
          s = MFMA(ld16<16>(ws, l16 + KB, R_KVA + t * 4 * KB), q1, s);
          m = max16(s, m);
        }
        set_shift(q1, xhalf_max(m), h, 0u);
        f32x16 o = {};
#pragma unroll 1
        for (int t = 0; t < 64; ++t) {
          f32x16 s = MFMA(ld16<16>(ws, l16, R_KVA + t * 4 * KB), q0, Z);
          s = MFMA(ld16<16>(ws, l16 + KB, R_KVA + t * 4 * KB), q1, s);
          u32x4 p0, p1;
          exp_pack(s, p0, p1);
          o = MFMA(ld16<16>(ws, l16 + 2 * KB, R_KVA + t * 4 * KB), p0, o);
          o = MFMA(ld16<16>(ws, l16 + 3 * KB, R_KVA + t * 4 * KB), p1, o);
        }
        u32x4 y0, y1;
        norm_pack(o, h, y0, y1);
        st16<16>(y0, ws, l16, R_OFA + (rt * 32 + sub * 8 + 2 * w) * KB);
        st16<16>(y1, ws, l16 + KB, R_OFA + (rt * 32 + sub * 8 + 2 * w) * KB);
        lds_w16(scr + (sub * 8 + 2 * w) * KB, y0); lds_w16(scr + (sub * 8 + 2 * w + 1) * KB, y1);
      }
      if (w != sub) {
        chk_handoff<8>(of, ws, l16, src);
#pragma unroll
        for (int i = 0; i < 8; ++i) lds_w16(scr + (8 * w + i) * KB, of[i]);
      }
      BAR();
    }
    f32x4 ck[4], cq[4];
    const float cv = *(LAS float*)(lds + M_CS + 6 * 128 + r * 4);
#pragma unroll
    for (int g = 0; g < 4; ++g) { cq[g] = lds_rf4(lds + M_CS + 4 * 128 + (8 * g + 4 * h) * 4); ck[g] = lds_rf4(lds + M_CS + 5 * 128 + (8 * g + 4 * h) * 4); }
    BAR();
    u32x4 q0, q1, k0, k1, v0, v1;
    const Clk ck0 = clk_now();
    {
      const int xo = R_XF1 + (rb * 4 + w) * 28 * KB;
      const ldsp wb0 = opaque(lds + L_WB + l16), wb1 = opaque(lds + L_WB + 28 * KB + l16);
      const ldsp st = lds + M_ST + w * 256;
      f32x4 m4[4], r4[4];
      poll_wait(cx, (((jj & ~15) | (w << 2) | (lane & 3)) << 3) | xcd, 1);
      f32x16 aq, ak, av;
      float mu, rs;
      u32x4 xf[28], wk[3], wv[3], wq[4];
#define LDX(i) do { xf[i] = ld16<16>(ws, l16 + ((i) & 3) * KB, xo + ((i) >> 2) * 4 * KB); SBAR(); } while (0)
#define CHKX(i) do { if (__builtin_expect(__builtin_amdgcn_ballot_w64(smax(0u, xf[i]) == SENT) != 0ull, 0)) { \
          _Pragma("unroll 1") for (int spin = 0; spin < SPIN_CAP; ++spin) { CBAR(); LDX(i); if (__builtin_amdgcn_ballot_w64(smax(0u, xf[i]) == SENT) == 0ull) break; } } SBAR(); } while (0)
#define QST(i) do { if ((i) < 28) { SBAR(); aq = MFMA(wq[(i) & 3], xf[(i) < 28 ? (i) : 0], (i) ? aq : Z); SBAR(); if ((i) + 3 < 28) wq[((i) + 3) & 3] = lds_r16(wb0 + ((i) + 3) * KB); SBAR(); } } while (0)
#pragma unroll
      for (int i = 0; i < X1_AHEAD; ++i) LDX(i);
#pragma unroll
      for (int i = 0; i < 2; ++i) { wk[i] = lds_r16(wb1 + i * KB); wv[i] = lds_r16(wb1 + (28 + i) * KB); }
#pragma unroll
      for (int i = 0; i < 3; ++i) wq[i] = lds_r16(wb0 + i * KB);
      SBAR();
      CHKX(0);
#pragma unroll
      for (int ks = 0; ks < 28; ++ks) {
        ak = MFMA(wk[ks % 3], xf[ks], ks ? ak : Z); SBAR();
        if (ks + 2 < 28) { wk[(ks + 2) % 3] = lds_r16(wb1 + (ks + 2) * KB); wv[(ks + 2) % 3] = lds_r16(wb1 + (28 + ks + 2) * KB); }
        if (X1_AHEAD + 2 * ks < 28) LDX(X1_AHEAD + 2 * ks);
        if (ks == 25) {
          lds_wait<0>(cx, 12 + w, 1u);
          mu = *(LAS float*)(st + r * 4); rs = *(LAS float*)(st + 128 + r * 4);
        }
        if (ks == 26) {
#pragma unroll
          for (int g = 0; g < 4; ++g) { m4[g] = lds_rf4(st + (8 * g + 4 * h) * 4); r4[g] = lds_rf4(st + 128 + (8 * g + 4 * h) * 4); }
        }
        SBAR();
        av = MFMA(xf[ks], wv[ks % 3], ks ? av : Z); SBAR();
        if (X1_AHEAD + 2 * ks + 1 < 28) LDX(X1_AHEAD + 2 * ks + 1);
        if (ks < QS) QST(ks);
        if (ks + 1 < 28) CHKX(ks + 1);
        if (ks == POST_KV) post(cx, b, 4 + w, ck0, DT_KV);
      }
      const int ko = R_KVB + (hd * 64 + rb * 4 + w) * 4 * KB;
      QST(QS); QST(QS + 1);
#pragma unroll
      for (int g = 0; g < 4; ++g) {
#pragma unroll
        for (int i = 4 * g; i < 4 * g + 4; ++i) ak[i] = (ak[i] - mu * ck[g][i & 3]) * rs;
        unsigned a0 = pk(ak[4 * g], ak[4 * g + 1]), a1 = pk(ak[4 * g + 2], ak[4 * g + 3]);
        if (g == 3 && h == 1) { a0 = ONES2; a1 = 0x00003F80u; }
        PIN(a0); PIN(a1);
        if (g < 2) { k0[2 * g] = a0; k0[2 * g + 1] = a1; } else { k1[2 * g - 4] = a0; k1[2 * g - 3] = a1; }
        if (g == 1) st16<16>(k0, ws, l16, ko);
        if (g == 3) st16<16>(k1, ws, l16 + KB, ko);
        QST(QS + 2 + g);
      }
      const unsigned vpad = r == 28 ? ONES2 : 0u;
#pragma unroll
      for (int g = 0; g < 4; ++g) {
#pragma unroll
        for (int i = 4 * g; i < 4 * g + 4; ++i) av[i] = (av[i] - m4[g][i & 3] * cv) * r4[g][i & 3];
        unsigned a0 = pk(av[4 * g], av[4 * g + 1]), a1 = pk(av[4 * g + 2], av[4 * g + 3]);
        if (r >= 28) { a0 = vpad; a1 = vpad; }
        PIN(a0); PIN(a1);
        if (g < 2) { v0[2 * g] = a0; v0[2 * g + 1] = a1; } else { v1[2 * g - 4] = a0; v1[2 * g - 3] = a1; }
        if (g == 1) st16<16>(v0, ws, l16 + 2 * KB, ko);
        if (g == 3) st16<16>(v1, ws, l16 + 3 * KB, ko);
        QST(QS + 6 + g);
      }
      lds_w16(scr + (32 + 4 * w) * KB, k0); QST(QS + 10); lds_w16(scr + (33 + 4 * w) * KB, k1); QST(QS + 11);
      lds_w16(scr + (34 + 4 * w) * KB, v0); QST(QS + 12); lds_w16(scr + (35 + 4 * w) * KB, v1);
      lds_sig(cx, 4);
#pragma unroll
      for (int i = QS + 13; i < 28; ++i) QST(i);
      const float rq = rs * C2;
#pragma unroll
      for (int i = 0; i < 16; ++i) aq[i] = (aq[i] - mu * cq[i >> 2][i & 3]) * rq;
      q0 = pack8(aq, 0); q1 = pack8(aq, 1);
      if (h == 1) { q1[2] = 0; q1[3] = NEG31; }
    }
    {
      f32x16 cd, cf;
#pragma unroll
      for (int i = 0; i < 16; ++i) {
        const int kk = (i & 3) + 8 * (i >> 2) + 4 * h;
        cd[i] = kk <= r ? 0.f : -1e30f; cf[i] = kk <= r ? -1e30f : 0.f;
      }
      f32x16 sa = MFMA(k0, q0, cd), sb;
      sa = MFMA(k1, q1, sa);
      set_shift(q1, xhalf_max(max16(sa, sa[0])), h, NEG31);
      const ldsp kp = scr + (8 + w) * 4 * KB;
      auto need = [&](int u) {
        const int ti = 8 + w - u;
        if (u == 1 && w > 0) lds_wait<0>(cx, 4, 4u);
        if (__builtin_expect((ti & 3) == 3, 0)) lds_wait<1>(cx, 5 + (ti >> 2), 2u);
      };
      u32x4 K[2][2], V[2][2], pp;
      f32x16 o = {};
      sa = MFMA(k0, q0, cd); sa = MFMA(k1, q1, sa);
      V[0][0] = v0; V[0][1] = v1;
      need(1);
      K[1][0] = lds_r16(kp - 4 * KB); K[1][1] = lds_r16(kp - 3 * KB); V[1][0] = lds_r16(kp - 2 * KB);
#pragma unroll
      for (int u = 0; u < 9; ++u) {
        const int c = u & 1, n = c ^ 1;
        auto ha = [&]() {
          if (u + 1 < 9) V[n][1] = lds_r16(kp - (u + 1) * 4 * KB + 3 * KB);
          if (u + 2 < 9) { need(u + 2); K[c][0] = lds_r16(kp - (u + 2) * 4 * KB); K[c][1] = lds_r16(kp - (u + 2) * 4 * KB + KB); }
        };
        auto hc = [&]() { if (u + 2 < 9) V[c][0] = lds_r16(kp - (u + 2) * 4 * KB + 2 * KB); if (u >= 4 && u <= 6 && __builtin_expect(u == (w < 2 ? 4 : 3 + w), 0)) post(cx, b, 8 + w, ck0, DT_OB + (8 - u) * DT_UNIT); };
        f32x16& s = c ? sb : sa; f32x16& sn = c ? sa : sb;
        if (u == 0) unit<true, false>(s, sn, Z, o, o, pp, K[n][0], K[n][1], q0, q1, V[c][0], V[n][1], ha, nop, hc);
        else if (u < 7) unit<true, true>(s, sn, Z, o, o, pp, K[n][0], K[n][1], q0, q1, V[c][0], V[n][1], ha, nop, hc);
        else if (u == 7) unit<true, true>(s, sn, cf, o, o, pp, K[n][0], K[n][1], q0, q1, V[c][0], V[n][1], ha, nop, hc);
        else unit<false, true>(s, sn, Z, o, o, pp, K[n][0], K[n][1], q0, q1, V[c][0], V[n][1], ha, nop, hc);
      }
      o = MFMA(V[0][1], pp, o);
      if (__builtin_expect(__builtin_amdgcn_ballot_w64(h == 1 && !(o[12] < LBIG)) != 0ull, 0)) {
        if (h == 1) { q1[2] = 0; q1[3] = NEG31; }
        float m = -INFINITY;
#pragma unroll 1
        for (int u = 0; u < 9; ++u) {
          f32x16 s;
#pragma unroll
          for (int i = 0; i < 16; ++i) s[i] = u == 0 ? cd[i] : u == 8 ? cf[i] : 0.f;
          s = MFMA(lds_r16(kp - u * 4 * KB), q0, s); s = MFMA(lds_r16(kp - u * 4 * KB + KB), q1, s);
          m = max16(s, m);
        }
        set_shift(q1, xhalf_max(m), h, NEG31);
        o = Z;
#pragma unroll 1
        for (int u = 0; u < 9; ++u) {
          f32x16 s;
#pragma unroll
          for (int i = 0; i < 16; ++i) s[i] = u == 0 ? cd[i] : u == 8 ? cf[i] : 0.f;
          s = MFMA(lds_r16(kp - u * 4 * KB), q0, s); s = MFMA(lds_r16(kp - u * 4 * KB + KB), q1, s);
          u32x4 p0, p1;
          exp_pack(s, p0, p1);
          o = MFMA(lds_r16(kp - u * 4 * KB + 2 * KB), p0, o); o = MFMA(lds_r16(kp - u * 4 * KB + 3 * KB), p1, o);
        }
      }
      u32x4 y0, y1;
      norm_pack(o, h, y0, y1);
      const int oo = R_OFB + ((rb * 4 + w) * 16 + hd) * 2 * KB;
      st16<16>(y0, ws, l16, oo); st16<16>(y1, ws, l16 + KB, oo);
      lds_sig(cx, 9);
    }
    u32x4 wa1[WA1];
    {
      u32x4 of[8];
      const int src = R_OFB + (rt * 32 + 8 * w) * KB;
#pragma unroll
      for (int i = 0; i < WA1; ++i) { wa1[i] = ld16<16>(wt, l16 + (i & 3) * KB, pair ? OFF_PAIR : OFF_WAT + ((4 * sub + w) * 28 + (i & ~3)) * KB); SBAR(); }
      poll_wait(cx, (((jj & ~15) | (w << 2) | (lane & 3)) << 3) | xcd, 8 + (rt & 3));
      ld_handoff<8>(of, ws, l16, src);
      chk_handoff<8>(of, ws, l16, src);
      lds_wait<1>(cx, 9, 4u);
#pragma unroll
      for (int i = 0; i < 8; ++i) lds_w16(scr + (8 * w + i) * KB, of[i]);
      BAR();
    }
    if (pair == 0) {
      u32x4 wa[28];
#pragma unroll
      for (int i = 0; i < WA1; ++i) wa[i] = wa1[i];
#pragma unroll
      for (int i = WA1; i < 28; ++i) { wa[i] = ld16<16>(wt, l16 + (i & 3) * KB, OFF_WAT + ((4 * sub + w) * 28 + (i & ~3)) * KB); SBAR(); }
      f32x4 cq[4];
#pragma unroll
      for (int g = 0; g < 4; ++g) cq[g] = lds_rf4(lds + M_CS + w * 128 + (8 * g + 4 * h) * 4);
      if (w >= 2) poll_wait<2>(cx, (((jj & ~3) | (lane & 3)) << 3) | xcd, 2);
      if (w == 3) stats_gather(ws, R_ST2 + rt * 14 * 256, lds + M_ST + 4 * 256, lane);
      if (w == 2 && sub < 2) {
        const unsigned t = eta(ck0, DT_Q);
        if (lane == 0) __hip_atomic_store((GAS unsigned*)(cx.flg + PAIR_SZ + b * 128 + 12 * 4), t, __ATOMIC_RELAXED, __HIP_MEMORY_SCOPE_AGENT);
      }
      BAR();
      if (sub < 2 && w < 2) __builtin_amdgcn_s_sleep(4);
      const float mu = *(LAS float*)(lds + M_ST + 4 * 256 + r * 4), rq = *(LAS float*)(lds + M_ST + 4 * 256 + 128 + r * 4) * C2;
      f32x16 acc = gemm_lds<28, false>(wa, scr + 32 * KB, [&](int i) { if (i == 8) kve = flag_ld(cx, PAIR_SZ / 128 + (((((4 * (lane & 1) + w) << 2) | ((lane >> 1) & 1)) << 3) | xcd), 12); });
#pragma unroll
      for (int i = 0; i < 16; ++i) acc[i] = (acc[i] - mu * cq[i >> 2][i & 3]) * rq;
      qn0 = pack8(acc, 0); qn1 = pack8(acc, 1);
    }
   }
   return;
  }

  __builtin_amdgcn_s_setprio(2);
  phase0_h(p, lds, epo, tag);
  u32x4 x0 = {}, x1 = {};
#pragma unroll 1
 for (int pair = 0; pair < 2; ++pair) {
  PAIR_SETUP();
  __builtin_amdgcn_s_setprio(HPRIO);
  const int ct0 = sub < 2 ? 4 * sub : 3 * sub + 2, nct = sub < 2 ? 4 : 3;
  const bool ctok = w < nct;
  const int ct = ct0 + (ctok ? w : 0);
  const int xo1 = R_XF1 + (rt * 28 + ct * 2) * KB, xo2 = R_XF2 + (rt * 28 + ct * 2) * KB;
  const int so1 = R_ST1 + (rt * 14 + ct) * 256, so2 = R_ST2 + (rt * 14 + ct) * 256;
  lds_wait<4>(cx, 7, 4u);
  __builtin_amdgcn_s_setprio(HPRIO);
  u32x4 wf[32];
  Clk ck0 = clk_now();
  {
    const u32x4 sv = {SENT, SENT, SENT, SENT};
    auto fill = [&](int i) {
      const int hi = (i & 1) * KB;
      if (i < 2) st16<16>(sv, ws, l16 + hi, R_OFA + (rt * 32 + sub * 8 + 2 * w) * KB);
      else if (i < 4) { if (ctok) { st16<16>(sv, ws, l16 + hi, xo1); if (i == 2) st4<16>(SENT, ws, lane * 4, so1); } }
      else if (i < 8) st16<16>(sv, ws, l16 + (i - 4) * KB, R_KVB + (hd * 64 + rb * 4 + w) * 4 * KB);
      else if (i < 10) st16<16>(sv, ws, l16 + hi, R_OFB + ((rb * 4 + w) * 16 + hd) * 2 * KB);
      else if (i < 12) { if (ctok && pair == 0) { st16<16>(sv, ws, l16 + hi, xo2); if (i == 10) st4<16>(SENT, ws, lane * 4, so2); } }
      else if (i == 12 && pair == 0 && sub < 2 && w < 2) st16<16>(sv, ws, l16, PAIR_SZ + R_KVA + (rt * 4 + 2 * sub + w) * KB);
      SBAR();
    };
#pragma unroll
    for (int i = 0; i < NFILL0; ++i) { fill(i); __builtin_amdgcn_s_sleep(PACEF); }
    if (pair == 0) {
#pragma unroll 1
      for (int spin = 0; spin < SPIN_CAP; ++spin) {
        bool ok = true;
#pragma unroll
        for (int i = 0; i < 4; ++i) ok &= ld_tag(p.ws + OFF_INI + (xcd * 256 + i * 64 + lane) * 8) == tag;
        if (__builtin_amdgcn_ballot_w64(!ok) == 0ull) break;
        __builtin_amdgcn_s_sleep(8);
      }
      CBAR();
    }
    unsigned c4[16] = {};
    if (pair == 0) {
      const unsigned short* xr = p.x + (size_t)(rt * 32 + r) * D_ + ct * 32 + 4 * h;
      const u32x2 a0 = *(const u32x2*)xr, a1 = *(const u32x2*)(xr + 8), a2 = *(const u32x2*)(xr + 16), a3 = *(const u32x2*)(xr + 24);
      x0 = u32x4{a0[0], a0[1], a1[0], a1[1]}; x1 = u32x4{a2[0], a2[1], a3[0], a3[1]};
      const int co = (h ? (w < 3 ? 16 + hd * 3 + w : 64 + (sub & 1)) : 4 * sub + w) * 2048 + r * 4;
#pragma unroll
      for (int i = 0; i < 16; ++i) c4[i] = ld4<16>(wt, co + i * 128, OFF_CS);
      SBAR();
    }
    const unsigned char* wbsrc = p.ws + OFF_WBT + (hd * 84 + w * 21) * KB + l16;
    const unsigned wbdst = (unsigned)(uintptr_t)(lds + L_WB) + w * 21 * KB;
#pragma unroll
    for (int i = 0; i < 32; ++i) {
      wf[i] = ld16<16>(wt, l16 + (i & 3) * KB, OFF_WOA + (ct * 32 + (i & ~3)) * KB); SBAR();
      if (i % 3 != 2 || i > 29) { const int g = i - (i + 1) / 3; if (g < 21 && pair == 0) glds16_sc1(wbsrc + g * KB, wbdst + g * KB); }
      if ((i & 1) == 0 && NFILL0 + (i >> 1) < 13) fill(NFILL0 + (i >> 1));
      if (i == 10) { asm volatile("s_waitcnt vmcnt(5)" ::: "memory"); lds_sig(cx, 8); }
      if (i % PGRP == PGRP - 1) { SBAR(); if (pair) __builtin_amdgcn_s_sleep(PACE3); else __builtin_amdgcn_s_sleep(PACE); SBAR(); }
    }
    if (pair == 0) {
      float t = 0.f;
#pragma unroll
      for (int i = 0; i < 16; ++i) t += __uint_as_float(c4[i]);
      *(LAS float*)(lds + M_CS + (4 * h + w) * 128 + r * 4) = t;
    }
    asm volatile("s_waitcnt vmcnt(0)" ::: "memory");
  }
  {
    u32x4 keep[2];
    const int s = w & 1;
    __builtin_amdgcn_s_setprio(2);
    lds_wait<2>(cx, 8, 4u);
#pragma unroll 1
    for (int spin = 0; spin < SPIN_CAP; ++spin) {
      if (__builtin_amdgcn_readfirstlane(*(volatile LAS unsigned*)(cx.sig + 11 * 16 + w * 4))) break;
      __builtin_amdgcn_s_sleep(DOZE);
    }
#pragma unroll
    for (int it = 0; it < 2; ++it) {
      const int T = 2 * it + (w >> 1);
      if (it == 0) lds_wait<2>(cx, T, 4u); else lds_wait<1>(cx, T, 4u);
      if (it == 0) { if (w == POST_OA) post(cx, b, 0, ck0, DT_OA); ck0 = clk_now(); }

      const ldsp red = scr + T * 16 * KB;
      f32x4 a0[4], a1[4]; float l4[4];
#pragma unroll
      for (int k = 0; k < 4; ++k) {
        l4[k] = *(LAS float*)(lds + L_SCR + T * 16 * KB + k * 4 * KB + 3 * KB + (32 + r) * 16);
        a0[k] = lds_rf4(red + k * 4 * KB + (2 * s) * KB);
        a1[k] = lds_rf4(red + k * 4 * KB + (2 * s + 1) * KB);
      }
      const float l = (l4[0] + l4[1]) + (l4[2] + l4[3]);
      const f32x4 b0 = (a0[0] + a0[1]) + (a0[2] + a0[3]), b1 = (a1[0] + a1[1]) + (a1[2] + a1[3]);
      const float inv = __builtin_amdgcn_rcpf(l);
      u32x4 o = {pk(b0[0] * inv, b0[1] * inv), pk(b0[2] * inv, b0[3] * inv), pk(b1[0] * inv, b1[1] * inv), pk(b1[2] * inv, b1[3] * inv)};
      if (s == 1 && h == 1) { o[2] = 0; o[3] = 0; }
      if (__builtin_expect(__builtin_amdgcn_ballot_w64(!(l < LBIG)) == 0ull, 1)) st16<16>(o, ws, l16, R_OFA + (rt * 32 + sub * 8 + 2 * T + s) * KB);
      else if (lane == 0) __hip_atomic_fetch_or((LAS unsigned*)(cx.sig + 10 * 16), 1u << T, __ATOMIC_RELAXED, __HIP_MEMORY_SCOPE_WORKGROUP);
      keep[it] = o;
    }
    BAR();
    const unsigned bad = __builtin_amdgcn_readfirstlane(*(LAS unsigned*)(cx.sig + 10 * 16));
#pragma unroll
    for (int it = 0; it < 2; ++it) {
      const int T = 2 * it + (w >> 1);
      if (!((bad >> T) & 1u)) lds_w16(scr + (sub * 8 + 2 * T + s) * KB, keep[it]);
    }
    BAR();
  }
  {
    const f32x16 acc = gemm_lds<32, false>(wf, scr, [&](int i) { if (i == 0 && w == 0) post(cx, b, 1, ck0, DT_X); if (i == 24) BAR();   });
    add_res(acc, x0, x1, x0, x1);
    if (ctok) { st16<16>(x0, ws, l16, xo1); st16<16>(x1, ws, l16 + KB, xo1); SBAR(); st4<16>(__float_as_uint(row_stats(x0, x1, h)), ws, lane * 4, so1); }
  }
  u32x4 wkv[7];
  {
    __builtin_amdgcn_s_setprio(0);
    poll_wait<2>(cx, (((jj & ~15) | (w << 2) | (lane & 3)) << 3) | xcd, 1);
    __builtin_amdgcn_s_sleep(ST_DLY);
    stats_gather(ws, R_ST1 + (rb * 4 + w) * 14 * 256, lds + M_ST + w * 256, lane);
    lds_sig(cx, 12 + w);
    const int kt = 4 * rb - 8 + 2 * w;
    u32x4 hv[8];
    if (kt >= 0) {
      const int prb = kt >> 2;
      __builtin_amdgcn_s_setprio(0);
      poll_wait<HALO_SLP>(cx, (((prb & 1) * 16 + hd) << 3) | (prb >> 1), 4 + (kt & 3) + (lane & 1));
      __builtin_amdgcn_s_setprio(2);
      ld_handoff<8>(hv, ws, l16, R_KVB + (hd * 64 + kt) * 4 * KB);
      chk_handoff<8>(hv, ws, l16, R_KVB + (hd * 64 + kt) * 4 * KB);
    } else {
#pragma unroll
      for (int i = 0; i < 8; ++i) hv[i] = u32x4{0u, 0u, 0u, ((i & 3) == 1 && h == 1) ? 0x3F800000u : 0u};
    }
#pragma unroll
    for (int i = 0; i < 8; ++i) lds_w16(scr + (8 * w + i) * KB, hv[i]);
    lds_sig(cx, 5 + (w >> 1));
    __builtin_amdgcn_s_setprio(0);
#pragma unroll
    for (int i = 0; i < 32; ++i) { wf[i] = ld16<16>(wt, l16 + (i & 3) * KB, OFF_WOB + (ct * 32 + (i & ~3)) * KB); SBAR(); }
#pragma unroll
    for (int i = 0; i < 7; ++i) wkv[i] = ld16<16>(wt, l16 + (i & 3) * KB, OFF_WKV + ((sub & 1) * 28 + 7 * w + (i & 4)) * KB);
    asm volatile("s_waitcnt vmcnt(0)" ::: "memory");
    BAR();
  }
  __builtin_amdgcn_s_setprio(2);
  {
    const f32x16 acc = gemm_lds<32, false>(wf, scr, [&](int i) { if (i == 0 && w == 0 && pair == 0) post(cx, b, 2, ck0, DT_X); if (i == 31 && pair == 0) lds_sig(cx, 32 + 5); });
    add_res(acc, x0, x1, x0, x1);
    if (pair == 1) {
      if (ctok) {
        unsigned short* orow = p.out + (size_t)(rt * 32 + r) * D_ + ct * 32 + 4 * h;
        *(u32x2*)orow = u32x2{x0[0], x0[1]}; *(u32x2*)(orow + 8) = u32x2{x0[2], x0[3]};
        *(u32x2*)(orow + 16) = u32x2{x1[0], x1[1]}; *(u32x2*)(orow + 24) = u32x2{x1[2], x1[3]};
      }
      if (b == 0 && w == 0 && lane == 0) st_tag(p.ws + OFF_EPO, tag);
      return;
    }
    if (ctok) { st16<16>(x0, ws, l16, xo2); st16<16>(x1, ws, l16 + KB, xo2); SBAR(); st4<16>(__float_as_uint(row_stats(x0, x1, h)), ws, lane * 4, so2); }
  }
  {
    u32x4 xf[7];
    const int src = R_XF2 + (rt * 28 + 7 * w) * KB;
    lds_wait<0>(cx, 32 + 5, 4u);
    poll_wait(cx, (((jj & ~3) | (lane & 3)) << 3) | xcd, 2);
    ld_handoff<7>(xf, ws, l16, src);
    chk_handoff<7>(xf, ws, l16, src);
    if (sub < 2) {
      f32x16 acc = {};
      if (sub) {
#pragma unroll
        for (int i = 0; i < 7; ++i) { acc = MFMA(xf[i], wkv[i], acc); SBAR(); lds_w16(scr + (32 + 7 * w + i) * KB, xf[i]); SBAR(); }
      } else {
#pragma unroll
        for (int i = 0; i < 7; ++i) { acc = MFMA(wkv[i], xf[i], acc); SBAR(); lds_w16(scr + (32 + 7 * w + i) * KB, xf[i]); SBAR(); }
      }
      part_write(scr + w * 4 * KB, acc);
    } else {
#pragma unroll
      for (int i = 0; i < 7; ++i) lds_w16(scr + (32 + 7 * w + i) * KB, xf[i]);
    }
    BAR();
    if (sub < 2 && w < 2) kv_finish<true>(lds + L_SCR, 4 * KB, sub, w, lds + M_ST + 4 * 256, lds + M_CS + 7 * 128, lane, ws, PAIR_SZ + R_KVA + rt * 4 * KB, reg + PAIR_SZ + R_FKV + rt * 128, tag);
  }
 }
}

extern "C" void kernel_launch(void* const* d_in, const int* in_sizes, int n_in, void* d_out, int out_size, void* d_ws, size_t ws_size,
                              hipStream_t stream) {
  Params p;
  p.x = (const unsigned short*)d_in[0];
  p.wq = (const unsigned short*)d_in[1];
  p.wk = (const unsigned short*)d_in[2];
  p.wv = (const unsigned short*)d_in[3];
  p.woa = (const unsigned short*)d_in[4];
  p.wqkv = (const unsigned short*)d_in[6];
  p.wob = (const unsigned short*)d_in[7];
  p.out = (unsigned short*)d_out;
  p.ws = (unsigned char*)d_ws;
  k_main<<<256, 512, 0, stream>>>(p);
}
```

```cpp
#include <hip/hip_runtime.h>
#include <hip/hip_bf16.h>
#include <stdint.h>

typedef short bf16x8 __attribute__((ext_vector_type(8)));
typedef short s16x4 __attribute__((ext_vector_type(4)));
typedef float f32x16 __attribute__((ext_vector_type(16)));
typedef float f32x4 __attribute__((ext_vector_type(4)));
typedef float f32x2 __attribute__((ext_vector_type(2)));
typedef unsigned u32x4 __attribute__((ext_vector_type(4)));
typedef unsigned u32x2 __attribute__((ext_vector_type(2)));
typedef __bf16 bf16x2_t __attribute__((ext_vector_type(2)));
#define DI __device__ __forceinline__
#define LAS __attribute__((address_space(3)))
#define GAS __attribute__((address_space(1)))
typedef LAS char* ldsp;

#define MFMA(a, b, c) __builtin_amdgcn_mfma_f32_32x32x16_bf16(__builtin_bit_cast(bf16x8, (a)), __builtin_bit_cast(bf16x8, (b)), (c), 0, 0, 0)
#define BAR() asm volatile("s_waitcnt lgkmcnt(0)\n\ts_barrier" ::: "memory")
#define CBAR() asm volatile("" ::: "memory")
#define SBAR() __builtin_amdgcn_sched_barrier(0)

constexpr int D_ = 448;
constexpr float C2 = 0.18898223650461362f * 1.4426950408889634f;
constexpr float EPS = 1e-5f;
constexpr unsigned SENT = 0xFFFFFFFFu;
constexpr unsigned ONES2 = 0x3F803F80u;
constexpr int SPIN_CAP = 1 << 16;

constexpr int KB = 1024;
constexpr int OFF_WAT = 0;
constexpr int OFF_WBT = OFF_WAT + 448 * KB;
constexpr int OFF_WKV = OFF_WBT + 1344 * KB;
constexpr int OFF_WOA = OFF_WKV + 56 * KB;
constexpr int OFF_WOB = OFF_WOA + 448 * KB;
constexpr int OFF_CS = OFF_WOB + 448 * KB;
constexpr int OFF_EPO = OFF_CS + 132 * KB;
constexpr int OFF_INI = OFF_EPO + KB;
constexpr int OFF_PAIR = OFF_INI + 16 * KB;
constexpr int QA_SZ = 64 * 28 * KB;
constexpr int R_KVA = 0;
constexpr int R_OFA = R_KVA + 256 * KB;
constexpr int R_XF1 = R_OFA + 2048 * KB;
constexpr int R_KVB = R_XF1 + QA_SZ;
constexpr int R_OFB = R_KVB + 4096 * KB;
constexpr int R_XF2 = R_OFB + 2048 * KB;
constexpr int R_ST1 = R_XF2 + QA_SZ;
constexpr int R_ST2 = R_ST1 + 64 * 14 * 256;
constexpr int R_FLG = R_ST2 + 64 * 14 * 256;
constexpr int R_FKV = R_FLG + 32 * KB;
constexpr int PAIR_SZ = R_FKV + 64 * KB;
constexpr int WS_BYTES = OFF_PAIR + 2 * PAIR_SZ;

constexpr int PTRUNC = 1, FSKIP = 1, KVE_AT = 8, HPRIO = 0, DT_OA = 1900, DT_X = 1850, DT_KV = 3700, DT_OB = 850;
constexpr int DT_UNIT = 240, X1_AHEAD = 8, WA1 = 8, PRO_DLY = 2, PACE3 = 8, DT_Q = 3000, QS = 14, ST_DLY = 20;
constexpr int CODE_KB = 44, PGRP = 2, DOZE = 14, NFILL0 = 6, PACEF = 2, HALO_SLP = 8, PACE = 3, POST_OA = 2;
constexpr int POST_KV = 12;

DI unsigned pk(float a, float b) { f32x2 v = {a, b}; bf16x2_t r = __builtin_convertvector(v, bf16x2_t); return __builtin_bit_cast(unsigned, r); }
DI unsigned pkt(float a, float b) { return PTRUNC ? __builtin_amdgcn_perm(__float_as_uint(b), __float_as_uint(a), 0x07060302u) : pk(a, b); }
DI float dot2(unsigned a, unsigned b, float c) { return __builtin_amdgcn_fdot2_f32_bf16(__builtin_bit_cast(bf16x2_t, a), __builtin_bit_cast(bf16x2_t, b), c, false); }
DI float bflo(unsigned u) { return __uint_as_float(u << 16); }
DI float bfhi(unsigned u) { return __uint_as_float(u & 0xFFFF0000u); }
DI float xhalf_add(float v) { auto r = __builtin_amdgcn_permlane32_swap(__float_as_uint(v), __float_as_uint(v), false, false); return __uint_as_float(r[0]) + __uint_as_float(r[1]); }
DI float xhalf_max(float v) { auto r = __builtin_amdgcn_permlane32_swap(__float_as_uint(v), __float_as_uint(v), false, false); return __builtin_fmaxf(__uint_as_float(r[0]), __uint_as_float(r[1])); }
DI float from_hi(float v) { auto r = __builtin_amdgcn_permlane32_swap(__float_as_uint(v), __float_as_uint(v), false, false); return __uint_as_float(r[1]); }
DI float max3f(float a, float b, float c) { return __builtin_fmaxf(__builtin_fmaxf(a, b), c); }
DI float max16(const f32x16& s, float m) {
#pragma unroll
  for (int i = 0; i < 16; i += 2) m = max3f(m, s[i], s[i + 1]);
  return m;
}
typedef __amdgpu_buffer_rsrc_t rsrc_t;
DI rsrc_t mkrs(const void* p, unsigned n) { return __builtin_amdgcn_make_buffer_rsrc((void*)p, 0, n, 0x00020000); }
template <int AUX> DI u32x4 ld16(rsrc_t rs, int voff, int soff) { return __builtin_amdgcn_raw_buffer_load_b128(rs, voff, soff, AUX); }
template <int AUX> DI void st16(u32x4 v, rsrc_t rs, int voff, int soff) { __builtin_amdgcn_raw_buffer_store_b128(v, rs, voff, soff, AUX); }
template <int AUX> DI unsigned ld4(rsrc_t rs, int voff, int soff) { return __builtin_amdgcn_raw_buffer_load_b32(rs, voff, soff, AUX); }
template <int AUX> DI void st4(unsigned v, rsrc_t rs, int voff, int soff) { __builtin_amdgcn_raw_buffer_store_b32(v, rs, voff, soff, AUX); }
DI ldsp opaque(ldsp p) { unsigned a = (unsigned)(uintptr_t)p; asm volatile("" : "+v"(a)); return (ldsp)(uintptr_t)a; }
typedef unsigned long long u64;
DI u64 ld_tag(const unsigned char* a) { return __hip_atomic_load((GAS u64*)a, __ATOMIC_RELAXED, __HIP_MEMORY_SCOPE_AGENT); }
DI void st_tag(unsigned char* a, u64 v) { __hip_atomic_store((GAS u64*)a, v, __ATOMIC_RELAXED, __HIP_MEMORY_SCOPE_AGENT); }
DI u32x4 lds_r16(ldsp p) { return *(LAS u32x4*)p; }
DI f32x4 lds_rf4(ldsp p) { return *(LAS f32x4*)p; }
DI void lds_w16(ldsp p, u32x4 v) { *(LAS u32x4*)p = v; }
DI void lds_wf4(ldsp p, f32x4 v) { *(LAS f32x4*)p = v; }
DI unsigned umax3(unsigned a, unsigned b, unsigned c) { return max(max(a, b), c); }
DI unsigned smax(unsigned m, u32x4 v) { return umax3(umax3(m, v[0], v[1]), v[2], v[3]); }
DI void glds16_sc1(const void* g, unsigned lds_base) {
  unsigned sv;
  asm volatile("s_mov_b32 %0, m0\n\ts_mov_b32 m0, %2\n\ts_nop 0\n\tglobal_load_lds_dwordx4 %1, off sc1\n\ts_mov_b32 m0, %0" : "=&s"(sv) : "v"(g), "s"(lds_base) : "memory");
}
DI void glds16(const void* g, unsigned lds_base) {
  unsigned sv;
  asm volatile("s_mov_b32 %0, m0\n\ts_mov_b32 m0, %2\n\ts_nop 0\n\tglobal_load_lds_dwordx4 %1, off\n\ts_mov_b32 m0, %0" : "=&s"(sv) : "v"(g), "s"(lds_base) : "memory");
}
DI u32x4 pack8(const f32x16& a, int s) {
  u32x4 r;
#pragma unroll
  for (int i = 0; i < 4; ++i) r[i] = pk(a[8 * s + 2 * i], a[8 * s + 2 * i + 1]);
  return r;
}
template <int N> DI void ld_handoff(u32x4 (&v)[N], rsrc_t rs, int voff, int soff) {
#pragma unroll
  for (int i = 0; i < N; ++i) { v[i] = ld16<16>(rs, voff + (i & 3) * KB, soff + (i >> 2) * 4 * KB); SBAR(); }
}
template <int N> DI bool any_sent(const u32x4 (&v)[N]) {
  unsigned m = 0;
#pragma unroll
  for (int i = 0; i < N; ++i) m = smax(m, v[i]);
  return __builtin_amdgcn_ballot_w64(m == SENT) != 0ull;
}
template <int N> DI void chk_handoff(u32x4 (&v)[N], rsrc_t rs, int voff, int soff) {
  if (__builtin_expect(any_sent<N>(v), 0)) {
#pragma unroll 1
    for (int spin = 0; spin < SPIN_CAP; ++spin) {
      CBAR();
#pragma unroll
      for (int i = 0; i < N; ++i)
        if (__builtin_amdgcn_ballot_w64(smax(0u, v[i]) == SENT) != 0ull) v[i] = ld16<16>(rs, voff + (i & 3) * KB, soff + (i >> 2) * 4 * KB);
      if (!any_sent<N>(v)) break;
    }
  }
}

struct Params {
  const unsigned short *x, *wq, *wk, *wv, *woa, *wqkv, *wob;
  unsigned short* out;
  unsigned char* ws;
};

constexpr int L_SCR = 0;
constexpr int L_WB = 64 * KB;
constexpr int L_QF = 148 * KB;
constexpr int L_MISC = L_QF + 8 * KB;
constexpr int M_SIG = L_MISC;
constexpr int M_ST = M_SIG + 768;
constexpr int M_CS = M_ST + 1280;
constexpr int L_BYTES = M_CS + 1024;
constexpr int XI_KS = 1056;
constexpr int P_KV = 0, P_KVW = 112 * 56, P_XI = 25600, P_WQ = P_XI + 28 * XI_KS;
static_assert(L_BYTES <= 160 * KB && P_WQ + 448 * 224 + 64 <= L_MISC, "LDS");

DI void exp_pack(const f32x16& s, u32x4& p0, u32x4& p1) {
#pragma unroll
  for (int i = 0; i < 4; ++i) p0[i] = pk(__builtin_amdgcn_exp2f(s[2 * i]), __builtin_amdgcn_exp2f(s[2 * i + 1]));
#pragma unroll
  for (int i = 0; i < 4; ++i) p1[i] = pk(__builtin_amdgcn_exp2f(s[8 + 2 * i]), __builtin_amdgcn_exp2f(s[8 + 2 * i + 1]));
}
DI void set_shift(u32x4& q1, float m, int h, unsigned hi31) {
  const unsigned a = pk(m, m); const float r1 = m - bflo(a);
  const unsigned c = pk(r1, r1); const float r2 = r1 - bflo(c);
  const unsigned e = pk(r2, 0.f);
  const unsigned d2 = ((a & 0xFFFFu) | (c << 16)) ^ 0x80008000u, d3 = (e ^ 0x00008000u) | hi31;
  q1[2] = h ? d2 : q1[2]; q1[3] = h ? d3 : q1[3];
}
#define PIN(x) asm volatile("" : "+v"(x))
template <bool DOQK, bool DOPVP, class FA, class FB, class FC>
DI void unit(const f32x16& s, f32x16& sn, const f32x16& cn, f32x16& o, f32x16& op, u32x4& pp, const u32x4& kn0, const u32x4& kn1, const u32x4& qn0,
             const u32x4& qn1, const u32x4& v0, const u32x4& v1p, FA ha, FB hb, FC hc) {
  float e[16]; u32x4 p0;
#define E_(i) e[i] = __builtin_amdgcn_exp2f(s[i]); PIN(e[i]); SBAR()
#define C0_(i) p0[i] = pkt(e[2 * i], e[2 * i + 1]); PIN(p0[i]); SBAR()
#define C1_(i) pp[i] = pkt(e[8 + 2 * i], e[8 + 2 * i + 1]); PIN(pp[i]); SBAR()
  SBAR();
  if (DOQK) { sn = MFMA(kn0, qn0, cn); SBAR(); }
  E_(0); E_(1); E_(2); E_(3);
  if (DOPVP) { op = MFMA(v1p, pp, op); SBAR(); }
  ha(); SBAR();
  E_(4); C0_(0); E_(5); C0_(1); E_(6); E_(7);
  if (DOQK) { sn = MFMA(kn1, qn1, sn); SBAR(); }
  hb(); SBAR();
  E_(8); C0_(2); E_(9); C0_(3); E_(10); E_(11);
  o = MFMA(v0, p0, o); SBAR();
  hc(); SBAR();
  E_(12); C1_(0); E_(13); C1_(1); E_(14); E_(15); C1_(2); C1_(3);
}

struct Ctx { unsigned char* flg; ldsp sig; int lane; int w; };
DI unsigned now32() { return (unsigned)__builtin_amdgcn_s_memrealtime(); }
struct Clk { unsigned c, r; };
DI Clk clk_now() { Clk k; k.c = (unsigned)__builtin_amdgcn_s_memtime(); k.r = now32(); return k; }
DI unsigned eta(const Clk& k0, int dcyc) {
  const Clk k = clk_now();
  return (k.r + (unsigned)((float)dcyc * (float)(k.r - k0.r) * __builtin_amdgcn_rcpf((float)(k.c - k0.c)))) | 1u;
}
DI void post(const Ctx& c, int b, int slot, const Clk& k0, int dcyc) {
  const unsigned t = eta(k0, dcyc);
  if (c.lane == 0) __hip_atomic_store((GAS unsigned*)(c.flg + b * 128 + slot * 4), t, __ATOMIC_RELAXED, __HIP_MEMORY_SCOPE_AGENT);
}
DI unsigned flag_ld(const Ctx& c, int pb, int slot) { return __hip_atomic_load((GAS unsigned*)(c.flg + pb * 128 + slot * 4), __ATOMIC_RELAXED, __HIP_MEMORY_SCOPE_AGENT); }
template <int SLP = 1> DI void poll_wait(const Ctx& c, int pb, int slot, unsigned f = 0u) {
#pragma unroll 1
  for (int spin = 0; spin < SPIN_CAP; ++spin) {
    if (__builtin_amdgcn_ballot_w64(f == 0u) == 0ull) break;
    if (spin) __builtin_amdgcn_s_sleep(SLP);
    f = __hip_atomic_load((GAS unsigned*)(c.flg + pb * 128 + slot * 4), __ATOMIC_RELAXED, __HIP_MEMORY_SCOPE_AGENT);
  }
  unsigned t = __builtin_amdgcn_readlane(f, 0);
#pragma unroll
  for (int i = 1; i < 4; ++i) { const unsigned u = __builtin_amdgcn_readlane(f, i); t = (int)(u - t) > 0 ? u : t; }
#pragma unroll 1
  for (int spin = 0; spin < 256; ++spin) {
    const int d = (int)(t - now32());
    if (d <= 0) break;
    if (d > 30) __builtin_amdgcn_s_sleep(6); else __builtin_amdgcn_s_sleep(1);
  }
  CBAR();
}
DI void lds_sig(const Ctx& c, int idx) {
  CBAR();
  *(volatile LAS unsigned*)(c.sig + idx * 16 + c.w * 4) = 1u;
  CBAR();
}
template <int SLP> DI void lds_wait(const Ctx& c, int idx, unsigned n) {
#pragma unroll 1
  for (int spin = 0; spin < (SPIN_CAP << 6); ++spin) {
    const u32x4 v = *(volatile LAS u32x4*)(c.sig + idx * 16);
    if (__builtin_amdgcn_readfirstlane(v[0] + v[1] + v[2] + v[3]) >= n) break;
    if (SLP) __builtin_amdgcn_s_sleep(SLP);
  }
  CBAR();
}
template <int N, bool SWAP, class F> DI f32x16 gemm_lds(const u32x4 (&wr)[N], ldsp bb, F mid) {
  f32x16 acc = {};
  u32x4 bf[8];
#pragma unroll
  for (int i = 0; i < 8; ++i) bf[i] = lds_r16(bb + i * KB);
  SBAR();
#pragma unroll
  for (int i = 0; i < N; ++i) {
    acc = SWAP ? MFMA(bf[i & 7], wr[i], acc) : MFMA(wr[i], bf[i & 7], acc);
    SBAR();
    if (i + 8 < N) bf[i & 7] = lds_r16(bb + (i + 8) * KB);
    mid(i);
    SBAR();
  }
  return acc;
}
DI void add_res(const f32x16& a, const u32x4& x0, const u32x4& x1, u32x4& y0, u32x4& y1) {
#pragma unroll
  for (int i = 0; i < 4; ++i) {
    y0[i] = pk(a[2 * i] + bflo(x0[i]), a[2 * i + 1] + bfhi(x0[i]));
    y1[i] = pk(a[8 + 2 * i] + bflo(x1[i]), a[8 + 2 * i + 1] + bfhi(x1[i]));
  }
}
DI float row_stats(const u32x4& y0, const u32x4& y1, int h) {
  float s0 = 0.f, s1 = 0.f, q0 = 0.f, q1 = 0.f;
#pragma unroll
  for (int i = 0; i < 4; ++i) { s0 = dot2(y0[i], ONES2, s0); s1 = dot2(y1[i], ONES2, s1); q0 = dot2(y0[i], y0[i], q0); q1 = dot2(y1[i], y1[i], q1); }
  const float s = xhalf_add(s0 + s1), q = xhalf_add(q0 + q1);
  return h ? q : s;
}
DI void stats_finish(float v, float& mu, float& rs) {
  auto t = __builtin_amdgcn_permlane32_swap(__float_as_uint(v), __float_as_uint(v), false, false);
  mu = __uint_as_float(t[0]) * (1.f / D_);
  rs = __builtin_amdgcn_rsqf(__builtin_fmaxf(__uint_as_float(t[1]) * (1.f / D_) - mu * mu, 0.f) + EPS);
}
DI void stats_gather(rsrc_t ws, int so, ldsp dst, int lane) {
#pragma unroll 1
  for (int spin = 0; spin < SPIN_CAP; ++spin) {
    CBAR();
    unsigned sv[14], mx = 0;
#pragma unroll
    for (int i = 0; i < 14; ++i) sv[i] = ld4<16>(ws, lane * 4 + i * 256, so);
    float t0 = 0.f, t1 = 0.f, mu, rs;
#pragma unroll
    for (int i = 0; i < 14; i += 2) { t0 += __uint_as_float(sv[i]); t1 += __uint_as_float(sv[i + 1]); mx = umax3(mx, sv[i], sv[i + 1]); }
    stats_finish(t0 + t1, mu, rs);
    *(LAS float*)(dst + lane * 4) = lane >= 32 ? rs : mu;
    if (__builtin_amdgcn_ballot_w64(mx == SENT) == 0ull) break;
  }
}
DI void norm_pack(const f32x16& o, int h, u32x4& y0, u32x4& y1) {
  const float inv = __builtin_amdgcn_rcpf(from_hi(o[12]));
#pragma unroll
  for (int i = 0; i < 4; ++i) { y0[i] = pk(o[2 * i] * inv, o[2 * i + 1] * inv); y1[i] = pk(o[8 + 2 * i] * inv, o[8 + 2 * i + 1] * inv); }
  if (h == 1) { y1[2] = 0; y1[3] = 0; }
}
constexpr float LBIG = 1e30f;
constexpr unsigned NEG31 = 0xF1490000u;

DI u32x4 tr_frag(ldsp a, int hi) {
  const s16x4 lo_ = __builtin_amdgcn_ds_read_tr16_b64_v4i16((LAS s16x4*)a), hi_ = __builtin_amdgcn_ds_read_tr16_b64_v4i16((LAS s16x4*)(a + hi));
  return __builtin_bit_cast(u32x4, __builtin_shufflevector(lo_, hi_, 0, 1, 2, 3, 4, 5, 6, 7));
}
DI void part_write(ldsp d, const f32x16& a) {
#pragma unroll
  for (int g = 0; g < 4; ++g) lds_wf4(d + g * KB, f32x4{a[4 * g], a[4 * g + 1], a[4 * g + 2], a[4 * g + 3]});
}
template <bool LN>
DI void kv_finish(ldsp part, int pstr, int isV, int s, ldsp st, ldsp cs, int lane, rsrc_t ws, int kvo, unsigned char* fkv, u64 tag) {
  const int r = lane & 31, h = lane >> 5;
  f32x4 a[2][4], c[2] = {}, m[2] = {};
  float l0 = 0.f, l1 = 0.f;
#pragma unroll
  for (int k = 0; k < 4; ++k) { a[0][k] = lds_rf4(part + k * pstr + 2 * s * KB + lane * 16); a[1][k] = lds_rf4(part + k * pstr + (2 * s + 1) * KB + lane * 16); }
  if (LN) {
    l0 = *(LAS float*)((isV ? cs : st) + r * 4); l1 = *(LAS float*)(st + 128 + r * 4);
#pragma unroll
    for (int g = 0; g < 2; ++g) { c[g] = lds_rf4((isV ? st : cs) + (16 * s + 8 * g + 4 * h) * 4); m[g] = lds_rf4(st + 128 + (16 * s + 8 * g + 4 * h) * 4); }
  }
  SBAR();
  float v[8];
#pragma unroll
  for (int i = 0; i < 8; ++i) v[i] = (a[i >> 2][0][i & 3] + a[i >> 2][1][i & 3]) + (a[i >> 2][2][i & 3] + a[i >> 2][3][i & 3]);
  if (LN) {
#pragma unroll
    for (int i = 0; i < 8; ++i) v[i] = (v[i] - l0 * c[i >> 2][i & 3]) * (isV ? m[i >> 2][i & 3] : l1);
  }
  u32x4 o = {pk(v[0], v[1]), pk(v[2], v[3]), pk(v[4], v[5]), pk(v[6], v[7])};
  if (isV) { if (r >= 28) { const unsigned f = r == 28 ? ONES2 : 0u; o = u32x4{f, f, f, f}; } }
  else if (s == 1 && h == 1) { o[2] = ONES2; o[3] = 0x00003F80u; }
  st16<16>(o, ws, lane * 16, kvo + (2 * isV + s) * KB);
  asm volatile("s_waitcnt vmcnt(0)" ::: "memory");
  if (lane < 8) st_tag(fkv + lane * 64 * 128 + (2 * isV + s) * 8, tag);
}

DI void phase0_h(const Params& p, const ldsp lds, u64 epo, u64& tag) {
  const int tid = threadIdx.x & 255, lane = tid & 63, r = lane & 31, h = lane >> 5;
  const int w = __builtin_amdgcn_readfirstlane(tid >> 6);
  const int b = blockIdx.x, xcd = b & 7, jj = b >> 3, rt = 8 * xcd + (jj >> 2), sub = jj & 3;
  const rsrc_t ws = mkrs(p.ws, WS_BYTES);
  const Ctx cx = {p.ws, lds + M_SIG + 512, lane, w};
  const ldsp priv = lds + P_KV + w * P_KVW;
  if (sub < 2) {
    const unsigned char* src = (const unsigned char*)(sub ? p.wv : p.wk) + w * P_KVW + lane * 16;
#pragma unroll
    for (int i = 0; i < 7; ++i) if (i < 6 || lane < 8) glds16(src + i * KB, (unsigned)(uintptr_t)priv + i * KB);
  }
  const int q8 = tid & 7, row8 = tid >> 3;
  u32x4 xv[7];
  {
    const unsigned short* xr = p.x + (size_t)(rt * 32 + row8) * D_ + q8 * 8;
#pragma unroll
    for (int i = 0; i < 7; ++i) xv[i] = *(const u32x4*)(xr + i * 64);
  }
  SBAR();
  const int tt = b >> 2, tks = (b & 3) * 7 + 2 * w, wog = b * 4 + w;
  u32x2 tv[4];
  unsigned te[8];
  const bool dead = (lane & 7) == 7;
  {
    const int pitch = tt < 16 ? 448 : 1344;
    const unsigned short* src = (tt < 16 ? p.wq + 28 * tt : p.wqkv + ((tt - 16) % 3) * 448 + ((tt - 16) / 3) * 28) + (size_t)(tks * 16 + (lane >> 3)) * pitch + (dead ? 0 : (lane & 7) * 4);
#pragma unroll
    for (int i = 0; i < 4; ++i) tv[i] = *(const u32x2*)(src + (size_t)((w == 3 && i >= 2) ? 0 : i * 8) * pitch);
    int g = wog < 896 ? wog : 0;
    const unsigned short* wo = g >= 448 ? p.wob : p.woa;
    if (g >= 448) g -= 448;
    const int ct = g >> 5, ks = g & 31, head = ks >> 1;
    const int k0 = head * 28 + 16 * (ks & 1) + 4 * h, kmax = head * 28 + 28;
#pragma unroll
    for (int j = 0; j < 8; ++j) {
      const int k = k0 + 8 * (j >> 2) + (j & 3);
      te[j] = wo[(k < kmax ? k : 0) * 448 + 32 * ct + r];
    }
  }
  SBAR();
  {
    float sm[2] = {}, sq[2] = {};
#pragma unroll
    for (int i = 0; i < 7; ++i)
#pragma unroll
      for (int d = 0; d < 4; ++d) { sm[d & 1] = dot2(xv[i][d], ONES2, sm[d & 1]); sq[d & 1] = dot2(xv[i][d], xv[i][d], sq[d & 1]); }
    float a = sm[0] + sm[1], c = sq[0] + sq[1];
#define DPP_ADD(v, ctl) v += __uint_as_float(__builtin_amdgcn_update_dpp(0u, __float_as_uint(v), ctl, 0xF, 0xF, true))
    DPP_ADD(a, 0xB1); DPP_ADD(c, 0xB1); DPP_ADD(a, 0x4E); DPP_ADD(c, 0x4E); DPP_ADD(a, 0x141); DPP_ADD(c, 0x141);
    const float mu = a * (1.f / D_), rs = __builtin_amdgcn_rsqf(__builtin_fmaxf(c * (1.f / D_) - mu * mu, 0.f) + EPS), nb = -mu * rs;
#pragma unroll
    for (int i = 0; i < 7; ++i) {
      unsigned y[4];
#pragma unroll
      for (int d = 0; d < 4; ++d) y[d] = pk(__builtin_fmaf(bflo(xv[i][d]), rs, nb), __builtin_fmaf(bfhi(xv[i][d]), rs, nb));
      const ldsp dst = lds + P_XI + (4 * i + (q8 >> 1)) * XI_KS + row8 * 16 + (q8 & 1) * 8;
      *(LAS u32x2*)dst = u32x2{y[0], y[1]};
      *(LAS u32x2*)(dst + 512) = u32x2{y[2], y[3]};
    }
  }
  lds_sig(cx, 0);
  tag = (((u64)__builtin_amdgcn_readfirstlane((unsigned)(epo >> 32)) << 32) | __builtin_amdgcn_readfirstlane((unsigned)epo)) + 1ull;
  const int i16 = lane & 15, trow = 4 * h + (i16 >> 2), tcol = ((lane >> 4) & 1) * 32 + (i16 & 3) * 8;
  if (sub < 2) {
    u32x4 wf[7];
#pragma unroll
    for (int i = 0; i < 7; ++i) wf[i] = tr_frag(priv + (16 * i + trow) * 56 + tcol, 8 * 56);
    lds_wait<0>(cx, 0, 4u);
    f32x16 acc = {};
#pragma unroll
    for (int i = 0; i < 7; ++i) { const u32x4 xf = lds_r16(lds + P_XI + (7 * w + i) * XI_KS + lane * 16); acc = sub ? MFMA(xf, wf[i], acc) : MFMA(wf[i], xf, acc); }
    part_write(priv + lane * 16, acc);
    lds_sig(cx, 1);
    if (w < 2) {
      lds_wait<0>(cx, 1, 4u);
      kv_finish<false>(lds + P_KV, P_KVW, sub, w, (ldsp)0, (ldsp)0, lane, ws, OFF_PAIR + R_KVA + rt * 4 * KB, p.ws + OFF_PAIR + R_FKV + rt * 128, tag);
    }
    if (rt == 0) {
      float cs = 0.f;
#pragma unroll
      for (int i = 0; i < 7; ++i) {
        st16<16>(wf[i], ws, lane * 16, OFF_WKV + (sub * 28 + 7 * w + i) * KB);
#pragma unroll
        for (int d = 0; d < 4; ++d) cs = dot2(wf[i][d], ONES2, cs);
      }
      cs = xhalf_add(cs);
      st4<16>(h ? 0u : __float_as_uint(cs), ws, lane * 4, OFF_CS + ((64 + sub) * 16 + 4 * w) * 128);
      st4<16>(0u, ws, lane * 4 + 256, OFF_CS + ((64 + sub) * 16 + 4 * w) * 128);
    }
  }
  {
    st4<16>(0u, ws, w == 0 ? h * PAIR_SZ + r * 4 : WS_BYTES, OFF_PAIR + R_FLG + b * 128);
    const ldsp img = priv + 4 * KB;
#pragma unroll
    for (int i = 0; i < 4; ++i) *(LAS u32x2*)(img + i * 512 + lane * 8) = dead ? u32x2{0u, 0u} : tv[i];
    CBAR();
    float cs = 0.f;
#pragma unroll
    for (int ks = 0; ks < 2; ++ks) {
      const u32x4 o = tr_frag(img + ks * KB + trow * 64 + tcol, 512);
      if (ks == 0 || w < 3) {
        st16<16>(o, ws, lane * 16, (tt * 28 + tks + ks) * KB);
#pragma unroll
        for (int i = 0; i < 4; ++i) cs = dot2(o[i], ONES2, cs);
      }
    }
    u32x4 o;
#pragma unroll
    for (int i = 0; i < 4; ++i) { asm volatile("" : "+v"(te[2 * i]), "+v"(te[2 * i + 1])); o[i] = te[2 * i] | (te[2 * i + 1] << 16); }
    if ((wog & 1) && h == 1) { o[2] = 0u; o[3] = 0u; }
    if (wog < 896) st16<16>(o, ws, lane * 16, OFF_WOA + wog * KB);
    cs = xhalf_add(cs);
    if (h == 0) st4<16>(__float_as_uint(cs), ws, r * 4, OFF_CS + wog * 128);
    asm volatile("s_waitcnt vmcnt(0)" ::: "memory");
    unsigned k = 0;
    if (lane == 0) k = __hip_atomic_fetch_add((LAS unsigned*)(cx.sig + 2 * 16), 1u, __ATOMIC_RELAXED, __HIP_MEMORY_SCOPE_WORKGROUP);
    if (__builtin_amdgcn_readfirstlane(k) == 3u && lane < 8) st_tag(p.ws + OFF_INI + (lane * 256 + b) * 8, tag);
  }
}
DI void phase0_c(const Params& p, const ldsp lds, u64 epo, u64& tag, u32x4& q0, u32x4& q1) {
  const int lane = threadIdx.x & 63, h = lane >> 5;
  const int w = __builtin_amdgcn_readfirstlane(threadIdx.x >> 6), sub = (blockIdx.x >> 3) & 3;
  const Ctx cx = {p.ws, lds + M_SIG + 512, lane, w};
  __builtin_amdgcn_s_sleep(PRO_DLY);
  if (lane < 56) {
    const unsigned short* src = p.wq + (size_t)(4 * w + lane / 14) * 448 + sub * 112 + (lane % 14) * 8;
#pragma unroll 4
    for (int i = 0; i < 28; ++i) glds16(src + (size_t)i * 16 * 448, (unsigned)(uintptr_t)(lds + P_WQ) + (i * 16 + 4 * w) * 224);
  }
  asm volatile("s_waitcnt vmcnt(0)" ::: "memory");
  tag = (((u64)__builtin_amdgcn_readfirstlane((unsigned)(epo >> 32)) << 32) | __builtin_amdgcn_readfirstlane((unsigned)epo)) + 1ull;
  lds_sig(cx, 3);
  lds_wait<1>(cx, 3, 4u); lds_wait<1>(cx, 0, 4u);
  const int i16 = lane & 15;
  const ldsp wb0 = opaque(lds + P_WQ + (4 * h + (i16 >> 2)) * 224 + w * 56 + ((lane >> 4) & 1) * 32 + (i16 & 3) * 8), wb1 = opaque(wb0 + 14 * 16 * 224);
  const ldsp xb = lds + P_XI + lane * 16;
  u32x4 wr[4], xr[4];
#define PRE_LD(i) do { wr[(i) & 3] = tr_frag(((i) < 14 ? wb0 : wb1) + ((i) % 14) * 16 * 224, 8 * 224); xr[(i) & 3] = lds_r16(xb + (i) * XI_KS); } while (0)
  PRE_LD(0); PRE_LD(1); PRE_LD(2); SBAR();
  f32x16 acc = {};
#pragma unroll
  for (int i = 0; i < 28; ++i) {
    if (i + 3 < 28) PRE_LD(i + 3);
    SBAR();
    acc = MFMA(wr[i & 3], xr[i & 3], acc);
    SBAR();
  }
  lds_sig(cx, 4);
#pragma unroll
  for (int i = 0; i < 16; ++i) acc[i] *= C2;
  q0 = pack8(acc, 0); q1 = pack8(acc, 1);
  lds_wait<0>(cx, 4, 4u);
}

__global__ void __launch_bounds__(512, 2) k_main(Params p) {
  __shared__ __attribute__((aligned(16))) char smem[L_BYTES];
  const ldsp lds = (ldsp)smem;
  const int tid = threadIdx.x;
  const int w8 = __builtin_amdgcn_readfirstlane(tid >> 6);
  const rsrc_t wt = mkrs(p.ws, OFF_PAIR);
  auto nop = []() {};
  const f32x16 Z = {};
  const u64 epo = ld_tag(p.ws + OFF_EPO);
  u64 tag;
  if (tid < 192) *(LAS unsigned*)(lds + M_SIG + tid * 4) = 0u;
  if (CODE_KB && w8 == 3) {
    const unsigned char* pc = (const unsigned char*)(__builtin_amdgcn_s_getpc() & ~127ull);
    const int ln = (((blockIdx.x >> 3) * 64 + (tid & 63)) % (CODE_KB * 8)) * 128;
    glds16(pc + ln, (unsigned)(uintptr_t)(lds + M_ST));
  }
  BAR();
#define PAIR_SETUP() \
    int lane = tid & 63, b = blockIdx.x, w = w8 & 3; \
    asm volatile("" : "+v"(lane), "+s"(b), "+s"(w)); \
    lane &= 63; b &= 255; w &= 3; \
    const int r = lane & 31, h = lane >> 5, l16 = lane * 16, xcd = b & 7, jj = b >> 3; \
    const int rt = 8 * xcd + (jj >> 2), sub = jj & 3;          \
    const int rb = 2 * xcd + (jj >> 4), hd = jj & 15;          \
    const ldsp scr = lds + L_SCR + l16; \
    unsigned char* const reg = p.ws + OFF_PAIR + pair * PAIR_SZ; \
    const rsrc_t ws = mkrs(reg, 2 * PAIR_SZ - pair * PAIR_SZ); \
    const Ctx cx = {reg + R_FLG, lds + M_SIG + pair * 256, lane, w}

  if (w8 < 4) {
    u32x4 qn0, qn1;
    unsigned kve = 0u;
    phase0_c(p, lds, epo, tag, qn0, qn1);
#pragma unroll 1
   for (int pair = 0; pair < 2; ++pair) {
    PAIR_SETUP();
    const unsigned char* const kpa = reg + R_FKV + (xcd * 64 + 4 * ((2 * xcd + 1 - (lane >> 2)) & 15) + w) * 128 + (lane & 3) * 8;
    u64 miss = 0ull, kvf;
    unsigned chk = pair, pend = 1u;
    int kvi = 0;
    auto kv_mask = [&]() { miss = __builtin_amdgcn_ballot_w64(kvf != tag); };
    auto kv_next = [&]() -> int {
      if (__builtin_expect(((unsigned)(miss >> (4 * kvi)) & 15u) != 0u, 0)) {
#pragma unroll 1
        for (int spin = 0; spin < SPIN_CAP; ++spin) {
          kvf = ld_tag(kpa);
          kv_mask();
          if (((unsigned)(miss >> (4 * kvi)) & 15u) == 0u) break;
          __builtin_amdgcn_s_sleep(2);
        }
        CBAR();
      }
      const int o = R_KVA + (4 * ((2 * xcd + 1 - kvi) & 15) + w) * 4 * KB;
      ++kvi;
      return o;
    };
    {
      u32x4 q[4][2], K[4][2], V[4][2];
      int kb[4];
      if (pair) {
        poll_wait(cx, ((((4 * (lane & 1) + w) << 2) | ((lane >> 1) & 1)) << 3) | xcd, 12, kve);
      } else {
#pragma unroll 1
        for (int spin = 0; spin < SPIN_CAP; ++spin) {
          if (((unsigned)__builtin_amdgcn_ballot_w64(ld_tag(kpa) != tag) & 0xFFu) == 0u) break;
          __builtin_amdgcn_s_sleep(2);
        }
        CBAR();
      }
#pragma unroll
      for (int t = 0; t < 2; ++t) {
        kb[t] = kv_next();
        K[t][0] = ld16<16>(ws, l16, kb[t]); K[t][1] = ld16<16>(ws, l16 + KB, kb[t]);
        V[t][0] = ld16<16>(ws, l16 + 2 * KB, kb[t]); V[t][1] = ld16<16>(ws, l16 + 3 * KB, kb[t]);
        SBAR();
      }
#pragma unroll
      for (int t = 2; t < 4; ++t) { kb[t] = kb[0]; K[t][0] = K[t][1] = V[t][0] = V[t][1] = u32x4{}; }
      kvf = ld_tag(kpa);
      f32x16 o0 = {}, o1 = {}, o2 = {}, o3 = {};
      PIN(o0); PIN(o1); PIN(o2); PIN(o3);
      if (h == 1) { qn1[2] = 0u; qn1[3] = 0u; }
      lds_w16(lds + L_QF + l16 + 2 * w * KB, qn0);
      if (pair) chk_handoff<2>(K[0], ws, l16, kb[0]);
      {
        f32x16 s0 = MFMA(K[0][0], qn0, Z);
        s0 = MFMA(K[0][1], qn1, s0);
        set_shift(qn1, xhalf_max(max16(s0, s0[0])), h, 0u);
        lds_w16(lds + L_QF + l16 + (2 * w + 1) * KB, qn1);
        lds_sig(cx, 7);
      }
      if (pair) chk_handoff<2>(V[0], ws, l16 + 2 * KB, kb[0]);
      lds_wait<0>(cx, 7, 4u);
#pragma unroll
      for (int a = 0; a < 4; ++a) { q[a][0] = lds_r16(lds + L_QF + l16 + 2 * a * KB); q[a][1] = lds_r16(lds + L_QF + l16 + (2 * a + 1) * KB); }
      f32x16 sa = MFMA(K[0][0], q[0][0], Z), sb;
      sa = MFMA(K[0][1], q[0][1], sa);
      u32x4 pp = {};
#define TILE(c, LD, LDV1) { \
        constexpr int n = (c + 1) & 3, pv = (c + 3) & 3; \
        auto ldV1 = [&]() { if (LDV1) V[pv][1] = ld16<16>(ws, l16 + 3 * KB, kb[pv]); }; \
        auto ldK = [&]() { if (LD) { kb[c] = kv_next(); K[c][0] = ld16<16>(ws, l16, kb[c]); K[c][1] = ld16<16>(ws, l16 + KB, kb[c]); } }; \
        auto ldV0 = [&]() { if (LD) V[c][0] = ld16<16>(ws, l16 + 2 * KB, kb[c]); }; \
        unit<true, true>(sa, sb, Z, o0, o3, pp, K[c][0], K[c][1], q[1][0], q[1][1], V[c][0], V[pv][1], ldV1, nop, nop); \
        unit<true, true>(sb, sa, Z, o1, o0, pp, K[c][0], K[c][1], q[2][0], q[2][1], V[c][0], V[c][1], nop, nop, nop); \
        if (LD && c == 0 && __builtin_expect(pend != 0u, 0)) {        \
          pend = 0u; kv_mask(); \
          _Pragma("unroll") for (int t = 2; t < 4; ++t) { kb[t] = kv_next(); K[t][0] = ld16<16>(ws, l16, kb[t]); K[t][1] = ld16<16>(ws, l16 + KB, kb[t]); \
            V[t][0] = ld16<16>(ws, l16 + 2 * KB, kb[t]); V[t][1] = ld16<16>(ws, l16 + 3 * KB, kb[t]); } } \
        unit<true, true>(sa, sb, Z, o2, o1, pp, K[c][0], K[c][1], q[3][0], q[3][1], V[c][0], V[c][1], nop, ldK, nop); \
        if (LD && c == 0 && __builtin_expect(chk != 0u, 0)) { chk = 0u; chk_handoff<2>(K[n], ws, l16, kb[n]); chk_handoff<2>(V[n], ws, l16 + 2 * KB, kb[n]); } \
        unit<true, true>(sb, sa, Z, o3, o2, pp, K[n][0], K[n][1], q[0][0], q[0][1], V[c][0], V[c][1], nop, nop, ldV0); }
#pragma unroll 1
      for (int tt = 0; tt < 12; tt += 4) {
        kv_mask(); if (!FSKIP || miss) kvf = ld_tag(kpa);
        TILE(0, true, true) TILE(1, true, true)
        kv_mask(); if (!FSKIP || miss) kvf = ld_tag(kpa);
        TILE(2, true, true) TILE(3, true, true)
      }
      TILE(0, false, true)
      lds_sig(cx, 11);
      lds_wait<1>(cx, 8, 4u);
      TILE(1, false, false)
      const ldsp red = scr + w * 4 * KB;
#define WR_LO(o, a) [&]() { lds_wf4(red + (a) * 16 * KB, f32x4{o[0], o[1], o[2], o[3]}); lds_wf4(red + (a) * 16 * KB + KB, f32x4{o[4], o[5], o[6], o[7]}); }
#define WR_HI(o, a) [&]() { lds_wf4(red + (a) * 16 * KB + 2 * KB, f32x4{o[8], o[9], o[10], o[11]}); lds_wf4(red + (a) * 16 * KB + 3 * KB, f32x4{o[12], o[13], o[14], o[15]}); lds_sig(cx, a); }
      unit<true, true>(sa, sb, Z, o0, o3, pp, K[3][0], K[3][1], q[0][0], q[0][1], V[2][0], V[1][1], nop, nop, nop);
      unit<true, true>(sb, sa, Z, o0, o0, pp, K[2][0], K[2][1], q[1][0], q[1][1], V[3][0], V[2][1], nop, nop, nop);
      unit<true, true>(sa, sb, Z, o1, o0, pp, K[3][0], K[3][1], q[1][0], q[1][1], V[2][0], V[3][1], nop, WR_LO(o0, 0), WR_HI(o0, 0));
      unit<true, true>(sb, sa, Z, o1, o1, pp, K[2][0], K[2][1], q[2][0], q[2][1], V[3][0], V[2][1], nop, nop, nop);
      unit<true, true>(sa, sb, Z, o2, o1, pp, K[3][0], K[3][1], q[2][0], q[2][1], V[2][0], V[3][1], nop, WR_LO(o1, 1), WR_HI(o1, 1));
      unit<true, true>(sb, sa, Z, o2, o2, pp, K[2][0], K[2][1], q[3][0], q[3][1], V[3][0], V[2][1], nop, nop, nop);
      unit<true, true>(sa, sb, Z, o3, o2, pp, K[3][0], K[3][1], q[3][0], q[3][1], V[2][0], V[3][1], nop, WR_LO(o2, 2), WR_HI(o2, 2));
      unit<false, true>(sb, sa, Z, o3, o3, pp, K[2][0], K[2][1], q[3][0], q[3][1], V[3][0], V[2][1], nop, nop, nop);
      o3 = MFMA(V[3][1], pp, o3);
      WR_LO(o3, 3)(); WR_HI(o3, 3)();
    }
    {
      u32x4 of[8] = {};
      const int src = R_OFA + (rt * 32 + 8 * w) * KB;
      if (w != sub) {
        poll_wait(cx, (((jj & ~3) | w) << 3) | xcd, 0);
        ld_handoff<8>(of, ws, l16, src);
      }
      BAR();
      const unsigned bad = __builtin_amdgcn_readfirstlane(*(LAS unsigned*)(cx.sig + 10 * 16));
      if (__builtin_expect((bad >> w) & 1u, 0)) {
        const u32x4 q0 = lds_r16(lds + L_QF + l16 + 2 * w * KB);
        u32x4 q1 = lds_r16(lds + L_QF + l16 + (2 * w + 1) * KB);
        if (h == 1) { q1[2] = 0; q1[3] = 0; }
        float m = -INFINITY;
#pragma unroll 1
        for (int t = 0; t < 64; ++t) {
          f32x16 s = MFMA(ld16<16>(ws, l16, R_KVA + t * 4 * KB), q0, Z);
          s = MFMA(ld16<16>(ws, l16 + KB, R_KVA + t * 4 * KB), q1, s);
          m = max16(s, m);
        }
        set_shift(q1, xhalf_max(m), h, 0u);
        f32x16 o = {};
#pragma unroll 1
        for (int t = 0; t < 64; ++t) {
          f32x16 s = MFMA(ld16<16>(ws, l16, R_KVA + t * 4 * KB), q0, Z);
          s = MFMA(ld16<16>(ws, l16 + KB, R_KVA + t * 4 * KB), q1, s);
          u32x4 p0, p1;
          exp_pack(s, p0, p1);
          o = MFMA(ld16<16>(ws, l16 + 2 * KB, R_KVA + t * 4 * KB), p0, o);
          o = MFMA(ld16<16>(ws, l16 + 3 * KB, R_KVA + t * 4 * KB), p1, o);
        }
        u32x4 y0, y1;
        norm_pack(o, h, y0, y1);
        st16<16>(y0, ws, l16, R_OFA + (rt * 32 + sub * 8 + 2 * w) * KB);
        st16<16>(y1, ws, l16 + KB, R_OFA + (rt * 32 + sub * 8 + 2 * w) * KB);
        lds_w16(scr + (sub * 8 + 2 * w) * KB, y0); lds_w16(scr + (sub * 8 + 2 * w + 1) * KB, y1);
      }
      if (w != sub) {
        chk_handoff<8>(of, ws, l16, src);
#pragma unroll
        for (int i = 0; i < 8; ++i) lds_w16(scr + (8 * w + i) * KB, of[i]);
      }
      BAR();
    }
    f32x4 ck[4], cq[4];
    const float cv = *(LAS float*)(lds + M_CS + 6 * 128 + r * 4);
#pragma unroll
    for (int g = 0; g < 4; ++g) { cq[g] = lds_rf4(lds + M_CS + 4 * 128 + (8 * g + 4 * h) * 4); ck[g] = lds_rf4(lds + M_CS + 5 * 128 + (8 * g + 4 * h) * 4); }
    BAR();
    u32x4 q0, q1, k0, k1, v0, v1;
    const Clk ck0 = clk_now();
    {
      const int xo = R_XF1 + (rb * 4 + w) * 28 * KB;
      const ldsp wb0 = opaque(lds + L_WB + l16), wb1 = opaque(lds + L_WB + 28 * KB + l16);
      const ldsp st = lds + M_ST + w * 256;
      f32x4 m4[4], r4[4];
      poll_wait(cx, (((jj & ~15) | (w << 2) | (lane & 3)) << 3) | xcd, 1);
      f32x16 aq, ak, av;
      float mu, rs;
      u32x4 xf[28], wk[3], wv[3], wq[4];
#define LDX(i) do { xf[i] = ld16<16>(ws, l16 + ((i) & 3) * KB, xo + ((i) >> 2) * 4 * KB); SBAR(); } while (0)
#define CHKX(i) do { if (__builtin_expect(__builtin_amdgcn_ballot_w64(smax(0u, xf[i]) == SENT) != 0ull, 0)) { \
          _Pragma("unroll 1") for (int spin = 0; spin < SPIN_CAP; ++spin) { CBAR(); LDX(i); if (__builtin_amdgcn_ballot_w64(smax(0u, xf[i]) == SENT) == 0ull) break; } } SBAR(); } while (0)
#define QST(i) do { if ((i) < 28) { SBAR(); aq = MFMA(wq[(i) & 3], xf[(i) < 28 ? (i) : 0], (i) ? aq : Z); SBAR(); if ((i) + 3 < 28) wq[((i) + 3) & 3] = lds_r16(wb0 + ((i) + 3) * KB); SBAR(); } } while (0)
#pragma unroll
      for (int i = 0; i < X1_AHEAD; ++i) LDX(i);
#pragma unroll
      for (int i = 0; i < 2; ++i) { wk[i] = lds_r16(wb1 + i * KB); wv[i] = lds_r16(wb1 + (28 + i) * KB); }
#pragma unroll
      for (int i = 0; i < 3; ++i) wq[i] = lds_r16(wb0 + i * KB);
      SBAR();
      CHKX(0);
#pragma unroll
      for (int ks = 0; ks < 28; ++ks) {
        ak = MFMA(wk[ks % 3], xf[ks], ks ? ak : Z); SBAR();
        if (ks + 2 < 28) { wk[(ks + 2) % 3] = lds_r16(wb1 + (ks + 2) * KB); wv[(ks + 2) % 3] = lds_r16(wb1 + (28 + ks + 2) * KB); }
        if (X1_AHEAD + 2 * ks < 28) LDX(X1_AHEAD + 2 * ks);
        if (ks == 25) {
          lds_wait<0>(cx, 12 + w, 1u);
          mu = *(LAS float*)(st + r * 4); rs = *(LAS float*)(st + 128 + r * 4);
        }
        if (ks == 26) {
#pragma unroll
          for (int g = 0; g < 4; ++g) { m4[g] = lds_rf4(st + (8 * g + 4 * h) * 4); r4[g] = lds_rf4(st + 128 + (8 * g + 4 * h) * 4); }
        }
        SBAR();
        av = MFMA(xf[ks], wv[ks % 3], ks ? av : Z); SBAR();
        if (X1_AHEAD + 2 * ks + 1 < 28) LDX(X1_AHEAD + 2 * ks + 1);
        if (ks < QS) QST(ks);
        if (ks + 1 < 28) CHKX(ks + 1);
        if (ks == POST_KV) post(cx, b, 4 + w, ck0, DT_KV);
      }
      const int ko = R_KVB + (hd * 64 + rb * 4 + w) * 4 * KB;
      QST(QS); QST(QS + 1);
#pragma unroll
      for (int g = 0; g < 4; ++g) {
#pragma unroll
        for (int i = 4 * g; i < 4 * g + 4; ++i) ak[i] = (ak[i] - mu * ck[g][i & 3]) * rs;
        unsigned a0 = pk(ak[4 * g], ak[4 * g + 1]), a1 = pk(ak[4 * g + 2], ak[4 * g + 3]);
        if (g == 3 && h == 1) { a0 = ONES2; a1 = 0x00003F80u; }
        PIN(a0); PIN(a1);
        if (g < 2) { k0[2 * g] = a0; k0[2 * g + 1] = a1; } else { k1[2 * g - 4] = a0; k1[2 * g - 3] = a1; }
        if (g == 1) st16<16>(k0, ws, l16, ko);
        if (g == 3) st16<16>(k1, ws, l16 + KB, ko);
        QST(QS + 2 + g);
      }
      const unsigned vpad = r == 28 ? ONES2 : 0u;
#pragma unroll
      for (int g = 0; g < 4; ++g) {
#pragma unroll
        for (int i = 4 * g; i < 4 * g + 4; ++i) av[i] = (av[i] - m4[g][i & 3] * cv) * r4[g][i & 3];
        unsigned a0 = pk(av[4 * g], av[4 * g + 1]), a1 = pk(av[4 * g + 2], av[4 * g + 3]);
        if (r >= 28) { a0 = vpad; a1 = vpad; }
        PIN(a0); PIN(a1);
        if (g < 2) { v0[2 * g] = a0; v0[2 * g + 1] = a1; } else { v1[2 * g - 4] = a0; v1[2 * g - 3] = a1; }
        if (g == 1) st16<16>(v0, ws, l16 + 2 * KB, ko);
        if (g == 3) st16<16>(v1, ws, l16 + 3 * KB, ko);
        QST(QS + 6 + g);
      }
      lds_w16(scr + (32 + 4 * w) * KB, k0); QST(QS + 10); lds_w16(scr + (33 + 4 * w) * KB, k1); QST(QS + 11);
      lds_w16(scr + (34 + 4 * w) * KB, v0); QST(QS + 12); lds_w16(scr + (35 + 4 * w) * KB, v1);
      lds_sig(cx, 4);
#pragma unroll
      for (int i = QS + 13; i < 28; ++i) QST(i);
      const float rq = rs * C2;
#pragma unroll
      for (int i = 0; i < 16; ++i) aq[i] = (aq[i] - mu * cq[i >> 2][i & 3]) * rq;
      q0 = pack8(aq, 0); q1 = pack8(aq, 1);
      if (h == 1) { q1[2] = 0; q1[3] = NEG31; }
    }
    {
      f32x16 cd, cf;
#pragma unroll
      for (int i = 0; i < 16; ++i) {
        const int kk = (i & 3) + 8 * (i >> 2) + 4 * h;
        cd[i] = kk <= r ? 0.f : -1e30f; cf[i] = kk <= r ? -1e30f : 0.f;
      }
      f32x16 sa = MFMA(k0, q0, cd), sb;
      sa = MFMA(k1, q1, sa);
      set_shift(q1, xhalf_max(max16(sa, sa[0])), h, NEG31);
      const ldsp kp = scr + (8 + w) * 4 * KB;
      auto need = [&](int u) {
        const int ti = 8 + w - u;
        if (u == 1 && w > 0) lds_wait<0>(cx, 4, 4u);
        if (__builtin_expect((ti & 3) == 3, 0)) lds_wait<1>(cx, 5 + (ti >> 2), 2u);
      };
      u32x4 K[2][2], V[2][2], pp;
      f32x16 o = {};
      sa = MFMA(k0, q0, cd); sa = MFMA(k1, q1, sa);
      V[0][0] = v0; V[0][1] = v1;
      need(1);
      K[1][0] = lds_r16(kp - 4 * KB); K[1][1] = lds_r16(kp - 3 * KB); V[1][0] = lds_r16(kp - 2 * KB);
#pragma unroll
      for (int u = 0; u < 9; ++u) {
        const int c = u & 1, n = c ^ 1;
        auto ha = [&]() {
          if (u + 1 < 9) V[n][1] = lds_r16(kp - (u + 1) * 4 * KB + 3 * KB);
          if (u + 2 < 9) { need(u + 2); K[c][0] = lds_r16(kp - (u + 2) * 4 * KB); K[c][1] = lds_r16(kp - (u + 2) * 4 * KB + KB); }
        };
        auto hc = [&]() { if (u + 2 < 9) V[c][0] = lds_r16(kp - (u + 2) * 4 * KB + 2 * KB); if (u >= 4 && u <= 6 && __builtin_expect(u == (w < 2 ? 4 : 3 + w), 0)) post(cx, b, 8 + w, ck0, DT_OB + (8 - u) * DT_UNIT); };
        f32x16& s = c ? sb : sa; f32x16& sn = c ? sa : sb;
        if (u == 0) unit<true, false>(s, sn, Z, o, o, pp, K[n][0], K[n][1], q0, q1, V[c][0], V[n][1], ha, nop, hc);
        else if (u < 7) unit<true, true>(s, sn, Z, o, o, pp, K[n][0], K[n][1], q0, q1, V[c][0], V[n][1], ha, nop, hc);
        else if (u == 7) unit<true, true>(s, sn, cf, o, o, pp, K[n][0], K[n][1], q0, q1, V[c][0], V[n][1], ha, nop, hc);
        else unit<false, true>(s, sn, Z, o, o, pp, K[n][0], K[n][1], q0, q1, V[c][0], V[n][1], ha, nop, hc);
      }
      o = MFMA(V[0][1], pp, o);
      if (__builtin_expect(__builtin_amdgcn_ballot_w64(h == 1 && !(o[12] < LBIG)) != 0ull, 0)) {
        if (h == 1) { q1[2] = 0; q1[3] = NEG31; }
        float m = -INFINITY;
#pragma unroll 1
        for (int u = 0; u < 9; ++u) {
          f32x16 s;
#pragma unroll
          for (int i = 0; i < 16; ++i) s[i] = u == 0 ? cd[i] : u == 8 ? cf[i] : 0.f;
          s = MFMA(lds_r16(kp - u * 4 * KB), q0, s); s = MFMA(lds_r16(kp - u * 4 * KB + KB), q1, s);
          m = max16(s, m);
        }
        set_shift(q1, xhalf_max(m), h, NEG31);
        o = Z;
#pragma unroll 1
        for (int u = 0; u < 9; ++u) {
          f32x16 s;
#pragma unroll
          for (int i = 0; i < 16; ++i) s[i] = u == 0 ? cd[i] : u == 8 ? cf[i] : 0.f;
          s = MFMA(lds_r16(kp - u * 4 * KB), q0, s); s = MFMA(lds_r16(kp - u * 4 * KB + KB), q1, s);
          u32x4 p0, p1;
          exp_pack(s, p0, p1);
          o = MFMA(lds_r16(kp - u * 4 * KB + 2 * KB), p0, o); o = MFMA(lds_r16(kp - u * 4 * KB + 3 * KB), p1, o);
        }
      }
      u32x4 y0, y1;
      norm_pack(o, h, y0, y1);
      const int oo = R_OFB + ((rb * 4 + w) * 16 + hd) * 2 * KB;
      st16<16>(y0, ws, l16, oo); st16<16>(y1, ws, l16 + KB, oo);
      lds_sig(cx, 9);
    }
    u32x4 wa1[WA1];
    {
      u32x4 of[8];
      const int src = R_OFB + (rt * 32 + 8 * w) * KB;
#pragma unroll
      for (int i = 0; i < WA1; ++i) { wa1[i] = ld16<16>(wt, l16 + (i & 3) * KB, pair ? OFF_PAIR : OFF_WAT + ((4 * sub + w) * 28 + (i & ~3)) * KB); SBAR(); }
      poll_wait(cx, (((jj & ~15) | (w << 2) | (lane & 3)) << 3) | xcd, 8 + (rt & 3));
      ld_handoff<8>(of, ws, l16, src);
      chk_handoff<8>(of, ws, l16, src);
      lds_wait<1>(cx, 9, 4u);
#pragma unroll
      for (int i = 0; i < 8; ++i) lds_w16(scr + (8 * w + i) * KB, of[i]);
      BAR();
    }
    if (pair == 0) {
      u32x4 wa[28];
#pragma unroll
      for (int i = 0; i < WA1; ++i) wa[i] = wa1[i];
#pragma unroll
      for (int i = WA1; i < 28; ++i) { wa[i] = ld16<16>(wt, l16 + (i & 3) * KB, OFF_WAT + ((4 * sub + w) * 28 + (i & ~3)) * KB); SBAR(); }
      f32x4 cq[4];
#pragma unroll
      for (int g = 0; g < 4; ++g) cq[g] = lds_rf4(lds + M_CS + w * 128 + (8 * g + 4 * h) * 4);
      if (w >= 2) poll_wait<2>(cx, (((jj & ~3) | (lane & 3)) << 3) | xcd, 2);
      if (w == 3) stats_gather(ws, R_ST2 + rt * 14 * 256, lds + M_ST + 4 * 256, lane);
      if (w == 2 && sub < 2) {
        const unsigned t = eta(ck0, DT_Q);
        if (lane == 0) __hip_atomic_store((GAS unsigned*)(cx.flg + PAIR_SZ + b * 128 + 12 * 4), t, __ATOMIC_RELAXED, __HIP_MEMORY_SCOPE_AGENT);
      }
      BAR();
      if (sub < 2 && w < 2) __builtin_amdgcn_s_sleep(4);
      const float mu = *(LAS float*)(lds + M_ST + 4 * 256 + r * 4), rq = *(LAS float*)(lds + M_ST + 4 * 256 + 128 + r * 4) * C2;
      f32x16 acc = gemm_lds<28, false>(wa, scr + 32 * KB, [&](int i) { if (i == KVE_AT) kve = flag_ld(cx, PAIR_SZ / 128 + (((((4 * (lane & 1) + w) << 2) | ((lane >> 1) & 1)) << 3) | xcd), 12); });
#pragma unroll
      for (int i = 0; i < 16; ++i) acc[i] = (acc[i] - mu * cq[i >> 2][i & 3]) * rq;
      qn0 = pack8(acc, 0); qn1 = pack8(acc, 1);
    }
   }
   return;
  }

  __builtin_amdgcn_s_setprio(2);
  phase0_h(p, lds, epo, tag);
  u32x4 x0 = {}, x1 = {};
#pragma unroll 1
 for (int pair = 0; pair < 2; ++pair) {
  PAIR_SETUP();
  __builtin_amdgcn_s_setprio(HPRIO);
  const int ct0 = sub < 2 ? 4 * sub : 3 * sub + 2, nct = sub < 2 ? 4 : 3;
  const bool ctok = w < nct;
  const int ct = ct0 + (ctok ? w : 0);
  const int xo1 = R_XF1 + (rt * 28 + ct * 2) * KB, xo2 = R_XF2 + (rt * 28 + ct * 2) * KB;
  const int so1 = R_ST1 + (rt * 14 + ct) * 256, so2 = R_ST2 + (rt * 14 + ct) * 256;
  lds_wait<4>(cx, 7, 4u);
  __builtin_amdgcn_s_setprio(HPRIO);
  u32x4 wf[32];
  Clk ck0 = clk_now();
  {
    const u32x4 sv = {SENT, SENT, SENT, SENT};
    auto fill = [&](int i) {
      const int hi = (i & 1) * KB;
      if (i < 2) st16<16>(sv, ws, l16 + hi, R_OFA + (rt * 32 + sub * 8 + 2 * w) * KB);
      else if (i < 4) { if (ctok) { st16<16>(sv, ws, l16 + hi, xo1); if (i == 2) st4<16>(SENT, ws, lane * 4, so1); } }
      else if (i < 8) st16<16>(sv, ws, l16 + (i - 4) * KB, R_KVB + (hd * 64 + rb * 4 + w) * 4 * KB);
      else if (i < 10) st16<16>(sv, ws, l16 + hi, R_OFB + ((rb * 4 + w) * 16 + hd) * 2 * KB);
      else if (i < 12) { if (ctok && pair == 0) { st16<16>(sv, ws, l16 + hi, xo2); if (i == 10) st4<16>(SENT, ws, lane * 4, so2); } }
      else if (i == 12 && pair == 0 && sub < 2 && w < 2) st16<16>(sv, ws, l16, PAIR_SZ + R_KVA + (rt * 4 + 2 * sub + w) * KB);
      SBAR();
    };
#pragma unroll
    for (int i = 0; i < NFILL0; ++i) { fill(i); __builtin_amdgcn_s_sleep(PACEF); }
    if (pair == 0) {
#pragma unroll 1
      for (int spin = 0; spin < SPIN_CAP; ++spin) {
        bool ok = true;
#pragma unroll
        for (int i = 0; i < 4; ++i) ok &= ld_tag(p.ws + OFF_INI + (xcd * 256 + i * 64 + lane) * 8) == tag;
        if (__builtin_amdgcn_ballot_w64(!ok) == 0ull) break;
        __builtin_amdgcn_s_sleep(8);
      }
      CBAR();
    }
    unsigned c4[16] = {};
    if (pair == 0) {
      const unsigned short* xr = p.x + (size_t)(rt * 32 + r) * D_ + ct * 32 + 4 * h;
      const u32x2 a0 = *(const u32x2*)xr, a1 = *(const u32x2*)(xr + 8), a2 = *(const u32x2*)(xr + 16), a3 = *(const u32x2*)(xr + 24);
      x0 = u32x4{a0[0], a0[1], a1[0], a1[1]}; x1 = u32x4{a2[0], a2[1], a3[0], a3[1]};
      const int co = (h ? (w < 3 ? 16 + hd * 3 + w : 64 + (sub & 1)) : 4 * sub + w) * 2048 + r * 4;
#pragma unroll
      for (int i = 0; i < 16; ++i) c4[i] = ld4<16>(wt, co + i * 128, OFF_CS);
      SBAR();
    }
    const unsigned char* wbsrc = p.ws + OFF_WBT + (hd * 84 + w * 21) * KB + l16;
    const unsigned wbdst = (unsigned)(uintptr_t)(lds + L_WB) + w * 21 * KB;
#pragma unroll
    for (int i = 0; i < 32; ++i) {
      wf[i] = ld16<16>(wt, l16 + (i & 3) * KB, OFF_WOA + (ct * 32 + (i & ~3)) * KB); SBAR();
      if (i % 3 != 2 || i > 29) { const int g = i - (i + 1) / 3; if (g < 21 && pair == 0) glds16_sc1(wbsrc + g * KB, wbdst + g * KB); }
      if ((i & 1) == 0 && NFILL0 + (i >> 1) < 13) fill(NFILL0 + (i >> 1));
      if (i == 10) { asm volatile("s_waitcnt vmcnt(5)" ::: "memory"); lds_sig(cx, 8); }
      if (i % PGRP == PGRP - 1) { SBAR(); if (pair) __builtin_amdgcn_s_sleep(PACE3); else __builtin_amdgcn_s_sleep(PACE); SBAR(); }
    }
    if (pair == 0) {
      float t = 0.f;
#pragma unroll
      for (int i = 0; i < 16; ++i) t += __uint_as_float(c4[i]);
      *(LAS float*)(lds + M_CS + (4 * h + w) * 128 + r * 4) = t;
    }
    asm volatile("s_waitcnt vmcnt(0)" ::: "memory");
  }
  {
    u32x4 keep[2];
    const int s = w & 1;
    __builtin_amdgcn_s_setprio(2);
    lds_wait<2>(cx, 8, 4u);
#pragma unroll 1
    for (int spin = 0; spin < SPIN_CAP; ++spin) {
      if (__builtin_amdgcn_readfirstlane(*(volatile LAS unsigned*)(cx.sig + 11 * 16 + w * 4))) break;
      __builtin_amdgcn_s_sleep(DOZE);
    }
#pragma unroll
    for (int it = 0; it < 2; ++it) {
      const int T = 2 * it + (w >> 1);
      if (it == 0) lds_wait<2>(cx, T, 4u); else lds_wait<1>(cx, T, 4u);
      if (it == 0) { if (w == POST_OA) post(cx, b, 0, ck0, DT_OA); ck0 = clk_now(); }

      const ldsp red = scr + T * 16 * KB;
      f32x4 a0[4], a1[4]; float l4[4];
#pragma unroll
      for (int k = 0; k < 4; ++k) {
        l4[k] = *(LAS float*)(lds + L_SCR + T * 16 * KB + k * 4 * KB + 3 * KB + (32 + r) * 16);
        a0[k] = lds_rf4(red + k * 4 * KB + (2 * s) * KB);
        a1[k] = lds_rf4(red + k * 4 * KB + (2 * s + 1) * KB);
      }
      const float l = (l4[0] + l4[1]) + (l4[2] + l4[3]);
      const f32x4 b0 = (a0[0] + a0[1]) + (a0[2] + a0[3]), b1 = (a1[0] + a1[1]) + (a1[2] + a1[3]);
      const float inv = __builtin_amdgcn_rcpf(l);
      u32x4 o = {pk(b0[0] * inv, b0[1] * inv), pk(b0[2] * inv, b0[3] * inv), pk(b1[0] * inv, b1[1] * inv), pk(b1[2] * inv, b1[3] * inv)};
      if (s == 1 && h == 1) { o[2] = 0; o[3] = 0; }
      if (__builtin_expect(__builtin_amdgcn_ballot_w64(!(l < LBIG)) == 0ull, 1)) st16<16>(o, ws, l16, R_OFA + (rt * 32 + sub * 8 + 2 * T + s) * KB);
      else if (lane == 0) __hip_atomic_fetch_or((LAS unsigned*)(cx.sig + 10 * 16), 1u << T, __ATOMIC_RELAXED, __HIP_MEMORY_SCOPE_WORKGROUP);
      keep[it] = o;
    }
    BAR();
    const unsigned bad = __builtin_amdgcn_readfirstlane(*(LAS unsigned*)(cx.sig + 10 * 16));
#pragma unroll
    for (int it = 0; it < 2; ++it) {
      const int T = 2 * it + (w >> 1);
      if (!((bad >> T) & 1u)) lds_w16(scr + (sub * 8 + 2 * T + s) * KB, keep[it]);
    }
    BAR();
  }
  {
    const f32x16 acc = gemm_lds<32, false>(wf, scr, [&](int i) { if (i == 0 && w == 0) post(cx, b, 1, ck0, DT_X); if (i == 24) BAR();   });
    add_res(acc, x0, x1, x0, x1);
    if (ctok) { st16<16>(x0, ws, l16, xo1); st16<16>(x1, ws, l16 + KB, xo1); SBAR(); st4<16>(__float_as_uint(row_stats(x0, x1, h)), ws, lane * 4, so1); }
  }
  u32x4 wkv[7];
  {
    __builtin_amdgcn_s_setprio(0);
    poll_wait<2>(cx, (((jj & ~15) | (w << 2) | (lane & 3)) << 3) | xcd, 1);
    __builtin_amdgcn_s_sleep(ST_DLY);
    stats_gather(ws, R_ST1 + (rb * 4 + w) * 14 * 256, lds + M_ST + w * 256, lane);
    lds_sig(cx, 12 + w);
    const int kt = 4 * rb - 8 + 2 * w;
    u32x4 hv[8];
    if (kt >= 0) {
      const int prb = kt >> 2;
      __builtin_amdgcn_s_setprio(0);
      poll_wait<HALO_SLP>(cx, (((prb & 1) * 16 + hd) << 3) | (prb >> 1), 4 + (kt & 3) + (lane & 1));
      __builtin_amdgcn_s_setprio(2);
      ld_handoff<8>(hv, ws, l16, R_KVB + (hd * 64 + kt) * 4 * KB);
      chk_handoff<8>(hv, ws, l16, R_KVB + (hd * 64 + kt) * 4 * KB);
    } else {
#pragma unroll
      for (int i = 0; i < 8; ++i) hv[i] = u32x4{0u, 0u, 0u, ((i & 3) == 1 && h == 1) ? 0x3F800000u : 0u};
    }
#pragma unroll
    for (int i = 0; i < 8; ++i) lds_w16(scr + (8 * w + i) * KB, hv[i]);
    lds_sig(cx, 5 + (w >> 1));
    __builtin_amdgcn_s_setprio(0);
#pragma unroll
    for (int i = 0; i < 32; ++i) { wf[i] = ld16<16>(wt, l16 + (i & 3) * KB, OFF_WOB + (ct * 32 + (i & ~3)) * KB); SBAR(); }
#pragma unroll
    for (int i = 0; i < 7; ++i) wkv[i] = ld16<16>(wt, l16 + (i & 3) * KB, OFF_WKV + ((sub & 1) * 28 + 7 * w + (i & 4)) * KB);
    asm volatile("s_waitcnt vmcnt(0)" ::: "memory");
    BAR();
  }
  __builtin_amdgcn_s_setprio(2);
  {
    const f32x16 acc = gemm_lds<32, false>(wf, scr, [&](int i) { if (i == 0 && w == 0 && pair == 0) post(cx, b, 2, ck0, DT_X); if (i == 31 && pair == 0) lds_sig(cx, 32 + 5); });
    add_res(acc, x0, x1, x0, x1);
    if (pair == 1) {
      if (ctok) {
        unsigned short* orow = p.out + (size_t)(rt * 32 + r) * D_ + ct * 32 + 4 * h;
        *(u32x2*)orow = u32x2{x0[0], x0[1]}; *(u32x2*)(orow + 8) = u32x2{x0[2], x0[3]};
        *(u32x2*)(orow + 16) = u32x2{x1[0], x1[1]}; *(u32x2*)(orow + 24) = u32x2{x1[2], x1[3]};
      }
      if (b == 0 && w == 0 && lane == 0) st_tag(p.ws + OFF_EPO, tag);
      return;
    }
    if (ctok) { st16<16>(x0, ws, l16, xo2); st16<16>(x1, ws, l16 + KB, xo2); SBAR(); st4<16>(__float_as_uint(row_stats(x0, x1, h)), ws, lane * 4, so2); }
  }
  {
    u32x4 xf[7];
    const int src = R_XF2 + (rt * 28 + 7 * w) * KB;
    lds_wait<0>(cx, 32 + 5, 4u);
    poll_wait(cx, (((jj & ~3) | (lane & 3)) << 3) | xcd, 2);
    ld_handoff<7>(xf, ws, l16, src);
    chk_handoff<7>(xf, ws, l16, src);
    if (sub < 2) {
      f32x16 acc = {};
      if (sub) {
#pragma unroll
        for (int i = 0; i < 7; ++i) { acc = MFMA(xf[i], wkv[i], acc); SBAR(); lds_w16(scr + (32 + 7 * w + i) * KB, xf[i]); SBAR(); }
      } else {
#pragma unroll
        for (int i = 0; i < 7; ++i) { acc = MFMA(wkv[i], xf[i], acc); SBAR(); lds_w16(scr + (32 + 7 * w + i) * KB, xf[i]); SBAR(); }
      }
      part_write(scr + w * 4 * KB, acc);
    } else {
#pragma unroll
      for (int i = 0; i < 7; ++i) lds_w16(scr + (32 + 7 * w + i) * KB, xf[i]);
    }
    BAR();
    if (sub < 2 && w < 2) kv_finish<true>(lds + L_SCR, 4 * KB, sub, w, lds + M_ST + 4 * 256, lds + M_CS + 7 * 128, lane, ws, PAIR_SZ + R_KVA + rt * 4 * KB, reg + PAIR_SZ + R_FKV + rt * 128, tag);
  }
 }
}

extern "C" void kernel_launch(void* const* d_in, const int* in_sizes, int n_in, void* d_out, int out_size, void* d_ws, size_t ws_size,
                              hipStream_t stream) {
  Params p;
  p.x = (const unsigned short*)d_in[0];
  p.wq = (const unsigned short*)d_in[1];
  p.wk = (const unsigned short*)d_in[2];
  p.wv = (const unsigned short*)d_in[3];
  p.woa = (const unsigned short*)d_in[4];
  p.wqkv = (const unsigned short*)d_in[6];
  p.wob = (const unsigned short*)d_in[7];
  p.out = (unsigned short*)d_out;
  p.ws = (unsigned char*)d_ws;
  k_main<<<256, 512, 0, stream>>>(p);
}
```

```cpp
#include <hip/hip_runtime.h>
#include <hip/hip_bf16.h>
#include <stdint.h>

typedef short bf16x8 __attribute__((ext_vector_type(8)));
typedef short s16x4 __attribute__((ext_vector_type(4)));
typedef float f32x16 __attribute__((ext_vector_type(16)));
typedef float f32x4 __attribute__((ext_vector_type(4)));
typedef float f32x2 __attribute__((ext_vector_type(2)));
typedef unsigned u32x4 __attribute__((ext_vector_type(4)));
typedef unsigned u32x2 __attribute__((ext_vector_type(2)));
typedef __bf16 bf16x2_t __attribute__((ext_vector_type(2)));
#define DI __device__ __forceinline__
#define LAS __attribute__((address_space(3)))
#define GAS __attribute__((address_space(1)))
typedef LAS char* ldsp;

#define MFMA(a, b, c) __builtin_amdgcn_mfma_f32_32x32x16_bf16(__builtin_bit_cast(bf16x8, (a)), __builtin_bit_cast(bf16x8, (b)), (c), 0, 0, 0)
#define BAR() asm volatile("s_waitcnt lgkmcnt(0)\n\ts_barrier" ::: "memory")
#define CBAR() asm volatile("" ::: "memory")
#define SBAR() __builtin_amdgcn_sched_barrier(0)

constexpr int D_ = 448;
constexpr float C2 = 0.18898223650461362f * 1.4426950408889634f;
constexpr float EPS = 1e-5f;
constexpr unsigned SENT = 0xFFFFFFFFu;
constexpr unsigned ONES2 = 0x3F803F80u;
constexpr int SPIN_CAP = 1 << 16;

constexpr int KB = 1024;
constexpr int OFF_WAT = 0;
constexpr int OFF_WBT = OFF_WAT + 448 * KB;
constexpr int OFF_WKV = OFF_WBT + 1344 * KB;
constexpr int OFF_WOA = OFF_WKV + 56 * KB;
constexpr int OFF_WOB = OFF_WOA + 448 * KB;
constexpr int OFF_CS = OFF_WOB + 448 * KB;
constexpr int OFF_EPO = OFF_CS + 132 * KB;
constexpr int OFF_INI = OFF_EPO + KB;
constexpr int OFF_PAIR = OFF_INI + 16 * KB;
constexpr int R_KVA = 0;
constexpr int R_FLG = R_KVA + 256 * KB;
constexpr int R_FKV = R_FLG + 32 * KB;
constexpr int PAIR_SZ = R_FKV + 64 * KB;
constexpr int OFF_BIG = OFF_PAIR + 2 * PAIR_SZ;
constexpr int F_OFA = 0;
constexpr int F_XF1 = F_OFA + 32 * KB;
constexpr int F_KVB = F_XF1 + 28 * KB;
constexpr int F_OFB = F_KVB + 64 * KB;
constexpr int F_XF2 = F_OFB + 32 * KB;
constexpr int F_ST1 = F_XF2 + 28 * KB;
constexpr int F_ST2 = F_ST1 + 4 * KB;
constexpr int RT_SZ = F_ST2 + 4 * KB;
constexpr int WS_BYTES = OFF_BIG + 128 * RT_SZ;

constexpr int PTRUNC = 1, FSKIP = 1, KVE_AT = 8, TOUCH = 1, HPRIO = 0, DT_OA = 1900, DT_X = 1850, DT_KV = 3700;
constexpr int DT_OB = 850, DT_UNIT = 240, X1_AHEAD = 8, WA1 = 8, PRO_DLY = 2, PACE3 = 8, DT_Q = 3000, QS = 14;
constexpr int ST_DLY = 20, CODE_KB = 44, PGRP = 2, DOZE = 14, NFILL0 = 6, PACEF = 2, HALO_SLP = 8, PACE = 3;
constexpr int POST_OA = 2, POST_KV = 12;

DI unsigned pk(float a, float b) { f32x2 v = {a, b}; bf16x2_t r = __builtin_convertvector(v, bf16x2_t); return __builtin_bit_cast(unsigned, r); }
DI unsigned pkt(float a, float b) { return PTRUNC ? __builtin_amdgcn_perm(__float_as_uint(b), __float_as_uint(a), 0x07060302u) : pk(a, b); }
DI float dot2(unsigned a, unsigned b, float c) { return __builtin_amdgcn_fdot2_f32_bf16(__builtin_bit_cast(bf16x2_t, a), __builtin_bit_cast(bf16x2_t, b), c, false); }
DI float bflo(unsigned u) { return __uint_as_float(u << 16); }
DI float bfhi(unsigned u) { return __uint_as_float(u & 0xFFFF0000u); }
DI float xhalf_add(float v) { auto r = __builtin_amdgcn_permlane32_swap(__float_as_uint(v), __float_as_uint(v), false, false); return __uint_as_float(r[0]) + __uint_as_float(r[1]); }
DI float xhalf_max(float v) { auto r = __builtin_amdgcn_permlane32_swap(__float_as_uint(v), __float_as_uint(v), false, false); return __builtin_fmaxf(__uint_as_float(r[0]), __uint_as_float(r[1])); }
DI float from_hi(float v) { auto r = __builtin_amdgcn_permlane32_swap(__float_as_uint(v), __float_as_uint(v), false, false); return __uint_as_float(r[1]); }
DI float max3f(float a, float b, float c) { return __builtin_fmaxf(__builtin_fmaxf(a, b), c); }
DI float max16(const f32x16& s, float m) {
#pragma unroll
  for (int i = 0; i < 16; i += 2) m = max3f(m, s[i], s[i + 1]);
  return m;
}
typedef __amdgpu_buffer_rsrc_t rsrc_t;
DI rsrc_t mkrs(const void* p, unsigned n) { return __builtin_amdgcn_make_buffer_rsrc((void*)p, 0, n, 0x00020000); }
template <int AUX> DI u32x4 ld16(rsrc_t rs, int voff, int soff) { return __builtin_amdgcn_raw_buffer_load_b128(rs, voff, soff, AUX); }
template <int AUX> DI void st16(u32x4 v, rsrc_t rs, int voff, int soff) { __builtin_amdgcn_raw_buffer_store_b128(v, rs, voff, soff, AUX); }
template <int AUX> DI unsigned ld4(rsrc_t rs, int voff, int soff) { return __builtin_amdgcn_raw_buffer_load_b32(rs, voff, soff, AUX); }
template <int AUX> DI void st4(unsigned v, rsrc_t rs, int voff, int soff) { __builtin_amdgcn_raw_buffer_store_b32(v, rs, voff, soff, AUX); }
DI ldsp opaque(ldsp p) { unsigned a = (unsigned)(uintptr_t)p; asm volatile("" : "+v"(a)); return (ldsp)(uintptr_t)a; }
typedef unsigned long long u64;
DI u64 ld_tag(const unsigned char* a) { return __hip_atomic_load((GAS u64*)a, __ATOMIC_RELAXED, __HIP_MEMORY_SCOPE_AGENT); }
DI void st_tag(unsigned char* a, u64 v) { __hip_atomic_store((GAS u64*)a, v, __ATOMIC_RELAXED, __HIP_MEMORY_SCOPE_AGENT); }
DI u32x4 lds_r16(ldsp p) { return *(LAS u32x4*)p; }
DI f32x4 lds_rf4(ldsp p) { return *(LAS f32x4*)p; }
DI void lds_w16(ldsp p, u32x4 v) { *(LAS u32x4*)p = v; }
DI void lds_wf4(ldsp p, f32x4 v) { *(LAS f32x4*)p = v; }
DI unsigned umax3(unsigned a, unsigned b, unsigned c) { return max(max(a, b), c); }
DI unsigned smax(unsigned m, u32x4 v) { return umax3(umax3(m, v[0], v[1]), v[2], v[3]); }
DI void glds16_sc1(const void* g, unsigned lds_base) {
  unsigned sv;
  asm volatile("s_mov_b32 %0, m0\n\ts_mov_b32 m0, %2\n\ts_nop 0\n\tglobal_load_lds_dwordx4 %1, off sc1\n\ts_mov_b32 m0, %0" : "=&s"(sv) : "v"(g), "s"(lds_base) : "memory");
}
DI void glds16(const void* g, unsigned lds_base) {
  unsigned sv;
  asm volatile("s_mov_b32 %0, m0\n\ts_mov_b32 m0, %2\n\ts_nop 0\n\tglobal_load_lds_dwordx4 %1, off\n\ts_mov_b32 m0, %0" : "=&s"(sv) : "v"(g), "s"(lds_base) : "memory");
}
DI u32x4 pack8(const f32x16& a, int s) {
  u32x4 r;
#pragma unroll
  for (int i = 0; i < 4; ++i) r[i] = pk(a[8 * s + 2 * i], a[8 * s + 2 * i + 1]);
  return r;
}
template <int N, int GS = 4 * KB> DI void ld_handoff(u32x4 (&v)[N], rsrc_t rs, int voff, int soff) {
#pragma unroll
  for (int i = 0; i < N; ++i) { v[i] = ld16<16>(rs, voff + (i & 3) * KB, soff + (i >> 2) * GS); SBAR(); }
}
template <int N> DI bool any_sent(const u32x4 (&v)[N]) {
  unsigned m = 0;
#pragma unroll
  for (int i = 0; i < N; ++i) m = smax(m, v[i]);
  return __builtin_amdgcn_ballot_w64(m == SENT) != 0ull;
}
template <int N, int GS = 4 * KB> DI void chk_handoff(u32x4 (&v)[N], rsrc_t rs, int voff, int soff) {
  if (__builtin_expect(any_sent<N>(v), 0)) {
#pragma unroll 1
    for (int spin = 0; spin < SPIN_CAP; ++spin) {
      CBAR();
#pragma unroll
      for (int i = 0; i < N; ++i)
        if (__builtin_amdgcn_ballot_w64(smax(0u, v[i]) == SENT) != 0ull) v[i] = ld16<16>(rs, voff + (i & 3) * KB, soff + (i >> 2) * GS);
      if (!any_sent<N>(v)) break;
    }
  }
}

struct Params {
  const unsigned short *x, *wq, *wk, *wv, *woa, *wqkv, *wob;
  unsigned short* out;
  unsigned char* ws;
};

constexpr int L_SCR = 0;
constexpr int L_WB = 64 * KB;
constexpr int L_QF = 148 * KB;
constexpr int L_MISC = L_QF + 8 * KB;
constexpr int M_SIG = L_MISC;
constexpr int M_ST = M_SIG + 768;
constexpr int M_CS = M_ST + 1280;
constexpr int L_BYTES = M_CS + 1024;
constexpr int XI_KS = 1056;
constexpr int P_KV = 0, P_KVW = 112 * 56, P_XI = 25600, P_WQ = P_XI + 28 * XI_KS;
static_assert(L_BYTES <= 160 * KB && P_WQ + 448 * 224 + 64 <= L_MISC, "LDS");

DI void exp_pack(const f32x16& s, u32x4& p0, u32x4& p1) {
#pragma unroll
  for (int i = 0; i < 4; ++i) p0[i] = pk(__builtin_amdgcn_exp2f(s[2 * i]), __builtin_amdgcn_exp2f(s[2 * i + 1]));
#pragma unroll
  for (int i = 0; i < 4; ++i) p1[i] = pk(__builtin_amdgcn_exp2f(s[8 + 2 * i]), __builtin_amdgcn_exp2f(s[8 + 2 * i + 1]));
}
DI void set_shift(u32x4& q1, float m, int h, unsigned hi31) {
  const unsigned a = pk(m, m); const float r1 = m - bflo(a);
  const unsigned c = pk(r1, r1); const float r2 = r1 - bflo(c);
  const unsigned e = pk(r2, 0.f);
  const unsigned d2 = ((a & 0xFFFFu) | (c << 16)) ^ 0x80008000u, d3 = (e ^ 0x00008000u) | hi31;
  q1[2] = h ? d2 : q1[2]; q1[3] = h ? d3 : q1[3];
}
#define PIN(x) asm volatile("" : "+v"(x))
template <bool DOQK, bool DOPVP, class FA, class FB, class FC>
DI void unit(const f32x16& s, f32x16& sn, const f32x16& cn, f32x16& o, f32x16& op, u32x4& pp, const u32x4& kn0, const u32x4& kn1, const u32x4& qn0,
             const u32x4& qn1, const u32x4& v0, const u32x4& v1p, FA ha, FB hb, FC hc) {
  float e[16]; u32x4 p0;
#define E_(i) e[i] = __builtin_amdgcn_exp2f(s[i]); PIN(e[i]); SBAR()
#define C0_(i) p0[i] = pkt(e[2 * i], e[2 * i + 1]); PIN(p0[i]); SBAR()
#define C1_(i) pp[i] = pkt(e[8 + 2 * i], e[8 + 2 * i + 1]); PIN(pp[i]); SBAR()
  SBAR();
  if (DOQK) { sn = MFMA(kn0, qn0, cn); SBAR(); }
  E_(0); E_(1); E_(2); E_(3);
  if (DOPVP) { op = MFMA(v1p, pp, op); SBAR(); }
  ha(); SBAR();
  E_(4); C0_(0); E_(5); C0_(1); E_(6); E_(7);
  if (DOQK) { sn = MFMA(kn1, qn1, sn); SBAR(); }
  hb(); SBAR();
  E_(8); C0_(2); E_(9); C0_(3); E_(10); E_(11);
  o = MFMA(v0, p0, o); SBAR();
  hc(); SBAR();
  E_(12); C1_(0); E_(13); C1_(1); E_(14); E_(15); C1_(2); C1_(3);
}

struct Ctx { unsigned char* flg; ldsp sig; int lane; int w; };
DI unsigned now32() { return (unsigned)__builtin_amdgcn_s_memrealtime(); }
struct Clk { unsigned c, r; };
DI Clk clk_now() { Clk k; k.c = (unsigned)__builtin_amdgcn_s_memtime(); k.r = now32(); return k; }
DI unsigned eta(const Clk& k0, int dcyc) {
  const Clk k = clk_now();
  return (k.r + (unsigned)((float)dcyc * (float)(k.r - k0.r) * __builtin_amdgcn_rcpf((float)(k.c - k0.c)))) | 1u;
}
DI void post(const Ctx& c, int b, int slot, const Clk& k0, int dcyc) {
  const unsigned t = eta(k0, dcyc);
  if (c.lane == 0) __hip_atomic_store((GAS unsigned*)(c.flg + b * 128 + slot * 4), t, __ATOMIC_RELAXED, __HIP_MEMORY_SCOPE_AGENT);
}
DI unsigned flag_ld(const Ctx& c, int pb, int slot) { return __hip_atomic_load((GAS unsigned*)(c.flg + pb * 128 + slot * 4), __ATOMIC_RELAXED, __HIP_MEMORY_SCOPE_AGENT); }
template <int SLP = 1> DI void poll_wait(const Ctx& c, int pb, int slot, unsigned f = 0u) {
#pragma unroll 1
  for (int spin = 0; spin < SPIN_CAP; ++spin) {
    if (__builtin_amdgcn_ballot_w64(f == 0u) == 0ull) break;
    if (spin) __builtin_amdgcn_s_sleep(SLP);
    f = __hip_atomic_load((GAS unsigned*)(c.flg + pb * 128 + slot * 4), __ATOMIC_RELAXED, __HIP_MEMORY_SCOPE_AGENT);
  }
  unsigned t = __builtin_amdgcn_readlane(f, 0);
#pragma unroll
  for (int i = 1; i < 4; ++i) { const unsigned u = __builtin_amdgcn_readlane(f, i); t = (int)(u - t) > 0 ? u : t; }
#pragma unroll 1
  for (int spin = 0; spin < 256; ++spin) {
    const int d = (int)(t - now32());
    if (d <= 0) break;
    if (d > 30) __builtin_amdgcn_s_sleep(6); else __builtin_amdgcn_s_sleep(1);
  }
  CBAR();
}
DI void lds_sig(const Ctx& c, int idx) {
  CBAR();
  *(volatile LAS unsigned*)(c.sig + idx * 16 + c.w * 4) = 1u;
  CBAR();
}
template <int SLP> DI void lds_wait(const Ctx& c, int idx, unsigned n) {
#pragma unroll 1
  for (int spin = 0; spin < (SPIN_CAP << 6); ++spin) {
    const u32x4 v = *(volatile LAS u32x4*)(c.sig + idx * 16);
    if (__builtin_amdgcn_readfirstlane(v[0] + v[1] + v[2] + v[3]) >= n) break;
    if (SLP) __builtin_amdgcn_s_sleep(SLP);
  }
  CBAR();
}
template <int N, bool SWAP, class F> DI f32x16 gemm_lds(const u32x4 (&wr)[N], ldsp bb, F mid) {
  f32x16 acc = {};
  u32x4 bf[8];
#pragma unroll
  for (int i = 0; i < 8; ++i) bf[i] = lds_r16(bb + i * KB);
  SBAR();
#pragma unroll
  for (int i = 0; i < N; ++i) {
    acc = SWAP ? MFMA(bf[i & 7], wr[i], acc) : MFMA(wr[i], bf[i & 7], acc);
    SBAR();
    if (i + 8 < N) bf[i & 7] = lds_r16(bb + (i + 8) * KB);
    mid(i);
    SBAR();
  }
  return acc;
}
DI void add_res(const f32x16& a, const u32x4& x0, const u32x4& x1, u32x4& y0, u32x4& y1) {
#pragma unroll
  for (int i = 0; i < 4; ++i) {
    y0[i] = pk(a[2 * i] + bflo(x0[i]), a[2 * i + 1] + bfhi(x0[i]));
    y1[i] = pk(a[8 + 2 * i] + bflo(x1[i]), a[8 + 2 * i + 1] + bfhi(x1[i]));
  }
}
DI float row_stats(const u32x4& y0, const u32x4& y1, int h) {
  float s0 = 0.f, s1 = 0.f, q0 = 0.f, q1 = 0.f;
#pragma unroll
  for (int i = 0; i < 4; ++i) { s0 = dot2(y0[i], ONES2, s0); s1 = dot2(y1[i], ONES2, s1); q0 = dot2(y0[i], y0[i], q0); q1 = dot2(y1[i], y1[i], q1); }
  const float s = xhalf_add(s0 + s1), q = xhalf_add(q0 + q1);
  return h ? q : s;
}
DI void stats_finish(float v, float& mu, float& rs) {
  auto t = __builtin_amdgcn_permlane32_swap(__float_as_uint(v), __float_as_uint(v), false, false);
  mu = __uint_as_float(t[0]) * (1.f / D_);
  rs = __builtin_amdgcn_rsqf(__builtin_fmaxf(__uint_as_float(t[1]) * (1.f / D_) - mu * mu, 0.f) + EPS);
}
DI void stats_gather(rsrc_t ws, int so, ldsp dst, int lane) {
#pragma unroll 1
  for (int spin = 0; spin < SPIN_CAP; ++spin) {
    CBAR();
    unsigned sv[14], mx = 0;
#pragma unroll
    for (int i = 0; i < 14; ++i) sv[i] = ld4<16>(ws, lane * 4 + i * 256, so);
    float t0 = 0.f, t1 = 0.f, mu, rs;
#pragma unroll
    for (int i = 0; i < 14; i += 2) { t0 += __uint_as_float(sv[i]); t1 += __uint_as_float(sv[i + 1]); mx = umax3(mx, sv[i], sv[i + 1]); }
    stats_finish(t0 + t1, mu, rs);
    *(LAS float*)(dst + lane * 4) = lane >= 32 ? rs : mu;
    if (__builtin_amdgcn_ballot_w64(mx == SENT) == 0ull) break;
  }
}
DI void norm_pack(const f32x16& o, int h, u32x4& y0, u32x4& y1) {
  const float inv = __builtin_amdgcn_rcpf(from_hi(o[12]));
#pragma unroll
  for (int i = 0; i < 4; ++i) { y0[i] = pk(o[2 * i] * inv, o[2 * i + 1] * inv); y1[i] = pk(o[8 + 2 * i] * inv, o[8 + 2 * i + 1] * inv); }
  if (h == 1) { y1[2] = 0; y1[3] = 0; }
}
constexpr float LBIG = 1e30f;
constexpr unsigned NEG31 = 0xF1490000u;

DI u32x4 tr_frag(ldsp a, int hi) {
  const s16x4 lo_ = __builtin_amdgcn_ds_read_tr16_b64_v4i16((LAS s16x4*)a), hi_ = __builtin_amdgcn_ds_read_tr16_b64_v4i16((LAS s16x4*)(a + hi));
  return __builtin_bit_cast(u32x4, __builtin_shufflevector(lo_, hi_, 0, 1, 2, 3, 4, 5, 6, 7));
}
DI void part_write(ldsp d, const f32x16& a) {
#pragma unroll
  for (int g = 0; g < 4; ++g) lds_wf4(d + g * KB, f32x4{a[4 * g], a[4 * g + 1], a[4 * g + 2], a[4 * g + 3]});
}
template <bool LN>
DI void kv_finish(ldsp part, int pstr, int isV, int s, ldsp st, ldsp cs, int lane, rsrc_t ws, int kvo, unsigned char* fkv, u64 tag) {
  const int r = lane & 31, h = lane >> 5;
  f32x4 a[2][4], c[2] = {}, m[2] = {};
  float l0 = 0.f, l1 = 0.f;
#pragma unroll
  for (int k = 0; k < 4; ++k) { a[0][k] = lds_rf4(part + k * pstr + 2 * s * KB + lane * 16); a[1][k] = lds_rf4(part + k * pstr + (2 * s + 1) * KB + lane * 16); }
  if (LN) {
    l0 = *(LAS float*)((isV ? cs : st) + r * 4); l1 = *(LAS float*)(st + 128 + r * 4);
#pragma unroll
    for (int g = 0; g < 2; ++g) { c[g] = lds_rf4((isV ? st : cs) + (16 * s + 8 * g + 4 * h) * 4); m[g] = lds_rf4(st + 128 + (16 * s + 8 * g + 4 * h) * 4); }
  }
  SBAR();
  float v[8];
#pragma unroll
  for (int i = 0; i < 8; ++i) v[i] = (a[i >> 2][0][i & 3] + a[i >> 2][1][i & 3]) + (a[i >> 2][2][i & 3] + a[i >> 2][3][i & 3]);
  if (LN) {
#pragma unroll
    for (int i = 0; i < 8; ++i) v[i] = (v[i] - l0 * c[i >> 2][i & 3]) * (isV ? m[i >> 2][i & 3] : l1);
  }
  u32x4 o = {pk(v[0], v[1]), pk(v[2], v[3]), pk(v[4], v[5]), pk(v[6], v[7])};
  if (isV) { if (r >= 28) { const unsigned f = r == 28 ? ONES2 : 0u; o = u32x4{f, f, f, f}; } }
  else if (s == 1 && h == 1) { o[2] = ONES2; o[3] = 0x00003F80u; }
  st16<16>(o, ws, lane * 16, kvo + (2 * isV + s) * KB);
  asm volatile("s_waitcnt vmcnt(0)" ::: "memory");
  if (lane < 8) st_tag(fkv + lane * 64 * 128 + (2 * isV + s) * 8, tag);
}

DI void phase0_h(const Params& p, const ldsp lds, u64 epo, u64& tag) {
  const int tid = threadIdx.x & 255, lane = tid & 63, r = lane & 31, h = lane >> 5;
  const int w = __builtin_amdgcn_readfirstlane(tid >> 6);
  const int b = blockIdx.x, xcd = b & 7, jj = b >> 3, rt = 8 * xcd + (jj >> 2), sub = jj & 3;
  const rsrc_t ws = mkrs(p.ws, WS_BYTES);
  const Ctx cx = {p.ws, lds + M_SIG + 512, lane, w};
  const ldsp priv = lds + P_KV + w * P_KVW;
  if (sub < 2) {
    const unsigned char* src = (const unsigned char*)(sub ? p.wv : p.wk) + w * P_KVW + lane * 16;
#pragma unroll
    for (int i = 0; i < 7; ++i) if (i < 6 || lane < 8) glds16(src + i * KB, (unsigned)(uintptr_t)priv + i * KB);
  }
  const int q8 = tid & 7, row8 = tid >> 3;
  u32x4 xv[7];
  {
    const unsigned short* xr = p.x + (size_t)(rt * 32 + row8) * D_ + q8 * 8;
#pragma unroll
    for (int i = 0; i < 7; ++i) xv[i] = *(const u32x4*)(xr + i * 64);
  }
  SBAR();
  const int tt = b >> 2, tks = (b & 3) * 7 + 2 * w, wog = b * 4 + w;
  u32x2 tv[4];
  unsigned te[8];
  const bool dead = (lane & 7) == 7;
  {
    const int pitch = tt < 16 ? 448 : 1344;
    const unsigned short* src = (tt < 16 ? p.wq + 28 * tt : p.wqkv + ((tt - 16) % 3) * 448 + ((tt - 16) / 3) * 28) + (size_t)(tks * 16 + (lane >> 3)) * pitch + (dead ? 0 : (lane & 7) * 4);
#pragma unroll
    for (int i = 0; i < 4; ++i) tv[i] = *(const u32x2*)(src + (size_t)((w == 3 && i >= 2) ? 0 : i * 8) * pitch);
    int g = wog < 896 ? wog : 0;
    const unsigned short* wo = g >= 448 ? p.wob : p.woa;
    if (g >= 448) g -= 448;
    const int ct = g >> 5, ks = g & 31, head = ks >> 1;
    const int k0 = head * 28 + 16 * (ks & 1) + 4 * h, kmax = head * 28 + 28;
#pragma unroll
    for (int j = 0; j < 8; ++j) {
      const int k = k0 + 8 * (j >> 2) + (j & 3);
      te[j] = wo[(k < kmax ? k : 0) * 448 + 32 * ct + r];
    }
  }
  SBAR();
  {
    float sm[2] = {}, sq[2] = {};
#pragma unroll
    for (int i = 0; i < 7; ++i)
#pragma unroll
      for (int d = 0; d < 4; ++d) { sm[d & 1] = dot2(xv[i][d], ONES2, sm[d & 1]); sq[d & 1] = dot2(xv[i][d], xv[i][d], sq[d & 1]); }
    float a = sm[0] + sm[1], c = sq[0] + sq[1];
#define DPP_ADD(v, ctl) v += __uint_as_float(__builtin_amdgcn_update_dpp(0u, __float_as_uint(v), ctl, 0xF, 0xF, true))
    DPP_ADD(a, 0xB1); DPP_ADD(c, 0xB1); DPP_ADD(a, 0x4E); DPP_ADD(c, 0x4E); DPP_ADD(a, 0x141); DPP_ADD(c, 0x141);
    const float mu = a * (1.f / D_), rs = __builtin_amdgcn_rsqf(__builtin_fmaxf(c * (1.f / D_) - mu * mu, 0.f) + EPS), nb = -mu * rs;
#pragma unroll
    for (int i = 0; i < 7; ++i) {
      unsigned y[4];
#pragma unroll
      for (int d = 0; d < 4; ++d) y[d] = pk(__builtin_fmaf(bflo(xv[i][d]), rs, nb), __builtin_fmaf(bfhi(xv[i][d]), rs, nb));
      const ldsp dst = lds + P_XI + (4 * i + (q8 >> 1)) * XI_KS + row8 * 16 + (q8 & 1) * 8;
      *(LAS u32x2*)dst = u32x2{y[0], y[1]};
      *(LAS u32x2*)(dst + 512) = u32x2{y[2], y[3]};
    }
  }
  lds_sig(cx, 0);
  tag = (((u64)__builtin_amdgcn_readfirstlane((unsigned)(epo >> 32)) << 32) | __builtin_amdgcn_readfirstlane((unsigned)epo)) + 1ull;
  const int i16 = lane & 15, trow = 4 * h + (i16 >> 2), tcol = ((lane >> 4) & 1) * 32 + (i16 & 3) * 8;
  if (sub < 2) {
    u32x4 wf[7];
#pragma unroll
    for (int i = 0; i < 7; ++i) wf[i] = tr_frag(priv + (16 * i + trow) * 56 + tcol, 8 * 56);
    lds_wait<0>(cx, 0, 4u);
    f32x16 acc = {};
#pragma unroll
    for (int i = 0; i < 7; ++i) { const u32x4 xf = lds_r16(lds + P_XI + (7 * w + i) * XI_KS + lane * 16); acc = sub ? MFMA(xf, wf[i], acc) : MFMA(wf[i], xf, acc); }
    part_write(priv + lane * 16, acc);
    lds_sig(cx, 1);
    if (w < 2) {
      lds_wait<0>(cx, 1, 4u);
      kv_finish<false>(lds + P_KV, P_KVW, sub, w, (ldsp)0, (ldsp)0, lane, ws, OFF_PAIR + R_KVA + rt * 4 * KB, p.ws + OFF_PAIR + R_FKV + rt * 128, tag);
    }
    if (rt == 0) {
      float cs = 0.f;
#pragma unroll
      for (int i = 0; i < 7; ++i) {
        st16<16>(wf[i], ws, lane * 16, OFF_WKV + (sub * 28 + 7 * w + i) * KB);
#pragma unroll
        for (int d = 0; d < 4; ++d) cs = dot2(wf[i][d], ONES2, cs);
      }
      cs = xhalf_add(cs);
      st4<16>(h ? 0u : __float_as_uint(cs), ws, lane * 4, OFF_CS + ((64 + sub) * 16 + 4 * w) * 128);
      st4<16>(0u, ws, lane * 4 + 256, OFF_CS + ((64 + sub) * 16 + 4 * w) * 128);
    }
  }
  {
    st4<16>(0u, ws, w == 0 ? h * PAIR_SZ + r * 4 : WS_BYTES, OFF_PAIR + R_FLG + b * 128);
    const ldsp img = priv + 4 * KB;
#pragma unroll
    for (int i = 0; i < 4; ++i) *(LAS u32x2*)(img + i * 512 + lane * 8) = dead ? u32x2{0u, 0u} : tv[i];
    CBAR();
    float cs = 0.f;
#pragma unroll
    for (int ks = 0; ks < 2; ++ks) {
      const u32x4 o = tr_frag(img + ks * KB + trow * 64 + tcol, 512);
      if (ks == 0 || w < 3) {
        st16<16>(o, ws, lane * 16, (tt * 28 + tks + ks) * KB);
#pragma unroll
        for (int i = 0; i < 4; ++i) cs = dot2(o[i], ONES2, cs);
      }
    }
    u32x4 o;
#pragma unroll
    for (int i = 0; i < 4; ++i) { asm volatile("" : "+v"(te[2 * i]), "+v"(te[2 * i + 1])); o[i] = te[2 * i] | (te[2 * i + 1] << 16); }
    if ((wog & 1) && h == 1) { o[2] = 0u; o[3] = 0u; }
    if (wog < 896) st16<16>(o, ws, lane * 16, OFF_WOA + wog * KB);
    cs = xhalf_add(cs);
    if (h == 0) st4<16>(__float_as_uint(cs), ws, r * 4, OFF_CS + wog * 128);
    asm volatile("s_waitcnt vmcnt(0)" ::: "memory");
    unsigned k = 0;
    if (lane == 0) k = __hip_atomic_fetch_add((LAS unsigned*)(cx.sig + 2 * 16), 1u, __ATOMIC_RELAXED, __HIP_MEMORY_SCOPE_WORKGROUP);
    if (__builtin_amdgcn_readfirstlane(k) == 3u && lane < 8) st_tag(p.ws + OFF_INI + (lane * 256 + b) * 8, tag);
  }
}
DI void phase0_c(const Params& p, const ldsp lds, u64 epo, u64& tag, u32x4& q0, u32x4& q1) {
  const int lane = threadIdx.x & 63, h = lane >> 5;
  const int w = __builtin_amdgcn_readfirstlane(threadIdx.x >> 6), sub = (blockIdx.x >> 3) & 3;
  const Ctx cx = {p.ws, lds + M_SIG + 512, lane, w};
  __builtin_amdgcn_s_sleep(PRO_DLY);
  if (lane < 56) {
    const unsigned short* src = p.wq + (size_t)(4 * w + lane / 14) * 448 + sub * 112 + (lane % 14) * 8;
#pragma unroll 4
    for (int i = 0; i < 28; ++i) glds16(src + (size_t)i * 16 * 448, (unsigned)(uintptr_t)(lds + P_WQ) + (i * 16 + 4 * w) * 224);
  }
  asm volatile("s_waitcnt vmcnt(0)" ::: "memory");
  tag = (((u64)__builtin_amdgcn_readfirstlane((unsigned)(epo >> 32)) << 32) | __builtin_amdgcn_readfirstlane((unsigned)epo)) + 1ull;
  lds_sig(cx, 3);
  lds_wait<1>(cx, 3, 4u); lds_wait<1>(cx, 0, 4u);
  const int i16 = lane & 15;
  const ldsp wb0 = opaque(lds + P_WQ + (4 * h + (i16 >> 2)) * 224 + w * 56 + ((lane >> 4) & 1) * 32 + (i16 & 3) * 8), wb1 = opaque(wb0 + 14 * 16 * 224);
  const ldsp xb = lds + P_XI + lane * 16;
  u32x4 wr[4], xr[4];
#define PRE_LD(i) do { wr[(i) & 3] = tr_frag(((i) < 14 ? wb0 : wb1) + ((i) % 14) * 16 * 224, 8 * 224); xr[(i) & 3] = lds_r16(xb + (i) * XI_KS); } while (0)
  PRE_LD(0); PRE_LD(1); PRE_LD(2); SBAR();
  f32x16 acc = {};
#pragma unroll
  for (int i = 0; i < 28; ++i) {
    if (i + 3 < 28) PRE_LD(i + 3);
    SBAR();
    acc = MFMA(wr[i & 3], xr[i & 3], acc);
    SBAR();
  }
  lds_sig(cx, 4);
#pragma unroll
  for (int i = 0; i < 16; ++i) acc[i] *= C2;
  q0 = pack8(acc, 0); q1 = pack8(acc, 1);
  lds_wait<0>(cx, 4, 4u);
}

__global__ void __launch_bounds__(512, 2) k_main(Params p) {
  __shared__ __attribute__((aligned(16))) char smem[L_BYTES];
  const ldsp lds = (ldsp)smem;
  const int tid = threadIdx.x;
  const int w8 = __builtin_amdgcn_readfirstlane(tid >> 6);
  const rsrc_t wt = mkrs(p.ws, OFF_PAIR);
  auto nop = []() {};
  const f32x16 Z = {};
  const u64 epo = ld_tag(p.ws + OFF_EPO);
  u64 tag;
  if (tid < 192) *(LAS unsigned*)(lds + M_SIG + tid * 4) = 0u;
  if (CODE_KB && w8 == 3) {
    const unsigned char* pc = (const unsigned char*)(__builtin_amdgcn_s_getpc() & ~127ull);
    const int ln = (((blockIdx.x >> 3) * 64 + (tid & 63)) % (CODE_KB * 8)) * 128;
    glds16(pc + ln, (unsigned)(uintptr_t)(lds + M_ST));
  }
  BAR();
#define RT(x) (big0 + (x) * 2 * RT_SZ)
#define PAIR_SETUP() \
    int lane = tid & 63, b = blockIdx.x, w = w8 & 3; \
    asm volatile("" : "+v"(lane), "+s"(b), "+s"(w)); \
    lane &= 63; b &= 255; w &= 3; \
    const int r = lane & 31, h = lane >> 5, l16 = lane * 16, xcd = b & 7, jj = b >> 3; \
    const int rt = 8 * xcd + (jj >> 2), sub = jj & 3;          \
    const int rb = 2 * xcd + (jj >> 4), hd = jj & 15;          \
    const ldsp scr = lds + L_SCR + l16; \
    unsigned char* const reg = p.ws + OFF_PAIR + pair * PAIR_SZ; \
    const rsrc_t ws = mkrs(reg, WS_BYTES - OFF_PAIR - pair * PAIR_SZ); \
    const int big0 = (2 - pair) * PAIR_SZ + pair * RT_SZ; \
    const Ctx cx = {reg + R_FLG, lds + M_SIG + pair * 256, lane, w}

  if (w8 < 4) {
    u32x4 qn0, qn1;
    unsigned kve = 0u;
    phase0_c(p, lds, epo, tag, qn0, qn1);
#pragma unroll 1
   for (int pair = 0; pair < 2; ++pair) {
    PAIR_SETUP();
    const unsigned char* const kpa = reg + R_FKV + (xcd * 64 + 4 * ((2 * xcd + 1 - (lane >> 2)) & 15) + w) * 128 + (lane & 3) * 8;
    u64 miss = 0ull, kvf;
    unsigned chk = pair, pend = 1u;
    int kvi = 0;
    auto kv_mask = [&]() { miss = __builtin_amdgcn_ballot_w64(kvf != tag); };
    auto kv_next = [&]() -> int {
      if (__builtin_expect(((unsigned)(miss >> (4 * kvi)) & 15u) != 0u, 0)) {
#pragma unroll 1
        for (int spin = 0; spin < SPIN_CAP; ++spin) {
          kvf = ld_tag(kpa);
          kv_mask();
          if (((unsigned)(miss >> (4 * kvi)) & 15u) == 0u) break;
          __builtin_amdgcn_s_sleep(2);
        }
        CBAR();
      }
      const int o = R_KVA + (4 * ((2 * xcd + 1 - kvi) & 15) + w) * 4 * KB;
      ++kvi;
      return o;
    };
    {
      u32x4 q[4][2], K[4][2], V[4][2];
      int kb[4];
      if (pair) {
        poll_wait(cx, ((((4 * (lane & 1) + w) << 2) | ((lane >> 1) & 1)) << 3) | xcd, 12, kve);
      } else {
#pragma unroll 1
        for (int spin = 0; spin < SPIN_CAP; ++spin) {
          if (((unsigned)__builtin_amdgcn_ballot_w64(ld_tag(kpa) != tag) & 0xFFu) == 0u) break;
          __builtin_amdgcn_s_sleep(2);
        }
        CBAR();
      }
#pragma unroll
      for (int t = 0; t < 2; ++t) {
        kb[t] = kv_next();
        K[t][0] = ld16<16>(ws, l16, kb[t]); K[t][1] = ld16<16>(ws, l16 + KB, kb[t]);
        V[t][0] = ld16<16>(ws, l16 + 2 * KB, kb[t]); V[t][1] = ld16<16>(ws, l16 + 3 * KB, kb[t]);
        SBAR();
      }
#pragma unroll
      for (int t = 2; t < 4; ++t) { kb[t] = kb[0]; K[t][0] = K[t][1] = V[t][0] = V[t][1] = u32x4{}; }
      kvf = ld_tag(kpa);
      f32x16 o0 = {}, o1 = {}, o2 = {}, o3 = {};
      PIN(o0); PIN(o1); PIN(o2); PIN(o3);
      if (h == 1) { qn1[2] = 0u; qn1[3] = 0u; }
      lds_w16(lds + L_QF + l16 + 2 * w * KB, qn0);
      if (pair) chk_handoff<2>(K[0], ws, l16, kb[0]);
      {
        f32x16 s0 = MFMA(K[0][0], qn0, Z);
        s0 = MFMA(K[0][1], qn1, s0);
        set_shift(qn1, xhalf_max(max16(s0, s0[0])), h, 0u);
        lds_w16(lds + L_QF + l16 + (2 * w + 1) * KB, qn1);
        lds_sig(cx, 7);
      }
      if (pair) chk_handoff<2>(V[0], ws, l16 + 2 * KB, kb[0]);
      lds_wait<0>(cx, 7, 4u);
#pragma unroll
      for (int a = 0; a < 4; ++a) { q[a][0] = lds_r16(lds + L_QF + l16 + 2 * a * KB); q[a][1] = lds_r16(lds + L_QF + l16 + (2 * a + 1) * KB); }
      f32x16 sa = MFMA(K[0][0], q[0][0], Z), sb;
      sa = MFMA(K[0][1], q[0][1], sa);
      u32x4 pp = {};
#define TILE(c, LD, LDV1) { \
        constexpr int n = (c + 1) & 3, pv = (c + 3) & 3; \
        auto ldV1 = [&]() { if (LDV1) V[pv][1] = ld16<16>(ws, l16 + 3 * KB, kb[pv]); }; \
        auto ldK = [&]() { if (LD) { kb[c] = kv_next(); K[c][0] = ld16<16>(ws, l16, kb[c]); K[c][1] = ld16<16>(ws, l16 + KB, kb[c]); } }; \
        auto ldV0 = [&]() { if (LD) V[c][0] = ld16<16>(ws, l16 + 2 * KB, kb[c]); }; \
        unit<true, true>(sa, sb, Z, o0, o3, pp, K[c][0], K[c][1], q[1][0], q[1][1], V[c][0], V[pv][1], ldV1, nop, nop); \
        unit<true, true>(sb, sa, Z, o1, o0, pp, K[c][0], K[c][1], q[2][0], q[2][1], V[c][0], V[c][1], nop, nop, nop); \
        if (LD && c == 0 && __builtin_expect(pend != 0u, 0)) {        \
          pend = 0u; kv_mask(); \
          _Pragma("unroll") for (int t = 2; t < 4; ++t) { kb[t] = kv_next(); K[t][0] = ld16<16>(ws, l16, kb[t]); K[t][1] = ld16<16>(ws, l16 + KB, kb[t]); \
            V[t][0] = ld16<16>(ws, l16 + 2 * KB, kb[t]); V[t][1] = ld16<16>(ws, l16 + 3 * KB, kb[t]); } } \
        unit<true, true>(sa, sb, Z, o2, o1, pp, K[c][0], K[c][1], q[3][0], q[3][1], V[c][0], V[c][1], nop, ldK, nop); \
        if (LD && c == 0 && __builtin_expect(chk != 0u, 0)) { chk = 0u; chk_handoff<2>(K[n], ws, l16, kb[n]); chk_handoff<2>(V[n], ws, l16 + 2 * KB, kb[n]); } \
        unit<true, true>(sb, sa, Z, o3, o2, pp, K[n][0], K[n][1], q[0][0], q[0][1], V[c][0], V[c][1], nop, nop, ldV0); }
#pragma unroll 1
      for (int tt = 0; tt < 12; tt += 4) {
        kv_mask(); if (!FSKIP || miss) kvf = ld_tag(kpa);
        TILE(0, true, true) TILE(1, true, true)
        kv_mask(); if (!FSKIP || miss) kvf = ld_tag(kpa);
        TILE(2, true, true) TILE(3, true, true)
      }
      TILE(0, false, true)
      lds_sig(cx, 11);
      lds_wait<1>(cx, 8, 4u);
      TILE(1, false, false)
      const ldsp red = scr + w * 4 * KB;
#define WR_LO(o, a) [&]() { lds_wf4(red + (a) * 16 * KB, f32x4{o[0], o[1], o[2], o[3]}); lds_wf4(red + (a) * 16 * KB + KB, f32x4{o[4], o[5], o[6], o[7]}); }
#define WR_HI(o, a) [&]() { lds_wf4(red + (a) * 16 * KB + 2 * KB, f32x4{o[8], o[9], o[10], o[11]}); lds_wf4(red + (a) * 16 * KB + 3 * KB, f32x4{o[12], o[13], o[14], o[15]}); lds_sig(cx, a); }
      unit<true, true>(sa, sb, Z, o0, o3, pp, K[3][0], K[3][1], q[0][0], q[0][1], V[2][0], V[1][1], nop, nop, nop);
      unit<true, true>(sb, sa, Z, o0, o0, pp, K[2][0], K[2][1], q[1][0], q[1][1], V[3][0], V[2][1], nop, nop, nop);
      unit<true, true>(sa, sb, Z, o1, o0, pp, K[3][0], K[3][1], q[1][0], q[1][1], V[2][0], V[3][1], nop, WR_LO(o0, 0), WR_HI(o0, 0));
      unit<true, true>(sb, sa, Z, o1, o1, pp, K[2][0], K[2][1], q[2][0], q[2][1], V[3][0], V[2][1], nop, nop, nop);
      unit<true, true>(sa, sb, Z, o2, o1, pp, K[3][0], K[3][1], q[2][0], q[2][1], V[2][0], V[3][1], nop, WR_LO(o1, 1), WR_HI(o1, 1));
      unit<true, true>(sb, sa, Z, o2, o2, pp, K[2][0], K[2][1], q[3][0], q[3][1], V[3][0], V[2][1], nop, nop, nop);
      unit<true, true>(sa, sb, Z, o3, o2, pp, K[3][0], K[3][1], q[3][0], q[3][1], V[2][0], V[3][1], nop, WR_LO(o2, 2), WR_HI(o2, 2));
      unit<false, true>(sb, sa, Z, o3, o3, pp, K[2][0], K[2][1], q[3][0], q[3][1], V[3][0], V[2][1], nop, nop, nop);
      o3 = MFMA(V[3][1], pp, o3);
      WR_LO(o3, 3)(); WR_HI(o3, 3)();
    }
    {
      u32x4 of[8] = {};
      const int src = RT(rt) + F_OFA + 8 * w * KB;
      if (w != sub) {
        poll_wait(cx, (((jj & ~3) | w) << 3) | xcd, 0);
        ld_handoff<8>(of, ws, l16, src);
      }
      BAR();
      const unsigned bad = __builtin_amdgcn_readfirstlane(*(LAS unsigned*)(cx.sig + 10 * 16));
      if (__builtin_expect((bad >> w) & 1u, 0)) {
        const u32x4 q0 = lds_r16(lds + L_QF + l16 + 2 * w * KB);
        u32x4 q1 = lds_r16(lds + L_QF + l16 + (2 * w + 1) * KB);
        if (h == 1) { q1[2] = 0; q1[3] = 0; }
        float m = -INFINITY;
#pragma unroll 1
        for (int t = 0; t < 64; ++t) {
          f32x16 s = MFMA(ld16<16>(ws, l16, R_KVA + t * 4 * KB), q0, Z);
          s = MFMA(ld16<16>(ws, l16 + KB, R_KVA + t * 4 * KB), q1, s);
          m = max16(s, m);
        }
        set_shift(q1, xhalf_max(m), h, 0u);
        f32x16 o = {};
#pragma unroll 1
        for (int t = 0; t < 64; ++t) {
          f32x16 s = MFMA(ld16<16>(ws, l16, R_KVA + t * 4 * KB), q0, Z);
          s = MFMA(ld16<16>(ws, l16 + KB, R_KVA + t * 4 * KB), q1, s);
          u32x4 p0, p1;
          exp_pack(s, p0, p1);
          o = MFMA(ld16<16>(ws, l16 + 2 * KB, R_KVA + t * 4 * KB), p0, o);
          o = MFMA(ld16<16>(ws, l16 + 3 * KB, R_KVA + t * 4 * KB), p1, o);
        }
        u32x4 y0, y1;
        norm_pack(o, h, y0, y1);
        st16<16>(y0, ws, l16, RT(rt) + F_OFA + (sub * 8 + 2 * w) * KB);
        st16<16>(y1, ws, l16 + KB, RT(rt) + F_OFA + (sub * 8 + 2 * w) * KB);
        lds_w16(scr + (sub * 8 + 2 * w) * KB, y0); lds_w16(scr + (sub * 8 + 2 * w + 1) * KB, y1);
      }
      if (w != sub) {
        chk_handoff<8>(of, ws, l16, src);
#pragma unroll
        for (int i = 0; i < 8; ++i) lds_w16(scr + (8 * w + i) * KB, of[i]);
      }
      BAR();
    }
    f32x4 ck[4], cq[4];
    const float cv = *(LAS float*)(lds + M_CS + 6 * 128 + r * 4);
#pragma unroll
    for (int g = 0; g < 4; ++g) { cq[g] = lds_rf4(lds + M_CS + 4 * 128 + (8 * g + 4 * h) * 4); ck[g] = lds_rf4(lds + M_CS + 5 * 128 + (8 * g + 4 * h) * 4); }
    BAR();
    u32x4 q0, q1, k0, k1, v0, v1;
    const Clk ck0 = clk_now();
    {
      const int xo = RT(rb * 4 + w) + F_XF1;
      const ldsp wb0 = opaque(lds + L_WB + l16), wb1 = opaque(lds + L_WB + 28 * KB + l16);
      const ldsp st = lds + M_ST + w * 256;
      f32x4 m4[4], r4[4];
      poll_wait(cx, (((jj & ~15) | (w << 2) | (lane & 3)) << 3) | xcd, 1);
      f32x16 aq, ak, av;
      float mu, rs;
      u32x4 xf[28], wk[3], wv[3], wq[4];
#define LDX(i) do { xf[i] = ld16<16>(ws, l16 + ((i) & 3) * KB, xo + ((i) >> 2) * 4 * KB); SBAR(); } while (0)
#define CHKX(i) do { if (__builtin_expect(__builtin_amdgcn_ballot_w64(smax(0u, xf[i]) == SENT) != 0ull, 0)) { \
          _Pragma("unroll 1") for (int spin = 0; spin < SPIN_CAP; ++spin) { CBAR(); LDX(i); if (__builtin_amdgcn_ballot_w64(smax(0u, xf[i]) == SENT) == 0ull) break; } } SBAR(); } while (0)
#define QST(i) do { if ((i) < 28) { SBAR(); aq = MFMA(wq[(i) & 3], xf[(i) < 28 ? (i) : 0], (i) ? aq : Z); SBAR(); if ((i) + 3 < 28) wq[((i) + 3) & 3] = lds_r16(wb0 + ((i) + 3) * KB); SBAR(); } } while (0)
#pragma unroll
      for (int i = 0; i < X1_AHEAD; ++i) LDX(i);
#pragma unroll
      for (int i = 0; i < 2; ++i) { wk[i] = lds_r16(wb1 + i * KB); wv[i] = lds_r16(wb1 + (28 + i) * KB); }
#pragma unroll
      for (int i = 0; i < 3; ++i) wq[i] = lds_r16(wb0 + i * KB);
      SBAR();
      CHKX(0);
#pragma unroll
      for (int ks = 0; ks < 28; ++ks) {
        ak = MFMA(wk[ks % 3], xf[ks], ks ? ak : Z); SBAR();
        if (ks + 2 < 28) { wk[(ks + 2) % 3] = lds_r16(wb1 + (ks + 2) * KB); wv[(ks + 2) % 3] = lds_r16(wb1 + (28 + ks + 2) * KB); }
        if (X1_AHEAD + 2 * ks < 28) LDX(X1_AHEAD + 2 * ks);
        if (ks == 25) {
          lds_wait<0>(cx, 12 + w, 1u);
          mu = *(LAS float*)(st + r * 4); rs = *(LAS float*)(st + 128 + r * 4);
        }
        if (ks == 26) {
#pragma unroll
          for (int g = 0; g < 4; ++g) { m4[g] = lds_rf4(st + (8 * g + 4 * h) * 4); r4[g] = lds_rf4(st + 128 + (8 * g + 4 * h) * 4); }
        }
        SBAR();
        av = MFMA(xf[ks], wv[ks % 3], ks ? av : Z); SBAR();
        if (X1_AHEAD + 2 * ks + 1 < 28) LDX(X1_AHEAD + 2 * ks + 1);
        if (ks < QS) QST(ks);
        if (ks + 1 < 28) CHKX(ks + 1);
        if (ks == POST_KV) post(cx, b, 4 + w, ck0, DT_KV);
      }
      const int ko = RT(rb * 4 + w) + F_KVB + hd * 4 * KB;
      QST(QS); QST(QS + 1);
#pragma unroll
      for (int g = 0; g < 4; ++g) {
#pragma unroll
        for (int i = 4 * g; i < 4 * g + 4; ++i) ak[i] = (ak[i] - mu * ck[g][i & 3]) * rs;
        unsigned a0 = pk(ak[4 * g], ak[4 * g + 1]), a1 = pk(ak[4 * g + 2], ak[4 * g + 3]);
        if (g == 3 && h == 1) { a0 = ONES2; a1 = 0x00003F80u; }
        PIN(a0); PIN(a1);
        if (g < 2) { k0[2 * g] = a0; k0[2 * g + 1] = a1; } else { k1[2 * g - 4] = a0; k1[2 * g - 3] = a1; }
        if (g == 1) st16<16>(k0, ws, l16, ko);
        if (g == 3) st16<16>(k1, ws, l16 + KB, ko);
        QST(QS + 2 + g);
      }
      const unsigned vpad = r == 28 ? ONES2 : 0u;
#pragma unroll
      for (int g = 0; g < 4; ++g) {
#pragma unroll
        for (int i = 4 * g; i < 4 * g + 4; ++i) av[i] = (av[i] - m4[g][i & 3] * cv) * r4[g][i & 3];
        unsigned a0 = pk(av[4 * g], av[4 * g + 1]), a1 = pk(av[4 * g + 2], av[4 * g + 3]);
        if (r >= 28) { a0 = vpad; a1 = vpad; }
        PIN(a0); PIN(a1);
        if (g < 2) { v0[2 * g] = a0; v0[2 * g + 1] = a1; } else { v1[2 * g - 4] = a0; v1[2 * g - 3] = a1; }
        if (g == 1) st16<16>(v0, ws, l16 + 2 * KB, ko);
        if (g == 3) st16<16>(v1, ws, l16 + 3 * KB, ko);
        QST(QS + 6 + g);
      }
      lds_w16(scr + (32 + 4 * w) * KB, k0); QST(QS + 10); lds_w16(scr + (33 + 4 * w) * KB, k1); QST(QS + 11);
      lds_w16(scr + (34 + 4 * w) * KB, v0); QST(QS + 12); lds_w16(scr + (35 + 4 * w) * KB, v1);
      lds_sig(cx, 4);
#pragma unroll
      for (int i = QS + 13; i < 28; ++i) QST(i);
      const float rq = rs * C2;
#pragma unroll
      for (int i = 0; i < 16; ++i) aq[i] = (aq[i] - mu * cq[i >> 2][i & 3]) * rq;
      q0 = pack8(aq, 0); q1 = pack8(aq, 1);
      if (h == 1) { q1[2] = 0; q1[3] = NEG31; }
    }
    {
      f32x16 cd, cf;
#pragma unroll
      for (int i = 0; i < 16; ++i) {
        const int kk = (i & 3) + 8 * (i >> 2) + 4 * h;
        cd[i] = kk <= r ? 0.f : -1e30f; cf[i] = kk <= r ? -1e30f : 0.f;
      }
      f32x16 sa = MFMA(k0, q0, cd), sb;
      sa = MFMA(k1, q1, sa);
      set_shift(q1, xhalf_max(max16(sa, sa[0])), h, NEG31);
      const ldsp kp = scr + (8 + w) * 4 * KB;
      auto need = [&](int u) {
        const int ti = 8 + w - u;
        if (u == 1 && w > 0) lds_wait<0>(cx, 4, 4u);
        if (__builtin_expect((ti & 3) == 3, 0)) lds_wait<1>(cx, 5 + (ti >> 2), 2u);
      };
      u32x4 K[2][2], V[2][2], pp;
      f32x16 o = {};
      sa = MFMA(k0, q0, cd); sa = MFMA(k1, q1, sa);
      V[0][0] = v0; V[0][1] = v1;
      need(1);
      K[1][0] = lds_r16(kp - 4 * KB); K[1][1] = lds_r16(kp - 3 * KB); V[1][0] = lds_r16(kp - 2 * KB);
#pragma unroll
      for (int u = 0; u < 9; ++u) {
        const int c = u & 1, n = c ^ 1;
        auto ha = [&]() {
          if (u + 1 < 9) V[n][1] = lds_r16(kp - (u + 1) * 4 * KB + 3 * KB);
          if (u + 2 < 9) { need(u + 2); K[c][0] = lds_r16(kp - (u + 2) * 4 * KB); K[c][1] = lds_r16(kp - (u + 2) * 4 * KB + KB); }
        };
        auto hc = [&]() { if (u + 2 < 9) V[c][0] = lds_r16(kp - (u + 2) * 4 * KB + 2 * KB); if (u >= 4 && u <= 6 && __builtin_expect(u == (w < 2 ? 4 : 3 + w), 0)) post(cx, b, 8 + w, ck0, DT_OB + (8 - u) * DT_UNIT); };
        f32x16& s = c ? sb : sa; f32x16& sn = c ? sa : sb;
        if (u == 0) unit<true, false>(s, sn, Z, o, o, pp, K[n][0], K[n][1], q0, q1, V[c][0], V[n][1], ha, nop, hc);
        else if (u < 7) unit<true, true>(s, sn, Z, o, o, pp, K[n][0], K[n][1], q0, q1, V[c][0], V[n][1], ha, nop, hc);
        else if (u == 7) unit<true, true>(s, sn, cf, o, o, pp, K[n][0], K[n][1], q0, q1, V[c][0], V[n][1], ha, nop, hc);
        else unit<false, true>(s, sn, Z, o, o, pp, K[n][0], K[n][1], q0, q1, V[c][0], V[n][1], ha, nop, hc);
      }
      o = MFMA(V[0][1], pp, o);
      if (__builtin_expect(__builtin_amdgcn_ballot_w64(h == 1 && !(o[12] < LBIG)) != 0ull, 0)) {
        if (h == 1) { q1[2] = 0; q1[3] = NEG31; }
        float m = -INFINITY;
#pragma unroll 1
        for (int u = 0; u < 9; ++u) {
          f32x16 s;
#pragma unroll
          for (int i = 0; i < 16; ++i) s[i] = u == 0 ? cd[i] : u == 8 ? cf[i] : 0.f;
          s = MFMA(lds_r16(kp - u * 4 * KB), q0, s); s = MFMA(lds_r16(kp - u * 4 * KB + KB), q1, s);
          m = max16(s, m);
        }
        set_shift(q1, xhalf_max(m), h, NEG31);
        o = Z;
#pragma unroll 1
        for (int u = 0; u < 9; ++u) {
          f32x16 s;
#pragma unroll
          for (int i = 0; i < 16; ++i) s[i] = u == 0 ? cd[i] : u == 8 ? cf[i] : 0.f;
          s = MFMA(lds_r16(kp - u * 4 * KB), q0, s); s = MFMA(lds_r16(kp - u * 4 * KB + KB), q1, s);
          u32x4 p0, p1;
          exp_pack(s, p0, p1);
          o = MFMA(lds_r16(kp - u * 4 * KB + 2 * KB), p0, o); o = MFMA(lds_r16(kp - u * 4 * KB + 3 * KB), p1, o);
        }
      }
      u32x4 y0, y1;
      norm_pack(o, h, y0, y1);
      const int oo = RT(rb * 4 + w) + F_OFB + hd * 2 * KB;
      st16<16>(y0, ws, l16, oo); st16<16>(y1, ws, l16 + KB, oo);
      lds_sig(cx, 9);
    }
    u32x4 wa1[WA1];
    {
      u32x4 of[8];
      const int src = RT(rt) + F_OFB + 8 * w * KB;
#pragma unroll
      for (int i = 0; i < WA1; ++i) { wa1[i] = ld16<16>(wt, l16 + (i & 3) * KB, pair ? OFF_PAIR : OFF_WAT + ((4 * sub + w) * 28 + (i & ~3)) * KB); SBAR(); }
      poll_wait(cx, (((jj & ~15) | (w << 2) | (lane & 3)) << 3) | xcd, 8 + (rt & 3));
      ld_handoff<8>(of, ws, l16, src);
      chk_handoff<8>(of, ws, l16, src);
      lds_wait<1>(cx, 9, 4u);
#pragma unroll
      for (int i = 0; i < 8; ++i) lds_w16(scr + (8 * w + i) * KB, of[i]);
      BAR();
    }
    if (pair == 0) {
      u32x4 wa[28];
#pragma unroll
      for (int i = 0; i < WA1; ++i) wa[i] = wa1[i];
#pragma unroll
      for (int i = WA1; i < 28; ++i) { wa[i] = ld16<16>(wt, l16 + (i & 3) * KB, OFF_WAT + ((4 * sub + w) * 28 + (i & ~3)) * KB); SBAR(); }
      f32x4 cq[4];
#pragma unroll
      for (int g = 0; g < 4; ++g) cq[g] = lds_rf4(lds + M_CS + w * 128 + (8 * g + 4 * h) * 4);
      if (w >= 2) poll_wait<2>(cx, (((jj & ~3) | (lane & 3)) << 3) | xcd, 2);
      if (w == 3) stats_gather(ws, RT(rt) + F_ST2, lds + M_ST + 4 * 256, lane);
      if (w == 2 && sub < 2) {
        const unsigned t = eta(ck0, DT_Q);
        if (lane == 0) __hip_atomic_store((GAS unsigned*)(cx.flg + PAIR_SZ + b * 128 + 12 * 4), t, __ATOMIC_RELAXED, __HIP_MEMORY_SCOPE_AGENT);
      }
      BAR();
      if (sub < 2 && w < 2) __builtin_amdgcn_s_sleep(4);
      const float mu = *(LAS float*)(lds + M_ST + 4 * 256 + r * 4), rq = *(LAS float*)(lds + M_ST + 4 * 256 + 128 + r * 4) * C2;
      f32x16 acc = gemm_lds<28, false>(wa, scr + 32 * KB, [&](int i) { if (i == KVE_AT) kve = flag_ld(cx, PAIR_SZ / 128 + (((((4 * (lane & 1) + w) << 2) | ((lane >> 1) & 1)) << 3) | xcd), 12); });
#pragma unroll
      for (int i = 0; i < 16; ++i) acc[i] = (acc[i] - mu * cq[i >> 2][i & 3]) * rq;
      qn0 = pack8(acc, 0); qn1 = pack8(acc, 1);
    }
   }
   return;
  }

  __builtin_amdgcn_s_setprio(2);
  phase0_h(p, lds, epo, tag);
  u32x4 x0 = {}, x1 = {};
#pragma unroll 1
 for (int pair = 0; pair < 2; ++pair) {
  PAIR_SETUP();
  __builtin_amdgcn_s_setprio(HPRIO);
  const int ct0 = sub < 2 ? 4 * sub : 3 * sub + 2, nct = sub < 2 ? 4 : 3;
  const bool ctok = w < nct;
  const int ct = ct0 + (ctok ? w : 0);
  const int xo1 = RT(rt) + F_XF1 + ct * 2 * KB, xo2 = RT(rt) + F_XF2 + ct * 2 * KB;
  const int so1 = RT(rt) + F_ST1 + ct * 256, so2 = RT(rt) + F_ST2 + ct * 256;
  lds_wait<4>(cx, 7, 4u);
  __builtin_amdgcn_s_setprio(HPRIO);
  u32x4 wf[32];
  Clk ck0 = clk_now();
  {
    const u32x4 sv = {SENT, SENT, SENT, SENT};
    auto fill = [&](int i) {
      const int hi = (i & 1) * KB;
      if (i < 2) st16<16>(sv, ws, l16 + hi, RT(rt) + F_OFA + (sub * 8 + 2 * w) * KB);
      else if (i < 4) { if (ctok) { st16<16>(sv, ws, l16 + hi, xo1); if (i == 2) st4<16>(SENT, ws, lane * 4, so1); } }
      else if (i < 8) st16<16>(sv, ws, l16 + (i - 4) * KB, RT(rb * 4 + w) + F_KVB + hd * 4 * KB);
      else if (i < 10) st16<16>(sv, ws, l16 + hi, RT(rb * 4 + w) + F_OFB + hd * 2 * KB);
      else if (i < 12) { if (ctok && pair == 0) { st16<16>(sv, ws, l16 + hi, xo2); if (i == 10) st4<16>(SENT, ws, lane * 4, so2); } }
      else if (i == 12 && pair == 0 && sub < 2 && w < 2) st16<16>(sv, ws, l16, PAIR_SZ + R_KVA + (rt * 4 + 2 * sub + w) * KB);
      SBAR();
    };
#pragma unroll
    for (int i = 0; i < NFILL0; ++i) { fill(i); __builtin_amdgcn_s_sleep(PACEF); }
    if (pair == 0) {
#pragma unroll 1
      for (int spin = 0; spin < SPIN_CAP; ++spin) {
        bool ok = true;
#pragma unroll
        for (int i = 0; i < 4; ++i) ok &= ld_tag(p.ws + OFF_INI + (xcd * 256 + i * 64 + lane) * 8) == tag;
        if (__builtin_amdgcn_ballot_w64(!ok) == 0ull) break;
        __builtin_amdgcn_s_sleep(8);
      }
      CBAR();
    }
    unsigned c4[16] = {};
    if (pair == 0) {
      const unsigned short* xr = p.x + (size_t)(rt * 32 + r) * D_ + ct * 32 + 4 * h;
      const u32x2 a0 = *(const u32x2*)xr, a1 = *(const u32x2*)(xr + 8), a2 = *(const u32x2*)(xr + 16), a3 = *(const u32x2*)(xr + 24);
      x0 = u32x4{a0[0], a0[1], a1[0], a1[1]}; x1 = u32x4{a2[0], a2[1], a3[0], a3[1]};
      const int co = (h ? (w < 3 ? 16 + hd * 3 + w : 64 + (sub & 1)) : 4 * sub + w) * 2048 + r * 4;
#pragma unroll
      for (int i = 0; i < 16; ++i) c4[i] = ld4<16>(wt, co + i * 128, OFF_CS);
      SBAR();
    } else if (TOUCH && lane < 2) {
      glds16((const unsigned char*)p.out + (size_t)(rt * 32 + lane * 31) * D_ * 2, (unsigned)(uintptr_t)(lds + M_ST));
    }
    const unsigned char* wbsrc = p.ws + OFF_WBT + (hd * 84 + w * 21) * KB + l16;
    const unsigned wbdst = (unsigned)(uintptr_t)(lds + L_WB) + w * 21 * KB;
#pragma unroll
    for (int i = 0; i < 32; ++i) {
      wf[i] = ld16<16>(wt, l16 + (i & 3) * KB, OFF_WOA + (ct * 32 + (i & ~3)) * KB); SBAR();
      if (i % 3 != 2 || i > 29) { const int g = i - (i + 1) / 3; if (g < 21 && pair == 0) glds16_sc1(wbsrc + g * KB, wbdst + g * KB); }
      if ((i & 1) == 0 && NFILL0 + (i >> 1) < 13) fill(NFILL0 + (i >> 1));
      if (i == 10) { asm volatile("s_waitcnt vmcnt(5)" ::: "memory"); lds_sig(cx, 8); }
      if (i % PGRP == PGRP - 1) { SBAR(); if (pair) __builtin_amdgcn_s_sleep(PACE3); else __builtin_amdgcn_s_sleep(PACE); SBAR(); }
    }
    if (pair == 0) {
      float t = 0.f;
#pragma unroll
      for (int i = 0; i < 16; ++i) t += __uint_as_float(c4[i]);
      *(LAS float*)(lds + M_CS + (4 * h + w) * 128 + r * 4) = t;
    }
    asm volatile("s_waitcnt vmcnt(0)" ::: "memory");
  }
  {
    u32x4 keep[2];
    const int s = w & 1;
    __builtin_amdgcn_s_setprio(2);
    lds_wait<2>(cx, 8, 4u);
#pragma unroll 1
    for (int spin = 0; spin < SPIN_CAP; ++spin) {
      if (__builtin_amdgcn_readfirstlane(*(volatile LAS unsigned*)(cx.sig + 11 * 16 + w * 4))) break;
      __builtin_amdgcn_s_sleep(DOZE);
    }
#pragma unroll
    for (int it = 0; it < 2; ++it) {
      const int T = 2 * it + (w >> 1);
      if (it == 0) lds_wait<2>(cx, T, 4u); else lds_wait<1>(cx, T, 4u);
      if (it == 0) { if (w == POST_OA) post(cx, b, 0, ck0, DT_OA); ck0 = clk_now(); }

      const ldsp red = scr + T * 16 * KB;
      f32x4 a0[4], a1[4]; float l4[4];
#pragma unroll
      for (int k = 0; k < 4; ++k) {
        l4[k] = *(LAS float*)(lds + L_SCR + T * 16 * KB + k * 4 * KB + 3 * KB + (32 + r) * 16);
        a0[k] = lds_rf4(red + k * 4 * KB + (2 * s) * KB);
        a1[k] = lds_rf4(red + k * 4 * KB + (2 * s + 1) * KB);
      }
      const float l = (l4[0] + l4[1]) + (l4[2] + l4[3]);
      const f32x4 b0 = (a0[0] + a0[1]) + (a0[2] + a0[3]), b1 = (a1[0] + a1[1]) + (a1[2] + a1[3]);
      const float inv = __builtin_amdgcn_rcpf(l);
      u32x4 o = {pk(b0[0] * inv, b0[1] * inv), pk(b0[2] * inv, b0[3] * inv), pk(b1[0] * inv, b1[1] * inv), pk(b1[2] * inv, b1[3] * inv)};
      if (s == 1 && h == 1) { o[2] = 0; o[3] = 0; }
      if (__builtin_expect(__builtin_amdgcn_ballot_w64(!(l < LBIG)) == 0ull, 1)) st16<16>(o, ws, l16, RT(rt) + F_OFA + (sub * 8 + 2 * T + s) * KB);
      else if (lane == 0) __hip_atomic_fetch_or((LAS unsigned*)(cx.sig + 10 * 16), 1u << T, __ATOMIC_RELAXED, __HIP_MEMORY_SCOPE_WORKGROUP);
      keep[it] = o;
    }
    BAR();
    const unsigned bad = __builtin_amdgcn_readfirstlane(*(LAS unsigned*)(cx.sig + 10 * 16));
#pragma unroll
    for (int it = 0; it < 2; ++it) {
      const int T = 2 * it + (w >> 1);
      if (!((bad >> T) & 1u)) lds_w16(scr + (sub * 8 + 2 * T + s) * KB, keep[it]);
    }
    BAR();
  }
  {
    const f32x16 acc = gemm_lds<32, false>(wf, scr, [&](int i) { if (i == 0 && w == 0) post(cx, b, 1, ck0, DT_X); if (i == 24) BAR();   });
    add_res(acc, x0, x1, x0, x1);
    if (ctok) { st16<16>(x0, ws, l16, xo1); st16<16>(x1, ws, l16 + KB, xo1); SBAR(); st4<16>(__float_as_uint(row_stats(x0, x1, h)), ws, lane * 4, so1); }
  }
  u32x4 wkv[7];
  {
    __builtin_amdgcn_s_setprio(0);
    poll_wait<2>(cx, (((jj & ~15) | (w << 2) | (lane & 3)) << 3) | xcd, 1);
    __builtin_amdgcn_s_sleep(ST_DLY);
    stats_gather(ws, RT(rb * 4 + w) + F_ST1, lds + M_ST + w * 256, lane);
    lds_sig(cx, 12 + w);
    const int kt = 4 * rb - 8 + 2 * w;
    u32x4 hv[8];
    if (kt >= 0) {
      const int prb = kt >> 2;
      __builtin_amdgcn_s_setprio(0);
      poll_wait<HALO_SLP>(cx, (((prb & 1) * 16 + hd) << 3) | (prb >> 1), 4 + (kt & 3) + (lane & 1));
      __builtin_amdgcn_s_setprio(2);
      ld_handoff<8, 2 * RT_SZ>(hv, ws, l16, RT(kt) + F_KVB + hd * 4 * KB);
      chk_handoff<8, 2 * RT_SZ>(hv, ws, l16, RT(kt) + F_KVB + hd * 4 * KB);
    } else {
#pragma unroll
      for (int i = 0; i < 8; ++i) hv[i] = u32x4{0u, 0u, 0u, ((i & 3) == 1 && h == 1) ? 0x3F800000u : 0u};
    }
#pragma unroll
    for (int i = 0; i < 8; ++i) lds_w16(scr + (8 * w + i) * KB, hv[i]);
    lds_sig(cx, 5 + (w >> 1));
    __builtin_amdgcn_s_setprio(0);
#pragma unroll
    for (int i = 0; i < 32; ++i) { wf[i] = ld16<16>(wt, l16 + (i & 3) * KB, OFF_WOB + (ct * 32 + (i & ~3)) * KB); SBAR(); }
#pragma unroll
    for (int i = 0; i < 7; ++i) wkv[i] = ld16<16>(wt, l16 + (i & 3) * KB, OFF_WKV + ((sub & 1) * 28 + 7 * w + (i & 4)) * KB);
    asm volatile("s_waitcnt vmcnt(0)" ::: "memory");
    BAR();
  }
  __builtin_amdgcn_s_setprio(2);
  {
    const f32x16 acc = gemm_lds<32, false>(wf, scr, [&](int i) { if (i == 0 && w == 0 && pair == 0) post(cx, b, 2, ck0, DT_X); if (i == 31 && pair == 0) lds_sig(cx, 32 + 5); });
    add_res(acc, x0, x1, x0, x1);
    if (pair == 1) {
      if (ctok) {
        unsigned short* orow = p.out + (size_t)(rt * 32 + r) * D_ + ct * 32 + 4 * h;
        *(u32x2*)orow = u32x2{x0[0], x0[1]}; *(u32x2*)(orow + 8) = u32x2{x0[2], x0[3]};
        *(u32x2*)(orow + 16) = u32x2{x1[0], x1[1]}; *(u32x2*)(orow + 24) = u32x2{x1[2], x1[3]};
      }
      if (b == 0 && w == 0 && lane == 0) st_tag(p.ws + OFF_EPO, tag);
      return;
    }
    if (ctok) { st16<16>(x0, ws, l16, xo2); st16<16>(x1, ws, l16 + KB, xo2); SBAR(); st4<16>(__float_as_uint(row_stats(x0, x1, h)), ws, lane * 4, so2); }
  }
  {
    u32x4 xf[7];
    const int src = RT(rt) + F_XF2 + 7 * w * KB;
    lds_wait<0>(cx, 32 + 5, 4u);
    poll_wait(cx, (((jj & ~3) | (lane & 3)) << 3) | xcd, 2);
    ld_handoff<7>(xf, ws, l16, src);
    chk_handoff<7>(xf, ws, l16, src);
    if (sub < 2) {
      f32x16 acc = {};
      if (sub) {
#pragma unroll
        for (int i = 0; i < 7; ++i) { acc = MFMA(xf[i], wkv[i], acc); SBAR(); lds_w16(scr + (32 + 7 * w + i) * KB, xf[i]); SBAR(); }
      } else {
#pragma unroll
        for (int i = 0; i < 7; ++i) { acc = MFMA(wkv[i], xf[i], acc); SBAR(); lds_w16(scr + (32 + 7 * w + i) * KB, xf[i]); SBAR(); }
      }
      part_write(scr + w * 4 * KB, acc);
    } else {
#pragma unroll
      for (int i = 0; i < 7; ++i) lds_w16(scr + (32 + 7 * w + i) * KB, xf[i]);
    }
    BAR();
    if (sub < 2 && w < 2) kv_finish<true>(lds + L_SCR, 4 * KB, sub, w, lds + M_ST + 4 * 256, lds + M_CS + 7 * 128, lane, ws, PAIR_SZ + R_KVA + rt * 4 * KB, reg + PAIR_SZ + R_FKV + rt * 128, tag);
  }
 }
}

extern "C" void kernel_launch(void* const* d_in, const int* in_sizes, int n_in, void* d_out, int out_size, void* d_ws, size_t ws_size,
                              hipStream_t stream) {
  Params p;
  p.x = (const unsigned short*)d_in[0];
  p.wq = (const unsigned short*)d_in[1];
  p.wk = (const unsigned short*)d_in[2];
  p.wv = (const unsigned short*)d_in[3];
  p.woa = (const unsigned short*)d_in[4];
  p.wqkv = (const unsigned short*)d_in[6];
  p.wob = (const unsigned short*)d_in[7];
  p.out = (unsigned short*)d_out;
  p.ws = (unsigned char*)d_ws;
  k_main<<<256, 512, 0, stream>>>(p);
}
```

```cpp
#include <hip/hip_runtime.h>
#include <hip/hip_bf16.h>
#include <stdint.h>

typedef short bf16x8 __attribute__((ext_vector_type(8)));
typedef short s16x4 __attribute__((ext_vector_type(4)));
typedef float f32x16 __attribute__((ext_vector_type(16)));
typedef float f32x4 __attribute__((ext_vector_type(4)));
typedef float f32x2 __attribute__((ext_vector_type(2)));
typedef unsigned u32x4 __attribute__((ext_vector_type(4)));
typedef unsigned u32x2 __attribute__((ext_vector_type(2)));
typedef __bf16 bf16x2_t __attribute__((ext_vector_type(2)));
#define DI __device__ __forceinline__
#define LAS __attribute__((address_space(3)))
#define GAS __attribute__((address_space(1)))
typedef LAS char* ldsp;

#define MFMA(a, b, c) __builtin_amdgcn_mfma_f32_32x32x16_bf16(__builtin_bit_cast(bf16x8, (a)), __builtin_bit_cast(bf16x8, (b)), (c), 0, 0, 0)
#define BAR() asm volatile("s_waitcnt lgkmcnt(0)\n\ts_barrier" ::: "memory")
#define CBAR() asm volatile("" ::: "memory")
#define SBAR() __builtin_amdgcn_sched_barrier(0)

constexpr int D_ = 448;
constexpr float C2 = 0.18898223650461362f * 1.4426950408889634f;
constexpr float EPS = 1e-5f;
constexpr unsigned SENT = 0xFFFFFFFFu;
constexpr unsigned ONES2 = 0x3F803F80u;
constexpr int SPIN_CAP = 1 << 16;

constexpr int KB = 1024;
constexpr int OFF_WAT = 0;
constexpr int OFF_WBT = OFF_WAT + 448 * KB;
constexpr int OFF_WKV = OFF_WBT + 1344 * KB;
constexpr int OFF_WOA = OFF_WKV + 56 * KB;
constexpr int OFF_WOB = OFF_WOA + 448 * KB;
constexpr int OFF_CS = OFF_WOB + 448 * KB;
constexpr int OFF_EPO = OFF_CS + 132 * KB;
constexpr int OFF_INI = OFF_EPO + KB;
constexpr int OFF_PAIR = OFF_INI + 16 * KB;
constexpr int QA_SZ = 64 * 28 * KB;
constexpr int R_KVA = 0;
constexpr int R_OFA = R_KVA + 256 * KB;
constexpr int R_XF1 = R_OFA + 2048 * KB;
constexpr int R_KVB = R_XF1 + QA_SZ;
constexpr int R_OFB = R_KVB + 4096 * KB;
constexpr int R_XF2 = R_OFB + 2048 * KB;
constexpr int R_ST1 = R_XF2 + QA_SZ;
constexpr int R_ST2 = R_ST1 + 64 * 14 * 256;
constexpr int R_FLG = R_ST2 + 64 * 14 * 256;
constexpr int R_FKV = R_FLG + 32 * KB;
constexpr int PAIR_SZ = R_FKV + 64 * KB;
constexpr int WS_BYTES = OFF_PAIR + 2 * PAIR_SZ;

constexpr int PTRUNC = 1, FMID = 1, KVE_AT = 8, HPRIO = 0, DT_OA = 1900, DT_X = 1850, DT_KV = 3700, DT_OB = 850;
constexpr int DT_UNIT = 240, X1_AHEAD = 8, WA1 = 8, PRO_DLY = 2, PACE3 = 8, DT_Q = 3000, QS = 14, ST_DLY = 20;
constexpr int CODE_KB = 44, PGRP = 2, DOZE = 14, NFILL0 = 6, PACEF = 2, HALO_SLP = 8, PACE = 3, POST_OA = 2;
constexpr int POST_KV = 12;

DI unsigned pk(float a, float b) { f32x2 v = {a, b}; bf16x2_t r = __builtin_convertvector(v, bf16x2_t); return __builtin_bit_cast(unsigned, r); }
DI unsigned pkt(float a, float b) { return PTRUNC ? __builtin_amdgcn_perm(__float_as_uint(b), __float_as_uint(a), 0x07060302u) : pk(a, b); }
DI float dot2(unsigned a, unsigned b, float c) { return __builtin_amdgcn_fdot2_f32_bf16(__builtin_bit_cast(bf16x2_t, a), __builtin_bit_cast(bf16x2_t, b), c, false); }
DI float bflo(unsigned u) { return __uint_as_float(u << 16); }
DI float bfhi(unsigned u) { return __uint_as_float(u & 0xFFFF0000u); }
DI float xhalf_add(float v) { auto r = __builtin_amdgcn_permlane32_swap(__float_as_uint(v), __float_as_uint(v), false, false); return __uint_as_float(r[0]) + __uint_as_float(r[1]); }
DI float xhalf_max(float v) { auto r = __builtin_amdgcn_permlane32_swap(__float_as_uint(v), __float_as_uint(v), false, false); return __builtin_fmaxf(__uint_as_float(r[0]), __uint_as_float(r[1])); }
DI float from_hi(float v) { auto r = __builtin_amdgcn_permlane32_swap(__float_as_uint(v), __float_as_uint(v), false, false); return __uint_as_float(r[1]); }
DI float max3f(float a, float b, float c) { return __builtin_fmaxf(__builtin_fmaxf(a, b), c); }
DI float max16(const f32x16& s, float m) {
#pragma unroll
  for (int i = 0; i < 16; i += 2) m = max3f(m, s[i], s[i + 1]);
  return m;
}
typedef __amdgpu_buffer_rsrc_t rsrc_t;
DI rsrc_t mkrs(const void* p, unsigned n) { return __builtin_amdgcn_make_buffer_rsrc((void*)p, 0, n, 0x00020000); }
template <int AUX> DI u32x4 ld16(rsrc_t rs, int voff, int soff) { return __builtin_amdgcn_raw_buffer_load_b128(rs, voff, soff, AUX); }
template <int AUX> DI void st16(u32x4 v, rsrc_t rs, int voff, int soff) { __builtin_amdgcn_raw_buffer_store_b128(v, rs, voff, soff, AUX); }
template <int AUX> DI unsigned ld4(rsrc_t rs, int voff, int soff) { return __builtin_amdgcn_raw_buffer_load_b32(rs, voff, soff, AUX); }
template <int AUX> DI void st4(unsigned v, rsrc_t rs, int voff, int soff) { __builtin_amdgcn_raw_buffer_store_b32(v, rs, voff, soff, AUX); }
DI ldsp opaque(ldsp p) { unsigned a = (unsigned)(uintptr_t)p; asm volatile("" : "+v"(a)); return (ldsp)(uintptr_t)a; }
typedef unsigned long long u64;
DI u64 ld_tag(const unsigned char* a) { return __hip_atomic_load((GAS u64*)a, __ATOMIC_RELAXED, __HIP_MEMORY_SCOPE_AGENT); }
DI void st_tag(unsigned char* a, u64 v) { __hip_atomic_store((GAS u64*)a, v, __ATOMIC_RELAXED, __HIP_MEMORY_SCOPE_AGENT); }
DI u32x4 lds_r16(ldsp p) { return *(LAS u32x4*)p; }
DI f32x4 lds_rf4(ldsp p) { return *(LAS f32x4*)p; }
DI void lds_w16(ldsp p, u32x4 v) { *(LAS u32x4*)p = v; }
DI void lds_wf4(ldsp p, f32x4 v) { *(LAS f32x4*)p = v; }
DI unsigned umax3(unsigned a, unsigned b, unsigned c) { return max(max(a, b), c); }
DI unsigned smax(unsigned m, u32x4 v) { return umax3(umax3(m, v[0], v[1]), v[2], v[3]); }
DI void glds16_sc1(const void* g, unsigned lds_base) {
  unsigned sv;
  asm volatile("s_mov_b32 %0, m0\n\ts_mov_b32 m0, %2\n\ts_nop 0\n\tglobal_load_lds_dwordx4 %1, off sc1\n\ts_mov_b32 m0, %0" : "=&s"(sv) : "v"(g), "s"(lds_base) : "memory");
}
DI void glds16(const void* g, unsigned lds_base) {
  unsigned sv;
  asm volatile("s_mov_b32 %0, m0\n\ts_mov_b32 m0, %2\n\ts_nop 0\n\tglobal_load_lds_dwordx4 %1, off\n\ts_mov_b32 m0, %0" : "=&s"(sv) : "v"(g), "s"(lds_base) : "memory");
}
DI u32x4 pack8(const f32x16& a, int s) {
  u32x4 r;
#pragma unroll
  for (int i = 0; i < 4; ++i) r[i] = pk(a[8 * s + 2 * i], a[8 * s + 2 * i + 1]);
  return r;
}
template <int N> DI void ld_handoff(u32x4 (&v)[N], rsrc_t rs, int voff, int soff) {
#pragma unroll
  for (int i = 0; i < N; ++i) { v[i] = ld16<16>(rs, voff + (i & 3) * KB, soff + (i >> 2) * 4 * KB); SBAR(); }
}
template <int N> DI bool any_sent(const u32x4 (&v)[N]) {
  unsigned m = 0;
#pragma unroll
  for (int i = 0; i < N; ++i) m = smax(m, v[i]);
  return __builtin_amdgcn_ballot_w64(m == SENT) != 0ull;
}
template <int N> DI void chk_handoff(u32x4 (&v)[N], rsrc_t rs, int voff, int soff) {
  if (__builtin_expect(any_sent<N>(v), 0)) {
#pragma unroll 1
    for (int spin = 0; spin < SPIN_CAP; ++spin) {
      CBAR();
#pragma unroll
      for (int i = 0; i < N; ++i)
        if (__builtin_amdgcn_ballot_w64(smax(0u, v[i]) == SENT) != 0ull) v[i] = ld16<16>(rs, voff + (i & 3) * KB, soff + (i >> 2) * 4 * KB);
      if (!any_sent<N>(v)) break;
    }
  }
}

struct Params {
  const unsigned short *x, *wq, *wk, *wv, *woa, *wqkv, *wob;
  unsigned short* out;
  unsigned char* ws;
};

constexpr int L_SCR = 0;
constexpr int L_WB = 64 * KB;
constexpr int L_QF = 148 * KB;
constexpr int L_MISC = L_QF + 8 * KB;
constexpr int M_SIG = L_MISC;
constexpr int M_ST = M_SIG + 768;
constexpr int M_CS = M_ST + 1280;
constexpr int L_BYTES = M_CS + 1024;
constexpr int XI_KS = 1056;
constexpr int P_KV = 0, P_KVW = 112 * 56, P_XI = 25600, P_WQ = P_XI + 28 * XI_KS;
static_assert(L_BYTES <= 160 * KB && P_WQ + 448 * 224 + 64 <= L_MISC, "LDS");

DI void exp_pack(const f32x16& s, u32x4& p0, u32x4& p1) {
#pragma unroll
  for (int i = 0; i < 4; ++i) p0[i] = pk(__builtin_amdgcn_exp2f(s[2 * i]), __builtin_amdgcn_exp2f(s[2 * i + 1]));
#pragma unroll
  for (int i = 0; i < 4; ++i) p1[i] = pk(__builtin_amdgcn_exp2f(s[8 + 2 * i]), __builtin_amdgcn_exp2f(s[8 + 2 * i + 1]));
}
DI void set_shift(u32x4& q1, float m, int h, unsigned hi31) {
  const unsigned a = pk(m, m); const float r1 = m - bflo(a);
  const unsigned c = pk(r1, r1); const float r2 = r1 - bflo(c);
  const unsigned e = pk(r2, 0.f);
  const unsigned d2 = ((a & 0xFFFFu) | (c << 16)) ^ 0x80008000u, d3 = (e ^ 0x00008000u) | hi31;
  q1[2] = h ? d2 : q1[2]; q1[3] = h ? d3 : q1[3];
}
#define PIN(x) asm volatile("" : "+v"(x))
template <bool DOQK, bool DOPVP, class FA, class FB, class FC>
DI void unit(const f32x16& s, f32x16& sn, const f32x16& cn, f32x16& o, f32x16& op, u32x4& pp, const u32x4& kn0, const u32x4& kn1, const u32x4& qn0,
             const u32x4& qn1, const u32x4& v0, const u32x4& v1p, FA ha, FB hb, FC hc) {
  float e[16]; u32x4 p0;
#define E_(i) e[i] = __builtin_amdgcn_exp2f(s[i]); PIN(e[i]); SBAR()
#define C0_(i) p0[i] = pkt(e[2 * i], e[2 * i + 1]); PIN(p0[i]); SBAR()
#define C1_(i) pp[i] = pkt(e[8 + 2 * i], e[8 + 2 * i + 1]); PIN(pp[i]); SBAR()
  SBAR();
  if (DOQK) { sn = MFMA(kn0, qn0, cn); SBAR(); }
  E_(0); E_(1); E_(2); E_(3);
  if (DOPVP) { op = MFMA(v1p, pp, op); SBAR(); }
  ha(); SBAR();
  E_(4); C0_(0); E_(5); C0_(1); E_(6); E_(7);
  if (DOQK) { sn = MFMA(kn1, qn1, sn); SBAR(); }
  hb(); SBAR();
  E_(8); C0_(2); E_(9); C0_(3); E_(10); E_(11);
  o = MFMA(v0, p0, o); SBAR();
  hc(); SBAR();
  E_(12); C1_(0); E_(13); C1_(1); E_(14); E_(15); C1_(2); C1_(3);
}

struct Ctx { unsigned char* flg; ldsp sig; int lane; int w; };
DI unsigned now32() { return (unsigned)__builtin_amdgcn_s_memrealtime(); }
struct Clk { unsigned c, r; };
DI Clk clk_now() { Clk k; k.c = (unsigned)__builtin_amdgcn_s_memtime(); k.r = now32(); return k; }
DI unsigned eta(const Clk& k0, int dcyc) {
  const Clk k = clk_now();
  return (k.r + (unsigned)((float)dcyc * (float)(k.r - k0.r) * __builtin_amdgcn_rcpf((float)(k.c - k0.c)))) | 1u;
}
DI void post(const Ctx& c, int b, int slot, const Clk& k0, int dcyc) {
  const unsigned t = eta(k0, dcyc);
  if (c.lane == 0) __hip_atomic_store((GAS unsigned*)(c.flg + b * 128 + slot * 4), t, __ATOMIC_RELAXED, __HIP_MEMORY_SCOPE_AGENT);
}
DI unsigned flag_ld(const Ctx& c, int pb, int slot) { return __hip_atomic_load((GAS unsigned*)(c.flg + pb * 128 + slot * 4), __ATOMIC_RELAXED, __HIP_MEMORY_SCOPE_AGENT); }
template <int SLP = 1> DI void poll_wait(const Ctx& c, int pb, int slot, unsigned f = 0u) {
#pragma unroll 1
  for (int spin = 0; spin < SPIN_CAP; ++spin) {
    if (__builtin_amdgcn_ballot_w64(f == 0u) == 0ull) break;
    if (spin) __builtin_amdgcn_s_sleep(SLP);
    f = __hip_atomic_load((GAS unsigned*)(c.flg + pb * 128 + slot * 4), __ATOMIC_RELAXED, __HIP_MEMORY_SCOPE_AGENT);
  }
  unsigned t = __builtin_amdgcn_readlane(f, 0);
#pragma unroll
  for (int i = 1; i < 4; ++i) { const unsigned u = __builtin_amdgcn_readlane(f, i); t = (int)(u - t) > 0 ? u : t; }
#pragma unroll 1
  for (int spin = 0; spin < 256; ++spin) {
    const int d = (int)(t - now32());
    if (d <= 0) break;
    if (d > 30) __builtin_amdgcn_s_sleep(6); else __builtin_amdgcn_s_sleep(1);
  }
  CBAR();
}
DI void lds_sig(const Ctx& c, int idx) {
  CBAR();
  *(volatile LAS unsigned*)(c.sig + idx * 16 + c.w * 4) = 1u;
  CBAR();
}
template <int SLP> DI void lds_wait(const Ctx& c, int idx, unsigned n) {
#pragma unroll 1
  for (int spin = 0; spin < (SPIN_CAP << 6); ++spin) {
    const u32x4 v = *(volatile LAS u32x4*)(c.sig + idx * 16);
    if (__builtin_amdgcn_readfirstlane(v[0] + v[1] + v[2] + v[3]) >= n) break;
    if (SLP) __builtin_amdgcn_s_sleep(SLP);
  }
  CBAR();
}
template <int N, bool SWAP, class F> DI f32x16 gemm_lds(const u32x4 (&wr)[N], ldsp bb, F mid) {
  f32x16 acc = {};
  u32x4 bf[8];
#pragma unroll
  for (int i = 0; i < 8; ++i) bf[i] = lds_r16(bb + i * KB);
  SBAR();
#pragma unroll
  for (int i = 0; i < N; ++i) {
    acc = SWAP ? MFMA(bf[i & 7], wr[i], acc) : MFMA(wr[i], bf[i & 7], acc);
    SBAR();
    if (i + 8 < N) bf[i & 7] = lds_r16(bb + (i + 8) * KB);
    mid(i);
    SBAR();
  }
  return acc;
}
DI void add_res(const f32x16& a, const u32x4& x0, const u32x4& x1, u32x4& y0, u32x4& y1) {
#pragma unroll
  for (int i = 0; i < 4; ++i) {
    y0[i] = pk(a[2 * i] + bflo(x0[i]), a[2 * i + 1] + bfhi(x0[i]));
    y1[i] = pk(a[8 + 2 * i] + bflo(x1[i]), a[8 + 2 * i + 1] + bfhi(x1[i]));
  }
}
DI float row_stats(const u32x4& y0, const u32x4& y1, int h) {
  float s0 = 0.f, s1 = 0.f, q0 = 0.f, q1 = 0.f;
#pragma unroll
  for (int i = 0; i < 4; ++i) { s0 = dot2(y0[i], ONES2, s0); s1 = dot2(y1[i], ONES2, s1); q0 = dot2(y0[i], y0[i], q0); q1 = dot2(y1[i], y1[i], q1); }
  const float s = xhalf_add(s0 + s1), q = xhalf_add(q0 + q1);
  return h ? q : s;
}
DI void stats_finish(float v, float& mu, float& rs) {
  auto t = __builtin_amdgcn_permlane32_swap(__float_as_uint(v), __float_as_uint(v), false, false);
  mu = __uint_as_float(t[0]) * (1.f / D_);
  rs = __builtin_amdgcn_rsqf(__builtin_fmaxf(__uint_as_float(t[1]) * (1.f / D_) - mu * mu, 0.f) + EPS);
}
DI void stats_gather(rsrc_t ws, int so, ldsp dst, int lane) {
#pragma unroll 1
  for (int spin = 0; spin < SPIN_CAP; ++spin) {
    CBAR();
    unsigned sv[14], mx = 0;
#pragma unroll
    for (int i = 0; i < 14; ++i) sv[i] = ld4<16>(ws, lane * 4 + i * 256, so);
    float t0 = 0.f, t1 = 0.f, mu, rs;
#pragma unroll
    for (int i = 0; i < 14; i += 2) { t0 += __uint_as_float(sv[i]); t1 += __uint_as_float(sv[i + 1]); mx = umax3(mx, sv[i], sv[i + 1]); }
    stats_finish(t0 + t1, mu, rs);
    *(LAS float*)(dst + lane * 4) = lane >= 32 ? rs : mu;
    if (__builtin_amdgcn_ballot_w64(mx == SENT) == 0ull) break;
  }
}
DI void norm_pack(const f32x16& o, int h, u32x4& y0, u32x4& y1) {
  const float inv = __builtin_amdgcn_rcpf(from_hi(o[12]));
#pragma unroll
  for (int i = 0; i < 4; ++i) { y0[i] = pk(o[2 * i] * inv, o[2 * i + 1] * inv); y1[i] = pk(o[8 + 2 * i] * inv, o[8 + 2 * i + 1] * inv); }
  if (h == 1) { y1[2] = 0; y1[3] = 0; }
}
constexpr float LBIG = 1e30f;
constexpr unsigned NEG31 = 0xF1490000u;

DI u32x4 tr_frag(ldsp a, int hi) {
  const s16x4 lo_ = __builtin_amdgcn_ds_read_tr16_b64_v4i16((LAS s16x4*)a), hi_ = __builtin_amdgcn_ds_read_tr16_b64_v4i16((LAS s16x4*)(a + hi));
  return __builtin_bit_cast(u32x4, __builtin_shufflevector(lo_, hi_, 0, 1, 2, 3, 4, 5, 6, 7));
}
DI void part_write(ldsp d, const f32x16& a) {
#pragma unroll
  for (int g = 0; g < 4; ++g) lds_wf4(d + g * KB, f32x4{a[4 * g], a[4 * g + 1], a[4 * g + 2], a[4 * g + 3]});
}
template <bool LN>
DI void kv_finish(ldsp part, int pstr, int isV, int s, ldsp st, ldsp cs, int lane, rsrc_t ws, int kvo, unsigned char* fkv, u64 tag) {
  const int r = lane & 31, h = lane >> 5;
  f32x4 a[2][4], c[2] = {}, m[2] = {};
  float l0 = 0.f, l1 = 0.f;
#pragma unroll
  for (int k = 0; k < 4; ++k) { a[0][k] = lds_rf4(part + k * pstr + 2 * s * KB + lane * 16); a[1][k] = lds_rf4(part + k * pstr + (2 * s + 1) * KB + lane * 16); }
  if (LN) {
    l0 = *(LAS float*)((isV ? cs : st) + r * 4); l1 = *(LAS float*)(st + 128 + r * 4);
#pragma unroll
    for (int g = 0; g < 2; ++g) { c[g] = lds_rf4((isV ? st : cs) + (16 * s + 8 * g + 4 * h) * 4); m[g] = lds_rf4(st + 128 + (16 * s + 8 * g + 4 * h) * 4); }
  }
  SBAR();
  float v[8];
#pragma unroll
  for (int i = 0; i < 8; ++i) v[i] = (a[i >> 2][0][i & 3] + a[i >> 2][1][i & 3]) + (a[i >> 2][2][i & 3] + a[i >> 2][3][i & 3]);
  if (LN) {
#pragma unroll
    for (int i = 0; i < 8; ++i) v[i] = (v[i] - l0 * c[i >> 2][i & 3]) * (isV ? m[i >> 2][i & 3] : l1);
  }
  u32x4 o = {pk(v[0], v[1]), pk(v[2], v[3]), pk(v[4], v[5]), pk(v[6], v[7])};
  if (isV) { if (r >= 28) { const unsigned f = r == 28 ? ONES2 : 0u; o = u32x4{f, f, f, f}; } }
  else if (s == 1 && h == 1) { o[2] = ONES2; o[3] = 0x00003F80u; }
  st16<16>(o, ws, lane * 16, kvo + (2 * isV + s) * KB);
  asm volatile("s_waitcnt vmcnt(0)" ::: "memory");
  if (lane < 8) st_tag(fkv + lane * 64 * 128 + (2 * isV + s) * 8, tag);
}

DI void phase0_h(const Params& p, const ldsp lds, u64 epo, u64& tag) {
  const int tid = threadIdx.x & 255, lane = tid & 63, r = lane & 31, h = lane >> 5;
  const int w = __builtin_amdgcn_readfirstlane(tid >> 6);
  const int b = blockIdx.x, xcd = b & 7, jj = b >> 3, rt = 8 * xcd + (jj >> 2), sub = jj & 3;
  const rsrc_t ws = mkrs(p.ws, WS_BYTES);
  const Ctx cx = {p.ws, lds + M_SIG + 512, lane, w};
  const ldsp priv = lds + P_KV + w * P_KVW;
  if (sub < 2) {
    const unsigned char* src = (const unsigned char*)(sub ? p.wv : p.wk) + w * P_KVW + lane * 16;
#pragma unroll
    for (int i = 0; i < 7; ++i) if (i < 6 || lane < 8) glds16(src + i * KB, (unsigned)(uintptr_t)priv + i * KB);
  }
  const int q8 = tid & 7, row8 = tid >> 3;
  u32x4 xv[7];
  {
    const unsigned short* xr = p.x + (size_t)(rt * 32 + row8) * D_ + q8 * 8;
#pragma unroll
    for (int i = 0; i < 7; ++i) xv[i] = *(const u32x4*)(xr + i * 64);
  }
  SBAR();
  const int tt = b >> 2, tks = (b & 3) * 7 + 2 * w, wog = b * 4 + w;
  u32x2 tv[4];
  unsigned te[8];
  const bool dead = (lane & 7) == 7;
  {
    const int pitch = tt < 16 ? 448 : 1344;
    const unsigned short* src = (tt < 16 ? p.wq + 28 * tt : p.wqkv + ((tt - 16) % 3) * 448 + ((tt - 16) / 3) * 28) + (size_t)(tks * 16 + (lane >> 3)) * pitch + (dead ? 0 : (lane & 7) * 4);
#pragma unroll
    for (int i = 0; i < 4; ++i) tv[i] = *(const u32x2*)(src + (size_t)((w == 3 && i >= 2) ? 0 : i * 8) * pitch);
    int g = wog < 896 ? wog : 0;
    const unsigned short* wo = g >= 448 ? p.wob : p.woa;
    if (g >= 448) g -= 448;
    const int ct = g >> 5, ks = g & 31, head = ks >> 1;
    const int k0 = head * 28 + 16 * (ks & 1) + 4 * h, kmax = head * 28 + 28;
#pragma unroll
    for (int j = 0; j < 8; ++j) {
      const int k = k0 + 8 * (j >> 2) + (j & 3);
      te[j] = wo[(k < kmax ? k : 0) * 448 + 32 * ct + r];
    }
  }
  SBAR();
  {
    float sm[2] = {}, sq[2] = {};
#pragma unroll
    for (int i = 0; i < 7; ++i)
#pragma unroll
      for (int d = 0; d < 4; ++d) { sm[d & 1] = dot2(xv[i][d], ONES2, sm[d & 1]); sq[d & 1] = dot2(xv[i][d], xv[i][d], sq[d & 1]); }
    float a = sm[0] + sm[1], c = sq[0] + sq[1];
#define DPP_ADD(v, ctl) v += __uint_as_float(__builtin_amdgcn_update_dpp(0u, __float_as_uint(v), ctl, 0xF, 0xF, true))
    DPP_ADD(a, 0xB1); DPP_ADD(c, 0xB1); DPP_ADD(a, 0x4E); DPP_ADD(c, 0x4E); DPP_ADD(a, 0x141); DPP_ADD(c, 0x141);
    const float mu = a * (1.f / D_), rs = __builtin_amdgcn_rsqf(__builtin_fmaxf(c * (1.f / D_) - mu * mu, 0.f) + EPS), nb = -mu * rs;
#pragma unroll
    for (int i = 0; i < 7; ++i) {
      unsigned y[4];
#pragma unroll
      for (int d = 0; d < 4; ++d) y[d] = pk(__builtin_fmaf(bflo(xv[i][d]), rs, nb), __builtin_fmaf(bfhi(xv[i][d]), rs, nb));
      const ldsp dst = lds + P_XI + (4 * i + (q8 >> 1)) * XI_KS + row8 * 16 + (q8 & 1) * 8;
      *(LAS u32x2*)dst = u32x2{y[0], y[1]};
      *(LAS u32x2*)(dst + 512) = u32x2{y[2], y[3]};
    }
  }
  lds_sig(cx, 0);
  tag = (((u64)__builtin_amdgcn_readfirstlane((unsigned)(epo >> 32)) << 32) | __builtin_amdgcn_readfirstlane((unsigned)epo)) + 1ull;
  const int i16 = lane & 15, trow = 4 * h + (i16 >> 2), tcol = ((lane >> 4) & 1) * 32 + (i16 & 3) * 8;
  if (sub < 2) {
    u32x4 wf[7];
#pragma unroll
    for (int i = 0; i < 7; ++i) wf[i] = tr_frag(priv + (16 * i + trow) * 56 + tcol, 8 * 56);
    lds_wait<0>(cx, 0, 4u);
    f32x16 acc = {};
#pragma unroll
    for (int i = 0; i < 7; ++i) { const u32x4 xf = lds_r16(lds + P_XI + (7 * w + i) * XI_KS + lane * 16); acc = sub ? MFMA(xf, wf[i], acc) : MFMA(wf[i], xf, acc); }
    part_write(priv + lane * 16, acc);
    lds_sig(cx, 1);
    if (w < 2) {
      lds_wait<0>(cx, 1, 4u);
      kv_finish<false>(lds + P_KV, P_KVW, sub, w, (ldsp)0, (ldsp)0, lane, ws, OFF_PAIR + R_KVA + rt * 4 * KB, p.ws + OFF_PAIR + R_FKV + rt * 128, tag);
    }
    if (rt == 0) {
      float cs = 0.f;
#pragma unroll
      for (int i = 0; i < 7; ++i) {
        st16<16>(wf[i], ws, lane * 16, OFF_WKV + (sub * 28 + 7 * w + i) * KB);
#pragma unroll
        for (int d = 0; d < 4; ++d) cs = dot2(wf[i][d], ONES2, cs);
      }
      cs = xhalf_add(cs);
      st4<16>(h ? 0u : __float_as_uint(cs), ws, lane * 4, OFF_CS + ((64 + sub) * 16 + 4 * w) * 128);
      st4<16>(0u, ws, lane * 4 + 256, OFF_CS + ((64 + sub) * 16 + 4 * w) * 128);
    }
  }
  {
    st4<16>(0u, ws, w == 0 ? h * PAIR_SZ + r * 4 : WS_BYTES, OFF_PAIR + R_FLG + b * 128);
    const ldsp img = priv + 4 * KB;
#pragma unroll
    for (int i = 0; i < 4; ++i) *(LAS u32x2*)(img + i * 512 + lane * 8) = dead ? u32x2{0u, 0u} : tv[i];
    CBAR();
    float cs = 0.f;
#pragma unroll
    for (int ks = 0; ks < 2; ++ks) {
      const u32x4 o = tr_frag(img + ks * KB + trow * 64 + tcol, 512);
      if (ks == 0 || w < 3) {
        st16<16>(o, ws, lane * 16, (tt * 28 + tks + ks) * KB);
#pragma unroll
        for (int i = 0; i < 4; ++i) cs = dot2(o[i], ONES2, cs);
      }
    }
    u32x4 o;
#pragma unroll
    for (int i = 0; i < 4; ++i) { asm volatile("" : "+v"(te[2 * i]), "+v"(te[2 * i + 1])); o[i] = te[2 * i] | (te[2 * i + 1] << 16); }
    if ((wog & 1) && h == 1) { o[2] = 0u; o[3] = 0u; }
    if (wog < 896) st16<16>(o, ws, lane * 16, OFF_WOA + wog * KB);
    cs = xhalf_add(cs);
    if (h == 0) st4<16>(__float_as_uint(cs), ws, r * 4, OFF_CS + wog * 128);
    asm volatile("s_waitcnt vmcnt(0)" ::: "memory");
    unsigned k = 0;
    if (lane == 0) k = __hip_atomic_fetch_add((LAS unsigned*)(cx.sig + 2 * 16), 1u, __ATOMIC_RELAXED, __HIP_MEMORY_SCOPE_WORKGROUP);
    if (__builtin_amdgcn_readfirstlane(k) == 3u && lane < 8) st_tag(p.ws + OFF_INI + (lane * 256 + b) * 8, tag);
  }
}
DI void phase0_c(const Params& p, const ldsp lds, u64 epo, u64& tag, u32x4& q0, u32x4& q1) {
  const int lane = threadIdx.x & 63, h = lane >> 5;
  const int w = __builtin_amdgcn_readfirstlane(threadIdx.x >> 6), sub = (blockIdx.x >> 3) & 3;
  const Ctx cx = {p.ws, lds + M_SIG + 512, lane, w};
  __builtin_amdgcn_s_sleep(PRO_DLY);
  if (lane < 56) {
    const unsigned short* src = p.wq + (size_t)(4 * w + lane / 14) * 448 + sub * 112 + (lane % 14) * 8;
#pragma unroll 4
    for (int i = 0; i < 28; ++i) glds16(src + (size_t)i * 16 * 448, (unsigned)(uintptr_t)(lds + P_WQ) + (i * 16 + 4 * w) * 224);
  }
  asm volatile("s_waitcnt vmcnt(0)" ::: "memory");
  tag = (((u64)__builtin_amdgcn_readfirstlane((unsigned)(epo >> 32)) << 32) | __builtin_amdgcn_readfirstlane((unsigned)epo)) + 1ull;
  lds_sig(cx, 3);
  lds_wait<1>(cx, 3, 4u); lds_wait<1>(cx, 0, 4u);
  const int i16 = lane & 15;
  const ldsp wb0 = opaque(lds + P_WQ + (4 * h + (i16 >> 2)) * 224 + w * 56 + ((lane >> 4) & 1) * 32 + (i16 & 3) * 8), wb1 = opaque(wb0 + 14 * 16 * 224);
  const ldsp xb = lds + P_XI + lane * 16;
  u32x4 wr[4], xr[4];
#define PRE_LD(i) do { wr[(i) & 3] = tr_frag(((i) < 14 ? wb0 : wb1) + ((i) % 14) * 16 * 224, 8 * 224); xr[(i) & 3] = lds_r16(xb + (i) * XI_KS); } while (0)
  PRE_LD(0); PRE_LD(1); PRE_LD(2); SBAR();
  f32x16 acc = {};
#pragma unroll
  for (int i = 0; i < 28; ++i) {
    if (i + 3 < 28) PRE_LD(i + 3);
    SBAR();
    acc = MFMA(wr[i & 3], xr[i & 3], acc);
    SBAR();
  }
  lds_sig(cx, 4);
#pragma unroll
  for (int i = 0; i < 16; ++i) acc[i] *= C2;
  q0 = pack8(acc, 0); q1 = pack8(acc, 1);
  lds_wait<0>(cx, 4, 4u);
}

__global__ void __launch_bounds__(512, 2) k_main(Params p) {
  __shared__ __attribute__((aligned(16))) char smem[L_BYTES];
  const ldsp lds = (ldsp)smem;
  const int tid = threadIdx.x;
  const int w8 = __builtin_amdgcn_readfirstlane(tid >> 6);
  const rsrc_t wt = mkrs(p.ws, OFF_PAIR);
  auto nop = []() {};
  const f32x16 Z = {};
  const u64 epo = ld_tag(p.ws + OFF_EPO);
  u64 tag;
  if (tid < 192) *(LAS unsigned*)(lds + M_SIG + tid * 4) = 0u;
  if (CODE_KB && w8 == 3) {
    const unsigned char* pc = (const unsigned char*)(__builtin_amdgcn_s_getpc() & ~127ull);
    const int ln = (((blockIdx.x >> 3) * 64 + (tid & 63)) % (CODE_KB * 8)) * 128;
    glds16(pc + ln, (unsigned)(uintptr_t)(lds + M_ST));
  }
  BAR();
#define PAIR_SETUP() \
    int lane = tid & 63, b = blockIdx.x, w = w8 & 3; \
    asm volatile("" : "+v"(lane), "+s"(b), "+s"(w)); \
    lane &= 63; b &= 255; w &= 3; \
    const int r = lane & 31, h = lane >> 5, l16 = lane * 16, xcd = b & 7, jj = b >> 3; \
    const int rt = 8 * xcd + (jj >> 2), sub = jj & 3;          \
    const int rb = 2 * xcd + (jj >> 4), hd = jj & 15;          \
    const ldsp scr = lds + L_SCR + l16; \
    unsigned char* const reg = p.ws + OFF_PAIR + pair * PAIR_SZ; \
    const rsrc_t ws = mkrs(reg, 2 * PAIR_SZ - pair * PAIR_SZ); \
    const Ctx cx = {reg + R_FLG, lds + M_SIG + pair * 256, lane, w}

  if (w8 < 4) {
    u32x4 qn0, qn1;
    unsigned kve = 0u;
    phase0_c(p, lds, epo, tag, qn0, qn1);
#pragma unroll 1
   for (int pair = 0; pair < 2; ++pair) {
    PAIR_SETUP();
    const unsigned char* const kpa = reg + R_FKV + (xcd * 64 + 4 * ((2 * xcd + 1 - (lane >> 2)) & 15) + w) * 128 + (lane & 3) * 8;
    u64 miss = 0ull, kvf;
    unsigned chk = pair, pend = 1u;
    int kvi = 0;
    auto kv_mask = [&]() { miss = __builtin_amdgcn_ballot_w64(kvf != tag); };
    auto kv_next = [&]() -> int {
      if (__builtin_expect(((unsigned)(miss >> (4 * kvi)) & 15u) != 0u, 0)) {
#pragma unroll 1
        for (int spin = 0; spin < SPIN_CAP; ++spin) {
          kvf = ld_tag(kpa);
          kv_mask();
          if (((unsigned)(miss >> (4 * kvi)) & 15u) == 0u) break;
          __builtin_amdgcn_s_sleep(2);
        }
        CBAR();
      }
      const int o = R_KVA + (4 * ((2 * xcd + 1 - kvi) & 15) + w) * 4 * KB;
      ++kvi;
      return o;
    };
    {
      u32x4 q[4][2], K[4][2], V[4][2];
      int kb[4];
      if (pair) {
        poll_wait(cx, ((((4 * (lane & 1) + w) << 2) | ((lane >> 1) & 1)) << 3) | xcd, 12, kve);
      } else {
#pragma unroll 1
        for (int spin = 0; spin < SPIN_CAP; ++spin) {
          if (((unsigned)__builtin_amdgcn_ballot_w64(ld_tag(kpa) != tag) & 0xFFu) == 0u) break;
          __builtin_amdgcn_s_sleep(2);
        }
        CBAR();
      }
#pragma unroll
      for (int t = 0; t < 2; ++t) {
        kb[t] = kv_next();
        K[t][0] = ld16<16>(ws, l16, kb[t]); K[t][1] = ld16<16>(ws, l16 + KB, kb[t]);
        V[t][0] = ld16<16>(ws, l16 + 2 * KB, kb[t]); V[t][1] = ld16<16>(ws, l16 + 3 * KB, kb[t]);
        SBAR();
      }
#pragma unroll
      for (int t = 2; t < 4; ++t) { kb[t] = kb[0]; K[t][0] = K[t][1] = V[t][0] = V[t][1] = u32x4{}; }
      kvf = ld_tag(kpa); miss = ~0ull;
      f32x16 o0 = {}, o1 = {}, o2 = {}, o3 = {};
      PIN(o0); PIN(o1); PIN(o2); PIN(o3);
      if (h == 1) { qn1[2] = 0u; qn1[3] = 0u; }
      lds_w16(lds + L_QF + l16 + 2 * w * KB, qn0);
      if (pair) chk_handoff<2>(K[0], ws, l16, kb[0]);
      {
        f32x16 s0 = MFMA(K[0][0], qn0, Z);
        s0 = MFMA(K[0][1], qn1, s0);
        set_shift(qn1, xhalf_max(max16(s0, s0[0])), h, 0u);
        lds_w16(lds + L_QF + l16 + (2 * w + 1) * KB, qn1);
        lds_sig(cx, 7);
      }
      if (pair) chk_handoff<2>(V[0], ws, l16 + 2 * KB, kb[0]);
      lds_wait<0>(cx, 7, 4u);
#pragma unroll
      for (int a = 0; a < 4; ++a) { q[a][0] = lds_r16(lds + L_QF + l16 + 2 * a * KB); q[a][1] = lds_r16(lds + L_QF + l16 + (2 * a + 1) * KB); }
      f32x16 sa = MFMA(K[0][0], q[0][0], Z), sb;
      sa = MFMA(K[0][1], q[0][1], sa);
      u32x4 pp = {};
#define TILE(c, LD, LDV1) { \
        constexpr int n = (c + 1) & 3, pv = (c + 3) & 3; \
        auto ldV1 = [&]() { if (LDV1) V[pv][1] = ld16<16>(ws, l16 + 3 * KB, kb[pv]); }; \
        auto ldK = [&]() { if (LD) { kb[c] = kv_next(); K[c][0] = ld16<16>(ws, l16, kb[c]); K[c][1] = ld16<16>(ws, l16 + KB, kb[c]); } }; \
        auto ldV0 = [&]() { if (LD) V[c][0] = ld16<16>(ws, l16 + 2 * KB, kb[c]); }; \
        unit<true, true>(sa, sb, Z, o0, o3, pp, K[c][0], K[c][1], q[1][0], q[1][1], V[c][0], V[pv][1], ldV1, nop, nop); \
        unit<true, true>(sb, sa, Z, o1, o0, pp, K[c][0], K[c][1], q[2][0], q[2][1], V[c][0], V[c][1], nop, nop, nop); \
        if (LD && c == 0 && __builtin_expect(pend != 0u, 0)) {        \
          pend = 0u; kv_mask(); \
          _Pragma("unroll") for (int t = 2; t < 4; ++t) { kb[t] = kv_next(); K[t][0] = ld16<16>(ws, l16, kb[t]); K[t][1] = ld16<16>(ws, l16 + KB, kb[t]); \
            V[t][0] = ld16<16>(ws, l16 + 2 * KB, kb[t]); V[t][1] = ld16<16>(ws, l16 + 3 * KB, kb[t]); } } \
        unit<true, true>(sa, sb, Z, o2, o1, pp, K[c][0], K[c][1], q[3][0], q[3][1], V[c][0], V[c][1], nop, ldK, nop); \
        if (LD && c == 0 && __builtin_expect(chk != 0u, 0)) { chk = 0u; chk_handoff<2>(K[n], ws, l16, kb[n]); chk_handoff<2>(V[n], ws, l16 + 2 * KB, kb[n]); } \
        unit<true, true>(sb, sa, Z, o3, o2, pp, K[n][0], K[n][1], q[0][0], q[0][1], V[c][0], V[c][1], nop, nop, ldV0); }
#pragma unroll 1
      for (int tt = 0; tt < 12; tt += 4) {
        if (miss) { kv_mask(); if (miss) kvf = ld_tag(kpa); }
        TILE(0, true, true) TILE(1, true, true)
        if (FMID && miss) { kv_mask(); if (miss) kvf = ld_tag(kpa); }
        TILE(2, true, true) TILE(3, true, true)
      }
      TILE(0, false, true)
      lds_sig(cx, 11);
      lds_wait<1>(cx, 8, 4u);
      TILE(1, false, false)
      const ldsp red = scr + w * 4 * KB;
#define WR_LO(o, a) [&]() { lds_wf4(red + (a) * 16 * KB, f32x4{o[0], o[1], o[2], o[3]}); lds_wf4(red + (a) * 16 * KB + KB, f32x4{o[4], o[5], o[6], o[7]}); }
#define WR_HI(o, a) [&]() { lds_wf4(red + (a) * 16 * KB + 2 * KB, f32x4{o[8], o[9], o[10], o[11]}); lds_wf4(red + (a) * 16 * KB + 3 * KB, f32x4{o[12], o[13], o[14], o[15]}); lds_sig(cx, a); }
      unit<true, true>(sa, sb, Z, o0, o3, pp, K[3][0], K[3][1], q[0][0], q[0][1], V[2][0], V[1][1], nop, nop, nop);
      unit<true, true>(sb, sa, Z, o0, o0, pp, K[2][0], K[2][1], q[1][0], q[1][1], V[3][0], V[2][1], nop, nop, nop);
      unit<true, true>(sa, sb, Z, o1, o0, pp, K[3][0], K[3][1], q[1][0], q[1][1], V[2][0], V[3][1], nop, WR_LO(o0, 0), WR_HI(o0, 0));
      unit<true, true>(sb, sa, Z, o1, o1, pp, K[2][0], K[2][1], q[2][0], q[2][1], V[3][0], V[2][1], nop, nop, nop);
      unit<true, true>(sa, sb, Z, o2, o1, pp, K[3][0], K[3][1], q[2][0], q[2][1], V[2][0], V[3][1], nop, WR_LO(o1, 1), WR_HI(o1, 1));
      unit<true, true>(sb, sa, Z, o2, o2, pp, K[2][0], K[2][1], q[3][0], q[3][1], V[3][0], V[2][1], nop, nop, nop);
      unit<true, true>(sa, sb, Z, o3, o2, pp, K[3][0], K[3][1], q[3][0], q[3][1], V[2][0], V[3][1], nop, WR_LO(o2, 2), WR_HI(o2, 2));
      unit<false, true>(sb, sa, Z, o3, o3, pp, K[2][0], K[2][1], q[3][0], q[3][1], V[3][0], V[2][1], nop, nop, nop);
      o3 = MFMA(V[3][1], pp, o3);
      WR_LO(o3, 3)(); WR_HI(o3, 3)();
    }
    {
      u32x4 of[8] = {};
      const int src = R_OFA + (rt * 32 + 8 * w) * KB;
      if (w != sub) {
        poll_wait(cx, (((jj & ~3) | w) << 3) | xcd, 0);
        ld_handoff<8>(of, ws, l16, src);
      }
      BAR();
      const unsigned bad = __builtin_amdgcn_readfirstlane(*(LAS unsigned*)(cx.sig + 10 * 16));
      if (__builtin_expect((bad >> w) & 1u, 0)) {
        const u32x4 q0 = lds_r16(lds + L_QF + l16 + 2 * w * KB);
        u32x4 q1 = lds_r16(lds + L_QF + l16 + (2 * w + 1) * KB);
        if (h == 1) { q1[2] = 0; q1[3] = 0; }
        float m = -INFINITY;
#pragma unroll 1
        for (int t = 0; t < 64; ++t) {
          f32x16 s = MFMA(ld16<16>(ws, l16, R_KVA + t * 4 * KB), q0, Z);
          s = MFMA(ld16<16>(ws, l16 + KB, R_KVA + t * 4 * KB), q1, s);
          m = max16(s, m);
        }
        set_shift(q1, xhalf_max(m), h, 0u);
        f32x16 o = {};
#pragma unroll 1
        for (int t = 0; t < 64; ++t) {
          f32x16 s = MFMA(ld16<16>(ws, l16, R_KVA + t * 4 * KB), q0, Z);
          s = MFMA(ld16<16>(ws, l16 + KB, R_KVA + t * 4 * KB), q1, s);
          u32x4 p0, p1;
          exp_pack(s, p0, p1);
          o = MFMA(ld16<16>(ws, l16 + 2 * KB, R_KVA + t * 4 * KB), p0, o);
          o = MFMA(ld16<16>(ws, l16 + 3 * KB, R_KVA + t * 4 * KB), p1, o);
        }
        u32x4 y0, y1;
        norm_pack(o, h, y0, y1);
        st16<16>(y0, ws, l16, R_OFA + (rt * 32 + sub * 8 + 2 * w) * KB);
        st16<16>(y1, ws, l16 + KB, R_OFA + (rt * 32 + sub * 8 + 2 * w) * KB);
        lds_w16(scr + (sub * 8 + 2 * w) * KB, y0); lds_w16(scr + (sub * 8 + 2 * w + 1) * KB, y1);
      }
      if (w != sub) {
        chk_handoff<8>(of, ws, l16, src);
#pragma unroll
        for (int i = 0; i < 8; ++i) lds_w16(scr + (8 * w + i) * KB, of[i]);
      }
      BAR();
    }
    f32x4 ck[4], cq[4];
    const float cv = *(LAS float*)(lds + M_CS + 6 * 128 + r * 4);
#pragma unroll
    for (int g = 0; g < 4; ++g) { cq[g] = lds_rf4(lds + M_CS + 4 * 128 + (8 * g + 4 * h) * 4); ck[g] = lds_rf4(lds + M_CS + 5 * 128 + (8 * g + 4 * h) * 4); }
    BAR();
    u32x4 q0, q1, k0, k1, v0, v1;
    const Clk ck0 = clk_now();
    {
      const int xo = R_XF1 + (rb * 4 + w) * 28 * KB;
      const ldsp wb0 = opaque(lds + L_WB + l16), wb1 = opaque(lds + L_WB + 28 * KB + l16);
      const ldsp st = lds + M_ST + w * 256;
      f32x4 m4[4], r4[4];
      poll_wait(cx, (((jj & ~15) | (w << 2) | (lane & 3)) << 3) | xcd, 1);
      f32x16 aq, ak, av;
      float mu, rs;
      u32x4 xf[28], wk[3], wv[3], wq[4];
#define LDX(i) do { xf[i] = ld16<16>(ws, l16 + ((i) & 3) * KB, xo + ((i) >> 2) * 4 * KB); SBAR(); } while (0)
#define CHKX(i) do { if (__builtin_expect(__builtin_amdgcn_ballot_w64(smax(0u, xf[i]) == SENT) != 0ull, 0)) { \
          _Pragma("unroll 1") for (int spin = 0; spin < SPIN_CAP; ++spin) { CBAR(); LDX(i); if (__builtin_amdgcn_ballot_w64(smax(0u, xf[i]) == SENT) == 0ull) break; } } SBAR(); } while (0)
#define QST(i) do { if ((i) < 28) { SBAR(); aq = MFMA(wq[(i) & 3], xf[(i) < 28 ? (i) : 0], (i) ? aq : Z); SBAR(); if ((i) + 3 < 28) wq[((i) + 3) & 3] = lds_r16(wb0 + ((i) + 3) * KB); SBAR(); } } while (0)
#pragma unroll
      for (int i = 0; i < X1_AHEAD; ++i) LDX(i);
#pragma unroll
      for (int i = 0; i < 2; ++i) { wk[i] = lds_r16(wb1 + i * KB); wv[i] = lds_r16(wb1 + (28 + i) * KB); }
#pragma unroll
      for (int i = 0; i < 3; ++i) wq[i] = lds_r16(wb0 + i * KB);
      SBAR();
      CHKX(0);
#pragma unroll
      for (int ks = 0; ks < 28; ++ks) {
        ak = MFMA(wk[ks % 3], xf[ks], ks ? ak : Z); SBAR();
        if (ks + 2 < 28) { wk[(ks + 2) % 3] = lds_r16(wb1 + (ks + 2) * KB); wv[(ks + 2) % 3] = lds_r16(wb1 + (28 + ks + 2) * KB); }
        if (X1_AHEAD + 2 * ks < 28) LDX(X1_AHEAD + 2 * ks);
        if (ks == 25) {
          lds_wait<0>(cx, 12 + w, 1u);
          mu = *(LAS float*)(st + r * 4); rs = *(LAS float*)(st + 128 + r * 4);
        }
        if (ks == 26) {
#pragma unroll
          for (int g = 0; g < 4; ++g) { m4[g] = lds_rf4(st + (8 * g + 4 * h) * 4); r4[g] = lds_rf4(st + 128 + (8 * g + 4 * h) * 4); }
        }
        SBAR();
        av = MFMA(xf[ks], wv[ks % 3], ks ? av : Z); SBAR();
        if (X1_AHEAD + 2 * ks + 1 < 28) LDX(X1_AHEAD + 2 * ks + 1);
        if (ks < QS) QST(ks);
        if (ks + 1 < 28) CHKX(ks + 1);
        if (ks == POST_KV) post(cx, b, 4 + w, ck0, DT_KV);
      }
      const int ko = R_KVB + (hd * 64 + rb * 4 + w) * 4 * KB;
      QST(QS); QST(QS + 1);
#pragma unroll
      for (int g = 0; g < 4; ++g) {
#pragma unroll
        for (int i = 4 * g; i < 4 * g + 4; ++i) ak[i] = (ak[i] - mu * ck[g][i & 3]) * rs;
        unsigned a0 = pk(ak[4 * g], ak[4 * g + 1]), a1 = pk(ak[4 * g + 2], ak[4 * g + 3]);
        if (g == 3 && h == 1) { a0 = ONES2; a1 = 0x00003F80u; }
        PIN(a0); PIN(a1);
        if (g < 2) { k0[2 * g] = a0; k0[2 * g + 1] = a1; } else { k1[2 * g - 4] = a0; k1[2 * g - 3] = a1; }
        if (g == 1) st16<16>(k0, ws, l16, ko);
        if (g == 3) st16<16>(k1, ws, l16 + KB, ko);
        QST(QS + 2 + g);
      }
      const unsigned vpad = r == 28 ? ONES2 : 0u;
#pragma unroll
      for (int g = 0; g < 4; ++g) {
#pragma unroll
        for (int i = 4 * g; i < 4 * g + 4; ++i) av[i] = (av[i] - m4[g][i & 3] * cv) * r4[g][i & 3];
        unsigned a0 = pk(av[4 * g], av[4 * g + 1]), a1 = pk(av[4 * g + 2], av[4 * g + 3]);
        if (r >= 28) { a0 = vpad; a1 = vpad; }
        PIN(a0); PIN(a1);
        if (g < 2) { v0[2 * g] = a0; v0[2 * g + 1] = a1; } else { v1[2 * g - 4] = a0; v1[2 * g - 3] = a1; }
        if (g == 1) st16<16>(v0, ws, l16 + 2 * KB, ko);
        if (g == 3) st16<16>(v1, ws, l16 + 3 * KB, ko);
        QST(QS + 6 + g);
      }
      lds_w16(scr + (32 + 4 * w) * KB, k0); QST(QS + 10); lds_w16(scr + (33 + 4 * w) * KB, k1); QST(QS + 11);
      lds_w16(scr + (34 + 4 * w) * KB, v0); QST(QS + 12); lds_w16(scr + (35 + 4 * w) * KB, v1);
      lds_sig(cx, 4);
#pragma unroll
      for (int i = QS + 13; i < 28; ++i) QST(i);
      const float rq = rs * C2;
#pragma unroll
      for (int i = 0; i < 16; ++i) aq[i] = (aq[i] - mu * cq[i >> 2][i & 3]) * rq;
      q0 = pack8(aq, 0); q1 = pack8(aq, 1);
      if (h == 1) { q1[2] = 0; q1[3] = NEG31; }
    }
    {
      f32x16 cd, cf;
#pragma unroll
      for (int i = 0; i < 16; ++i) {
        const int kk = (i & 3) + 8 * (i >> 2) + 4 * h;
        cd[i] = kk <= r ? 0.f : -1e30f; cf[i] = kk <= r ? -1e30f : 0.f;
      }
      f32x16 sa = MFMA(k0, q0, cd), sb;
      sa = MFMA(k1, q1, sa);
      set_shift(q1, xhalf_max(max16(sa, sa[0])), h, NEG31);
      const ldsp kp = scr + (8 + w) * 4 * KB;
      auto need = [&](int u) {
        const int ti = 8 + w - u;
        if (u == 1 && w > 0) lds_wait<0>(cx, 4, 4u);
        if (__builtin_expect((ti & 3) == 3, 0)) lds_wait<1>(cx, 5 + (ti >> 2), 2u);
      };
      u32x4 K[2][2], V[2][2], pp;
      f32x16 o = {};
      sa = MFMA(k0, q0, cd); sa = MFMA(k1, q1, sa);
      V[0][0] = v0; V[0][1] = v1;
      need(1);
      K[1][0] = lds_r16(kp - 4 * KB); K[1][1] = lds_r16(kp - 3 * KB); V[1][0] = lds_r16(kp - 2 * KB);
#pragma unroll
      for (int u = 0; u < 9; ++u) {
        const int c = u & 1, n = c ^ 1;
        auto ha = [&]() {
          if (u + 1 < 9) V[n][1] = lds_r16(kp - (u + 1) * 4 * KB + 3 * KB);
          if (u + 2 < 9) { need(u + 2); K[c][0] = lds_r16(kp - (u + 2) * 4 * KB); K[c][1] = lds_r16(kp - (u + 2) * 4 * KB + KB); }
        };
        auto hc = [&]() { if (u + 2 < 9) V[c][0] = lds_r16(kp - (u + 2) * 4 * KB + 2 * KB); if (u >= 4 && u <= 6 && __builtin_expect(u == (w < 2 ? 4 : 3 + w), 0)) post(cx, b, 8 + w, ck0, DT_OB + (8 - u) * DT_UNIT); };
        f32x16& s = c ? sb : sa; f32x16& sn = c ? sa : sb;
        if (u == 0) unit<true, false>(s, sn, Z, o, o, pp, K[n][0], K[n][1], q0, q1, V[c][0], V[n][1], ha, nop, hc);
        else if (u < 7) unit<true, true>(s, sn, Z, o, o, pp, K[n][0], K[n][1], q0, q1, V[c][0], V[n][1], ha, nop, hc);
        else if (u == 7) unit<true, true>(s, sn, cf, o, o, pp, K[n][0], K[n][1], q0, q1, V[c][0], V[n][1], ha, nop, hc);
        else unit<false, true>(s, sn, Z, o, o, pp, K[n][0], K[n][1], q0, q1, V[c][0], V[n][1], ha, nop, hc);
      }
      o = MFMA(V[0][1], pp, o);
      if (__builtin_expect(__builtin_amdgcn_ballot_w64(h == 1 && !(o[12] < LBIG)) != 0ull, 0)) {
        if (h == 1) { q1[2] = 0; q1[3] = NEG31; }
        float m = -INFINITY;
#pragma unroll 1
        for (int u = 0; u < 9; ++u) {
          f32x16 s;
#pragma unroll
          for (int i = 0; i < 16; ++i) s[i] = u == 0 ? cd[i] : u == 8 ? cf[i] : 0.f;
          s = MFMA(lds_r16(kp - u * 4 * KB), q0, s); s = MFMA(lds_r16(kp - u * 4 * KB + KB), q1, s);
          m = max16(s, m);
        }
        set_shift(q1, xhalf_max(m), h, NEG31);
        o = Z;
#pragma unroll 1
        for (int u = 0; u < 9; ++u) {
          f32x16 s;
#pragma unroll
          for (int i = 0; i < 16; ++i) s[i] = u == 0 ? cd[i] : u == 8 ? cf[i] : 0.f;
          s = MFMA(lds_r16(kp - u * 4 * KB), q0, s); s = MFMA(lds_r16(kp - u * 4 * KB + KB), q1, s);
          u32x4 p0, p1;
          exp_pack(s, p0, p1);
          o = MFMA(lds_r16(kp - u * 4 * KB + 2 * KB), p0, o); o = MFMA(lds_r16(kp - u * 4 * KB + 3 * KB), p1, o);
        }
      }
      u32x4 y0, y1;
      norm_pack(o, h, y0, y1);
      const int oo = R_OFB + ((rb * 4 + w) * 16 + hd) * 2 * KB;
      st16<16>(y0, ws, l16, oo); st16<16>(y1, ws, l16 + KB, oo);
      lds_sig(cx, 9);
    }
    u32x4 wa1[WA1];
    {
      u32x4 of[8];
      const int src = R_OFB + (rt * 32 + 8 * w) * KB;
#pragma unroll
      for (int i = 0; i < WA1; ++i) { wa1[i] = ld16<16>(wt, l16 + (i & 3) * KB, pair ? OFF_PAIR : OFF_WAT + ((4 * sub + w) * 28 + (i & ~3)) * KB); SBAR(); }
      poll_wait(cx, (((jj & ~15) | (w << 2) | (lane & 3)) << 3) | xcd, 8 + (rt & 3));
      ld_handoff<8>(of, ws, l16, src);
      chk_handoff<8>(of, ws, l16, src);
      lds_wait<1>(cx, 9, 4u);
#pragma unroll
      for (int i = 0; i < 8; ++i) lds_w16(scr + (8 * w + i) * KB, of[i]);
      BAR();
    }
    if (pair == 0) {
      u32x4 wa[28];
#pragma unroll
      for (int i = 0; i < WA1; ++i) wa[i] = wa1[i];
#pragma unroll
      for (int i = WA1; i < 28; ++i) { wa[i] = ld16<16>(wt, l16 + (i & 3) * KB, OFF_WAT + ((4 * sub + w) * 28 + (i & ~3)) * KB); SBAR(); }
      f32x4 cq[4];
#pragma unroll
      for (int g = 0; g < 4; ++g) cq[g] = lds_rf4(lds + M_CS + w * 128 + (8 * g + 4 * h) * 4);
      if (w >= 2) poll_wait<2>(cx, (((jj & ~3) | (lane & 3)) << 3) | xcd, 2);
      if (w == 3) stats_gather(ws, R_ST2 + rt * 14 * 256, lds + M_ST + 4 * 256, lane);
      if (w == 2 && sub < 2) {
        const unsigned t = eta(ck0, DT_Q);
        if (lane == 0) __hip_atomic_store((GAS unsigned*)(cx.flg + PAIR_SZ + b * 128 + 12 * 4), t, __ATOMIC_RELAXED, __HIP_MEMORY_SCOPE_AGENT);
      }
      BAR();
      if (sub < 2 && w < 2) __builtin_amdgcn_s_sleep(4);
      const float mu = *(LAS float*)(lds + M_ST + 4 * 256 + r * 4), rq = *(LAS float*)(lds + M_ST + 4 * 256 + 128 + r * 4) * C2;
      f32x16 acc = gemm_lds<28, false>(wa, scr + 32 * KB, [&](int i) { if (i == KVE_AT) kve = flag_ld(cx, PAIR_SZ / 128 + (((((4 * (lane & 1) + w) << 2) | ((lane >> 1) & 1)) << 3) | xcd), 12); });
#pragma unroll
      for (int i = 0; i < 16; ++i) acc[i] = (acc[i] - mu * cq[i >> 2][i & 3]) * rq;
      qn0 = pack8(acc, 0); qn1 = pack8(acc, 1);
    }
   }
   return;
  }

  __builtin_amdgcn_s_setprio(2);
  phase0_h(p, lds, epo, tag);
  u32x4 x0 = {}, x1 = {};
#pragma unroll 1
 for (int pair = 0; pair < 2; ++pair) {
  PAIR_SETUP();
  __builtin_amdgcn_s_setprio(HPRIO);
  const int ct0 = sub < 2 ? 4 * sub : 3 * sub + 2, nct = sub < 2 ? 4 : 3;
  const bool ctok = w < nct;
  const int ct = ct0 + (ctok ? w : 0);
  const int xo1 = R_XF1 + (rt * 28 + ct * 2) * KB, xo2 = R_XF2 + (rt * 28 + ct * 2) * KB;
  const int so1 = R_ST1 + (rt * 14 + ct) * 256, so2 = R_ST2 + (rt * 14 + ct) * 256;
  lds_wait<4>(cx, 7, 4u);
  __builtin_amdgcn_s_setprio(HPRIO);
  u32x4 wf[32];
  Clk ck0 = clk_now();
  {
    const u32x4 sv = {SENT, SENT, SENT, SENT};
    auto fill = [&](int i) {
      const int hi = (i & 1) * KB;
      if (i < 2) st16<16>(sv, ws, l16 + hi, R_OFA + (rt * 32 + sub * 8 + 2 * w) * KB);
      else if (i < 4) { if (ctok) { st16<16>(sv, ws, l16 + hi, xo1); if (i == 2) st4<16>(SENT, ws, lane * 4, so1); } }
      else if (i < 8) st16<16>(sv, ws, l16 + (i - 4) * KB, R_KVB + (hd * 64 + rb * 4 + w) * 4 * KB);
      else if (i < 10) st16<16>(sv, ws, l16 + hi, R_OFB + ((rb * 4 + w) * 16 + hd) * 2 * KB);
      else if (i < 12) { if (ctok && pair == 0) { st16<16>(sv, ws, l16 + hi, xo2); if (i == 10) st4<16>(SENT, ws, lane * 4, so2); } }
      else if (i == 12 && pair == 0 && sub < 2 && w < 2) st16<16>(sv, ws, l16, PAIR_SZ + R_KVA + (rt * 4 + 2 * sub + w) * KB);
      SBAR();
    };
#pragma unroll
    for (int i = 0; i < NFILL0; ++i) { fill(i); __builtin_amdgcn_s_sleep(PACEF); }
    if (pair == 0) {
#pragma unroll 1
      for (int spin = 0; spin < SPIN_CAP; ++spin) {
        bool ok = true;
#pragma unroll
        for (int i = 0; i < 4; ++i) ok &= ld_tag(p.ws + OFF_INI + (xcd * 256 + i * 64 + lane) * 8) == tag;
        if (__builtin_amdgcn_ballot_w64(!ok) == 0ull) break;
        __builtin_amdgcn_s_sleep(8);
      }
      CBAR();
    }
    unsigned c4[16] = {};
    if (pair == 0) {
      const unsigned short* xr = p.x + (size_t)(rt * 32 + r) * D_ + ct * 32 + 4 * h;
      const u32x2 a0 = *(const u32x2*)xr, a1 = *(const u32x2*)(xr + 8), a2 = *(const u32x2*)(xr + 16), a3 = *(const u32x2*)(xr + 24);
      x0 = u32x4{a0[0], a0[1], a1[0], a1[1]}; x1 = u32x4{a2[0], a2[1], a3[0], a3[1]};
      const int co = (h ? (w < 3 ? 16 + hd * 3 + w : 64 + (sub & 1)) : 4 * sub + w) * 2048 + r * 4;
#pragma unroll
      for (int i = 0; i < 16; ++i) c4[i] = ld4<16>(wt, co + i * 128, OFF_CS);
      SBAR();
    }
    const unsigned char* wbsrc = p.ws + OFF_WBT + (hd * 84 + w * 21) * KB + l16;
    const unsigned wbdst = (unsigned)(uintptr_t)(lds + L_WB) + w * 21 * KB;
#pragma unroll
    for (int i = 0; i < 32; ++i) {
      wf[i] = ld16<16>(wt, l16 + (i & 3) * KB, OFF_WOA + (ct * 32 + (i & ~3)) * KB); SBAR();
      if (i % 3 != 2 || i > 29) { const int g = i - (i + 1) / 3; if (g < 21 && pair == 0) glds16_sc1(wbsrc + g * KB, wbdst + g * KB); }
      if ((i & 1) == 0 && NFILL0 + (i >> 1) < 13) fill(NFILL0 + (i >> 1));
      if (i == 10) { asm volatile("s_waitcnt vmcnt(5)" ::: "memory"); lds_sig(cx, 8); }
      if (i % PGRP == PGRP - 1) { SBAR(); if (pair) __builtin_amdgcn_s_sleep(PACE3); else __builtin_amdgcn_s_sleep(PACE); SBAR(); }
    }
    if (pair == 0) {
      float t = 0.f;
#pragma unroll
      for (int i = 0; i < 16; ++i) t += __uint_as_float(c4[i]);
      *(LAS float*)(lds + M_CS + (4 * h + w) * 128 + r * 4) = t;
    }
    asm volatile("s_waitcnt vmcnt(0)" ::: "memory");
  }
  {
    u32x4 keep[2];
    const int s = w & 1;
    __builtin_amdgcn_s_setprio(2);
    lds_wait<2>(cx, 8, 4u);
#pragma unroll 1
    for (int spin = 0; spin < SPIN_CAP; ++spin) {
      if (__builtin_amdgcn_readfirstlane(*(volatile LAS unsigned*)(cx.sig + 11 * 16 + w * 4))) break;
      __builtin_amdgcn_s_sleep(DOZE);
    }
#pragma unroll
    for (int it = 0; it < 2; ++it) {
      const int T = 2 * it + (w >> 1);
      if (it == 0) lds_wait<2>(cx, T, 4u); else lds_wait<1>(cx, T, 4u);
      if (it == 0) { if (w == POST_OA) post(cx, b, 0, ck0, DT_OA); ck0 = clk_now(); }

      const ldsp red = scr + T * 16 * KB;
      f32x4 a0[4], a1[4]; float l4[4];
#pragma unroll
      for (int k = 0; k < 4; ++k) {
        l4[k] = *(LAS float*)(lds + L_SCR + T * 16 * KB + k * 4 * KB + 3 * KB + (32 + r) * 16);
        a0[k] = lds_rf4(red + k * 4 * KB + (2 * s) * KB);
        a1[k] = lds_rf4(red + k * 4 * KB + (2 * s + 1) * KB);
      }
      const float l = (l4[0] + l4[1]) + (l4[2] + l4[3]);
      const f32x4 b0 = (a0[0] + a0[1]) + (a0[2] + a0[3]), b1 = (a1[0] + a1[1]) + (a1[2] + a1[3]);
      const float inv = __builtin_amdgcn_rcpf(l);
      u32x4 o = {pk(b0[0] * inv, b0[1] * inv), pk(b0[2] * inv, b0[3] * inv), pk(b1[0] * inv, b1[1] * inv), pk(b1[2] * inv, b1[3] * inv)};
      if (s == 1 && h == 1) { o[2] = 0; o[3] = 0; }
      if (__builtin_expect(__builtin_amdgcn_ballot_w64(!(l < LBIG)) == 0ull, 1)) st16<16>(o, ws, l16, R_OFA + (rt * 32 + sub * 8 + 2 * T + s) * KB);
      else if (lane == 0) __hip_atomic_fetch_or((LAS unsigned*)(cx.sig + 10 * 16), 1u << T, __ATOMIC_RELAXED, __HIP_MEMORY_SCOPE_WORKGROUP);
      keep[it] = o;
    }
    BAR();
    const unsigned bad = __builtin_amdgcn_readfirstlane(*(LAS unsigned*)(cx.sig + 10 * 16));
#pragma unroll
    for (int it = 0; it < 2; ++it) {
      const int T = 2 * it + (w >> 1);
      if (!((bad >> T) & 1u)) lds_w16(scr + (sub * 8 + 2 * T + s) * KB, keep[it]);
    }
    BAR();
  }
  {
    const f32x16 acc = gemm_lds<32, false>(wf, scr, [&](int i) { if (i == 0 && w == 0) post(cx, b, 1, ck0, DT_X); if (i == 24) BAR();   });
    add_res(acc, x0, x1, x0, x1);
    if (ctok) { st16<16>(x0, ws, l16, xo1); st16<16>(x1, ws, l16 + KB, xo1); SBAR(); st4<16>(__float_as_uint(row_stats(x0, x1, h)), ws, lane * 4, so1); }
  }
  u32x4 wkv[7];
  {
    __builtin_amdgcn_s_setprio(0);
    poll_wait<2>(cx, (((jj & ~15) | (w << 2) | (lane & 3)) << 3) | xcd, 1);
    __builtin_amdgcn_s_sleep(ST_DLY);
    stats_gather(ws, R_ST1 + (rb * 4 + w) * 14 * 256, lds + M_ST + w * 256, lane);
    lds_sig(cx, 12 + w);
    const int kt = 4 * rb - 8 + 2 * w;
    u32x4 hv[8];
    if (kt >= 0) {
      const int prb = kt >> 2;
      __builtin_amdgcn_s_setprio(0);
      poll_wait<HALO_SLP>(cx, (((prb & 1) * 16 + hd) << 3) | (prb >> 1), 4 + (kt & 3) + (lane & 1));
      __builtin_amdgcn_s_setprio(2);
      ld_handoff<8>(hv, ws, l16, R_KVB + (hd * 64 + kt) * 4 * KB);
      chk_handoff<8>(hv, ws, l16, R_KVB + (hd * 64 + kt) * 4 * KB);
    } else {
#pragma unroll
      for (int i = 0; i < 8; ++i) hv[i] = u32x4{0u, 0u, 0u, ((i & 3) == 1 && h == 1) ? 0x3F800000u : 0u};
    }
#pragma unroll
    for (int i = 0; i < 8; ++i) lds_w16(scr + (8 * w + i) * KB, hv[i]);
    lds_sig(cx, 5 + (w >> 1));
    __builtin_amdgcn_s_setprio(0);
#pragma unroll
    for (int i = 0; i < 32; ++i) { wf[i] = ld16<16>(wt, l16 + (i & 3) * KB, OFF_WOB + (ct * 32 + (i & ~3)) * KB); SBAR(); }
#pragma unroll
    for (int i = 0; i < 7; ++i) wkv[i] = ld16<16>(wt, l16 + (i & 3) * KB, OFF_WKV + ((sub & 1) * 28 + 7 * w + (i & 4)) * KB);
    asm volatile("s_waitcnt vmcnt(0)" ::: "memory");
    BAR();
  }
  __builtin_amdgcn_s_setprio(2);
  {
    const f32x16 acc = gemm_lds<32, false>(wf, scr, [&](int i) { if (i == 0 && w == 0 && pair == 0) post(cx, b, 2, ck0, DT_X); if (i == 31 && pair == 0) lds_sig(cx, 32 + 5); });
    add_res(acc, x0, x1, x0, x1);
    if (pair == 1) {
      if (ctok) {
        unsigned short* orow = p.out + (size_t)(rt * 32 + r) * D_ + ct * 32 + 4 * h;
        *(u32x2*)orow = u32x2{x0[0], x0[1]}; *(u32x2*)(orow + 8) = u32x2{x0[2], x0[3]};
        *(u32x2*)(orow + 16) = u32x2{x1[0], x1[1]}; *(u32x2*)(orow + 24) = u32x2{x1[2], x1[3]};
      }
      if (b == 0 && w == 0 && lane == 0) st_tag(p.ws + OFF_EPO, tag);
      return;
    }
    if (ctok) { st16<16>(x0, ws, l16, xo2); st16<16>(x1, ws, l16 + KB, xo2); SBAR(); st4<16>(__float_as_uint(row_stats(x0, x1, h)), ws, lane * 4, so2); }
  }
  {
    u32x4 xf[7];
    const int src = R_XF2 + (rt * 28 + 7 * w) * KB;
    lds_wait<0>(cx, 32 + 5, 4u);
    poll_wait(cx, (((jj & ~3) | (lane & 3)) << 3) | xcd, 2);
    ld_handoff<7>(xf, ws, l16, src);
    chk_handoff<7>(xf, ws, l16, src);
    if (sub < 2) {
      f32x16 acc = {};
      if (sub) {
#pragma unroll
        for (int i = 0; i < 7; ++i) { acc = MFMA(xf[i], wkv[i], acc); SBAR(); lds_w16(scr + (32 + 7 * w + i) * KB, xf[i]); SBAR(); }
      } else {
#pragma unroll
        for (int i = 0; i < 7; ++i) { acc = MFMA(wkv[i], xf[i], acc); SBAR(); lds_w16(scr + (32 + 7 * w + i) * KB, xf[i]); SBAR(); }
      }
      part_write(scr + w * 4 * KB, acc);
    } else {
#pragma unroll
      for (int i = 0; i < 7; ++i) lds_w16(scr + (32 + 7 * w + i) * KB, xf[i]);
    }
    BAR();
    if (sub < 2 && w < 2) kv_finish<true>(lds + L_SCR, 4 * KB, sub, w, lds + M_ST + 4 * 256, lds + M_CS + 7 * 128, lane, ws, PAIR_SZ + R_KVA + rt * 4 * KB, reg + PAIR_SZ + R_FKV + rt * 128, tag);
  }
 }
}

extern "C" void kernel_launch(void* const* d_in, const int* in_sizes, int n_in, void* d_out, int out_size, void* d_ws, size_t ws_size,
                              hipStream_t stream) {
  Params p;
  p.x = (const unsigned short*)d_in[0];
  p.wq = (const unsigned short*)d_in[1];
  p.wk = (const unsigned short*)d_in[2];
  p.wv = (const unsigned short*)d_in[3];
  p.woa = (const unsigned short*)d_in[4];
  p.wqkv = (const unsigned short*)d_in[6];
  p.wob = (const unsigned short*)d_in[7];
  p.out = (unsigned short*)d_out;
  p.ws = (unsigned char*)d_ws;
  k_main<<<256, 512, 0, stream>>>(p);
}
```

```cpp
#include <hip/hip_runtime.h>
#include <hip/hip_bf16.h>
#include <stdint.h>

typedef short bf16x8 __attribute__((ext_vector_type(8)));
typedef short s16x4 __attribute__((ext_vector_type(4)));
typedef float f32x16 __attribute__((ext_vector_type(16)));
typedef float f32x4 __attribute__((ext_vector_type(4)));
typedef float f32x2 __attribute__((ext_vector_type(2)));
typedef unsigned u32x4 __attribute__((ext_vector_type(4)));
typedef unsigned u32x2 __attribute__((ext_vector_type(2)));
typedef __bf16 bf16x2_t __attribute__((ext_vector_type(2)));
#define DI __device__ __forceinline__
#define LAS __attribute__((address_space(3)))
#define GAS __attribute__((address_space(1)))
typedef LAS char* ldsp;

#define MFMA(a, b, c) __builtin_amdgcn_mfma_f32_32x32x16_bf16(__builtin_bit_cast(bf16x8, (a)), __builtin_bit_cast(bf16x8, (b)), (c), 0, 0, 0)
#define BAR() asm volatile("s_waitcnt lgkmcnt(0)\n\ts_barrier" ::: "memory")
#define CBAR() asm volatile("" ::: "memory")
#define SBAR() __builtin_amdgcn_sched_barrier(0)

constexpr int D_ = 448;
constexpr float C2 = 0.18898223650461362f * 1.4426950408889634f;
constexpr float EPS = 1e-5f;
constexpr unsigned SENT = 0xFFFFFFFFu;
constexpr unsigned ONES2 = 0x3F803F80u;
constexpr int SPIN_CAP = 1 << 16;

constexpr int KB = 1024;
constexpr int OFF_WAT = 0;
constexpr int OFF_WBT = OFF_WAT + 448 * KB;
constexpr int OFF_WKV = OFF_WBT + 1344 * KB;
constexpr int OFF_WOA = OFF_WKV + 56 * KB;
constexpr int OFF_WOB = OFF_WOA + 448 * KB;
constexpr int OFF_CS = OFF_WOB + 448 * KB;
constexpr int OFF_EPO = OFF_CS + 132 * KB;
constexpr int OFF_INI = OFF_EPO + KB;
constexpr int OFF_PAIR = OFF_INI + 16 * KB;
constexpr int QA_SZ = 64 * 28 * KB;
constexpr int R_KVA = 0;
constexpr int R_OFA = R_KVA + 256 * KB;
constexpr int R_XF1 = R_OFA + 2048 * KB;
constexpr int R_KVB = R_XF1 + QA_SZ;
constexpr int R_OFB = R_KVB + 4096 * KB;
constexpr int R_XF2 = R_OFB + 2048 * KB;
constexpr int R_ST1 = R_XF2 + QA_SZ;
constexpr int R_ST2 = R_ST1 + 64 * 14 * 256;
constexpr int R_FLG = R_ST2 + 64 * 14 * 256;
constexpr int R_FKV = R_FLG + 32 * KB;
constexpr int PAIR_SZ = R_FKV + 64 * KB;
constexpr int WS_BYTES = OFF_PAIR + 2 * PAIR_SZ;

constexpr int PTRUNC = 1, FSKIP = 1, KVE_AT = 8, HPRIO = 0, DT_OA = 1900, DT_X = 1850, DT_KV = 3700, DT_OB = 850;
constexpr int DT_UNIT = 240, X1_AHEAD = 8, WA1 = 8, PRO_DLY = 2, PACE3 = 8, DT_Q = 3000, QS = 14, ST_DLY = 20;
constexpr int CODE_KB = 44, PGRP = 2, DOZE = 14, NFILL0 = 6, PACEF = 2, HALO_SLP = 8, PACE = 3, POST_OA = 2;
constexpr int POST_KV = 12;

DI unsigned pk(float a, float b) { f32x2 v = {a, b}; bf16x2_t r = __builtin_convertvector(v, bf16x2_t); return __builtin_bit_cast(unsigned, r); }
DI unsigned pkt(float a, float b) { return PTRUNC ? __builtin_amdgcn_perm(__float_as_uint(b), __float_as_uint(a), 0x07060302u) : pk(a, b); }
DI float dot2(unsigned a, unsigned b, float c) { return __builtin_amdgcn_fdot2_f32_bf16(__builtin_bit_cast(bf16x2_t, a), __builtin_bit_cast(bf16x2_t, b), c, false); }
DI float bflo(unsigned u) { return __uint_as_float(u << 16); }
DI float bfhi(unsigned u) { return __uint_as_float(u & 0xFFFF0000u); }
DI float xhalf_add(float v) { auto r = __builtin_amdgcn_permlane32_swap(__float_as_uint(v), __float_as_uint(v), false, false); return __uint_as_float(r[0]) + __uint_as_float(r[1]); }
DI float xhalf_max(float v) { auto r = __builtin_amdgcn_permlane32_swap(__float_as_uint(v), __float_as_uint(v), false, false); return __builtin_fmaxf(__uint_as_float(r[0]), __uint_as_float(r[1])); }
DI float from_hi(float v) { auto r = __builtin_amdgcn_permlane32_swap(__float_as_uint(v), __float_as_uint(v), false, false); return __uint_as_float(r[1]); }
DI float max3f(float a, float b, float c) { return __builtin_fmaxf(__builtin_fmaxf(a, b), c); }
DI float max16(const f32x16& s, float m) {
#pragma unroll
  for (int i = 0; i < 16; i += 2) m = max3f(m, s[i], s[i + 1]);
  return m;
}
typedef __amdgpu_buffer_rsrc_t rsrc_t;
DI rsrc_t mkrs(const void* p, unsigned n) { return __builtin_amdgcn_make_buffer_rsrc((void*)p, 0, n, 0x00020000); }
template <int AUX> DI u32x4 ld16(rsrc_t rs, int voff, int soff) { return __builtin_amdgcn_raw_buffer_load_b128(rs, voff, soff, AUX); }
template <int AUX> DI void st16(u32x4 v, rsrc_t rs, int voff, int soff) { __builtin_amdgcn_raw_buffer_store_b128(v, rs, voff, soff, AUX); }
template <int AUX> DI unsigned ld4(rsrc_t rs, int voff, int soff) { return __builtin_amdgcn_raw_buffer_load_b32(rs, voff, soff, AUX); }
template <int AUX> DI void st4(unsigned v, rsrc_t rs, int voff, int soff) { __builtin_amdgcn_raw_buffer_store_b32(v, rs, voff, soff, AUX); }
DI ldsp opaque(ldsp p) { unsigned a = (unsigned)(uintptr_t)p; asm volatile("" : "+v"(a)); return (ldsp)(uintptr_t)a; }
typedef unsigned long long u64;
DI u64 ld_tag(const unsigned char* a) { return __hip_atomic_load((GAS u64*)a, __ATOMIC_RELAXED, __HIP_MEMORY_SCOPE_AGENT); }
DI void st_tag(unsigned char* a, u64 v) { __hip_atomic_store((GAS u64*)a, v, __ATOMIC_RELAXED, __HIP_MEMORY_SCOPE_AGENT); }
DI u32x4 lds_r16(ldsp p) { return *(LAS u32x4*)p; }
DI f32x4 lds_rf4(ldsp p) { return *(LAS f32x4*)p; }
DI void lds_w16(ldsp p, u32x4 v) { *(LAS u32x4*)p = v; }
DI void lds_wf4(ldsp p, f32x4 v) { *(LAS f32x4*)p = v; }
DI unsigned umax3(unsigned a, unsigned b, unsigned c) { return max(max(a, b), c); }
DI unsigned smax(unsigned m, u32x4 v) { return umax3(umax3(m, v[0], v[1]), v[2], v[3]); }
DI void glds16_sc1(const void* g, unsigned lds_base) {
  unsigned sv;
  asm volatile("s_mov_b32 %0, m0\n\ts_mov_b32 m0, %2\n\ts_nop 0\n\tglobal_load_lds_dwordx4 %1, off sc1\n\ts_mov_b32 m0, %0" : "=&s"(sv) : "v"(g), "s"(lds_base) : "memory");
}
DI void glds16(const void* g, unsigned lds_base) {
  unsigned sv;
  asm volatile("s_mov_b32 %0, m0\n\ts_mov_b32 m0, %2\n\ts_nop 0\n\tglobal_load_lds_dwordx4 %1, off\n\ts_mov_b32 m0, %0" : "=&s"(sv) : "v"(g), "s"(lds_base) : "memory");
}
DI u32x4 pack8(const f32x16& a, int s) {
  u32x4 r;
#pragma unroll
  for (int i = 0; i < 4; ++i) r[i] = pk(a[8 * s + 2 * i], a[8 * s + 2 * i + 1]);
  return r;
}
template <int N> DI void ld_handoff(u32x4 (&v)[N], rsrc_t rs, int voff, int soff) {
#pragma unroll
  for (int i = 0; i < N; ++i) { v[i] = ld16<16>(rs, voff + (i & 3) * KB, soff + (i >> 2) * 4 * KB); SBAR(); }
}
template <int N> DI bool any_sent(const u32x4 (&v)[N]) {
  unsigned m = 0;
#pragma unroll
  for (int i = 0; i < N; ++i) m = smax(m, v[i]);
  return __builtin_amdgcn_ballot_w64(m == SENT) != 0ull;
}
template <int N> DI void chk_handoff(u32x4 (&v)[N], rsrc_t rs, int voff, int soff) {
  if (__builtin_expect(any_sent<N>(v), 0)) {
#pragma unroll 1
    for (int spin = 0; spin < SPIN_CAP; ++spin) {
      CBAR();
#pragma unroll
      for (int i = 0; i < N; ++i)
        if (__builtin_amdgcn_ballot_w64(smax(0u, v[i]) == SENT) != 0ull) v[i] = ld16<16>(rs, voff + (i & 3) * KB, soff + (i >> 2) * 4 * KB);
      if (!any_sent<N>(v)) break;
    }
  }
}

struct Params {
  const unsigned short *x, *wq, *wk, *wv, *woa, *wqkv, *wob;
  unsigned short* out;
  unsigned char* ws;
};

constexpr int L_SCR = 0;
constexpr int L_WB = 64 * KB;
constexpr int L_QF = 148 * KB;
constexpr int L_MISC = L_QF + 8 * KB;
constexpr int M_SIG = L_MISC;
constexpr int M_ST = M_SIG + 768;
constexpr int M_CS = M_ST + 1280;
constexpr int L_BYTES = M_CS + 1024;
constexpr int XI_KS = 1056;
constexpr int P_KV = 0, P_KVW = 112 * 56, P_XI = 25600, P_WQ = P_XI + 28 * XI_KS;
static_assert(L_BYTES <= 160 * KB && P_WQ + 448 * 224 + 64 <= L_MISC, "LDS");

DI void exp_pack(const f32x16& s, u32x4& p0, u32x4& p1) {
#pragma unroll
  for (int i = 0; i < 4; ++i) p0[i] = pk(__builtin_amdgcn_exp2f(s[2 * i]), __builtin_amdgcn_exp2f(s[2 * i + 1]));
#pragma unroll
  for (int i = 0; i < 4; ++i) p1[i] = pk(__builtin_amdgcn_exp2f(s[8 + 2 * i]), __builtin_amdgcn_exp2f(s[8 + 2 * i + 1]));
}
DI void set_shift(u32x4& q1, float m, int h, unsigned hi31) {
  const unsigned a = pk(m, m); const float r1 = m - bflo(a);
  const unsigned c = pk(r1, r1); const float r2 = r1 - bflo(c);
  const unsigned e = pk(r2, 0.f);
  const unsigned d2 = ((a & 0xFFFFu) | (c << 16)) ^ 0x80008000u, d3 = (e ^ 0x00008000u) | hi31;
  q1[2] = h ? d2 : q1[2]; q1[3] = h ? d3 : q1[3];
}
#define PIN(x) asm volatile("" : "+v"(x))
template <bool DOQK, bool DOPVP, class FA, class FB, class FC>
DI void unit(const f32x16& s, f32x16& sn, const f32x16& cn, f32x16& o, f32x16& op, u32x4& pp, const u32x4& kn0, const u32x4& kn1, const u32x4& qn0,
             const u32x4& qn1, const u32x4& v0, const u32x4& v1p, FA ha, FB hb, FC hc) {
  float e[16]; u32x4 p0;
#define E_(i) e[i] = __builtin_amdgcn_exp2f(s[i]); PIN(e[i]); SBAR()
#define C0_(i) p0[i] = pkt(e[2 * i], e[2 * i + 1]); PIN(p0[i]); SBAR()
#define C1_(i) pp[i] = pkt(e[8 + 2 * i], e[8 + 2 * i + 1]); PIN(pp[i]); SBAR()
  SBAR();
  if (DOQK) { sn = MFMA(kn0, qn0, cn); SBAR(); }
  E_(0); E_(1); E_(2); E_(3);
  if (DOPVP) { op = MFMA(v1p, pp, op); SBAR(); }
  ha(); SBAR();
  E_(4); C0_(0); E_(5); C0_(1); E_(6); E_(7);
  if (DOQK) { sn = MFMA(kn1, qn1, sn); SBAR(); }
  hb(); SBAR();
  E_(8); C0_(2); E_(9); C0_(3); E_(10); E_(11);
  o = MFMA(v0, p0, o); SBAR();
  hc(); SBAR();
  E_(12); C1_(0); E_(13); C1_(1); E_(14); E_(15); C1_(2); C1_(3);
}

struct Ctx { unsigned char* flg; ldsp sig; int lane; int w; };
DI unsigned now32() { return (unsigned)__builtin_amdgcn_s_memrealtime(); }
struct Clk { unsigned c, r; };
DI Clk clk_now() { Clk k; k.c = (unsigned)__builtin_amdgcn_s_memtime(); k.r = now32(); return k; }
DI unsigned eta(const Clk& k0, int dcyc) {
  const Clk k = clk_now();
  return (k.r + (unsigned)((float)dcyc * (float)(k.r - k0.r) * __builtin_amdgcn_rcpf((float)(k.c - k0.c)))) | 1u;
}
DI void post(const Ctx& c, int b, int slot, const Clk& k0, int dcyc) {
  const unsigned t = eta(k0, dcyc);
  if (c.lane == 0) __hip_atomic_store((GAS unsigned*)(c.flg + b * 128 + slot * 4), t, __ATOMIC_RELAXED, __HIP_MEMORY_SCOPE_AGENT);
}
DI unsigned flag_ld(const Ctx& c, int pb, int slot) { return __hip_atomic_load((GAS unsigned*)(c.flg + pb * 128 + slot * 4), __ATOMIC_RELAXED, __HIP_MEMORY_SCOPE_AGENT); }
template <int SLP = 1> DI void poll_wait(const Ctx& c, int pb, int slot, unsigned f = 0u) {
#pragma unroll 1
  for (int spin = 0; spin < SPIN_CAP; ++spin) {
    if (__builtin_amdgcn_ballot_w64(f == 0u) == 0ull) break;
    if (spin) __builtin_amdgcn_s_sleep(SLP);
    f = __hip_atomic_load((GAS unsigned*)(c.flg + pb * 128 + slot * 4), __ATOMIC_RELAXED, __HIP_MEMORY_SCOPE_AGENT);
  }
  unsigned t = __builtin_amdgcn_readlane(f, 0);
#pragma unroll
  for (int i = 1; i < 4; ++i) { const unsigned u = __builtin_amdgcn_readlane(f, i); t = (int)(u - t) > 0 ? u : t; }
#pragma unroll 1
  for (int spin = 0; spin < 256; ++spin) {
    const int d = (int)(t - now32());
    if (d <= 0) break;
    if (d > 30) __builtin_amdgcn_s_sleep(6); else __builtin_amdgcn_s_sleep(1);
  }
  CBAR();
}
DI void lds_sig(const Ctx& c, int idx) {
  CBAR();
  *(volatile LAS unsigned*)(c.sig + idx * 16 + c.w * 4) = 1u;
  CBAR();
}
template <int SLP> DI void lds_wait(const Ctx& c, int idx, unsigned n) {
#pragma unroll 1
  for (int spin = 0; spin < (SPIN_CAP << 6); ++spin) {
    const u32x4 v = *(volatile LAS u32x4*)(c.sig + idx * 16);
    if (__builtin_amdgcn_readfirstlane(v[0] + v[1] + v[2] + v[3]) >= n) break;
    if (SLP) __builtin_amdgcn_s_sleep(SLP);
  }
  CBAR();
}
template <int N, bool SWAP, class F> DI f32x16 gemm_lds(const u32x4 (&wr)[N], ldsp bb, F mid) {
  f32x16 acc = {};
  u32x4 bf[8];
#pragma unroll
  for (int i = 0; i < 8; ++i) bf[i] = lds_r16(bb + i * KB);
  SBAR();
#pragma unroll
  for (int i = 0; i < N; ++i) {
    acc = SWAP ? MFMA(bf[i & 7], wr[i], acc) : MFMA(wr[i], bf[i & 7], acc);
    SBAR();
    if (i + 8 < N) bf[i & 7] = lds_r16(bb + (i + 8) * KB);
    mid(i);
    SBAR();
  }
  return acc;
}
DI void add_res(const f32x16& a, const u32x4& x0, const u32x4& x1, u32x4& y0, u32x4& y1) {
#pragma unroll
  for (int i = 0; i < 4; ++i) {
    y0[i] = pk(a[2 * i] + bflo(x0[i]), a[2 * i + 1] + bfhi(x0[i]));
    y1[i] = pk(a[8 + 2 * i] + bflo(x1[i]), a[8 + 2 * i + 1] + bfhi(x1[i]));
  }
}
DI float row_stats(const u32x4& y0, const u32x4& y1, int h) {
  float s0 = 0.f, s1 = 0.f, q0 = 0.f, q1 = 0.f;
#pragma unroll
  for (int i = 0; i < 4; ++i) { s0 = dot2(y0[i], ONES2, s0); s1 = dot2(y1[i], ONES2, s1); q0 = dot2(y0[i], y0[i], q0); q1 = dot2(y1[i], y1[i], q1); }
  const float s = xhalf_add(s0 + s1), q = xhalf_add(q0 + q1);
  return h ? q : s;
}
DI void stats_finish(float v, float& mu, float& rs) {
  auto t = __builtin_amdgcn_permlane32_swap(__float_as_uint(v), __float_as_uint(v), false, false);
  mu = __uint_as_float(t[0]) * (1.f / D_);
  rs = __builtin_amdgcn_rsqf(__builtin_fmaxf(__uint_as_float(t[1]) * (1.f / D_) - mu * mu, 0.f) + EPS);
}
DI void stats_gather(rsrc_t ws, int so, ldsp dst, int lane) {
#pragma unroll 1
  for (int spin = 0; spin < SPIN_CAP; ++spin) {
    CBAR();
    unsigned sv[14], mx = 0;
#pragma unroll
    for (int i = 0; i < 14; ++i) sv[i] = ld4<16>(ws, lane * 4 + i * 256, so);
    float t0 = 0.f, t1 = 0.f, mu, rs;
#pragma unroll
    for (int i = 0; i < 14; i += 2) { t0 += __uint_as_float(sv[i]); t1 += __uint_as_float(sv[i + 1]); mx = umax3(mx, sv[i], sv[i + 1]); }
    stats_finish(t0 + t1, mu, rs);
    *(LAS float*)(dst + lane * 4) = lane >= 32 ? rs : mu;
    if (__builtin_amdgcn_ballot_w64(mx == SENT) == 0ull) break;
  }
}
DI void norm_pack(const f32x16& o, int h, u32x4& y0, u32x4& y1) {
  const float inv = __builtin_amdgcn_rcpf(from_hi(o[12]));
#pragma unroll
  for (int i = 0; i < 4; ++i) { y0[i] = pk(o[2 * i] * inv, o[2 * i + 1] * inv); y1[i] = pk(o[8 + 2 * i] * inv, o[8 + 2 * i + 1] * inv); }
  if (h == 1) { y1[2] = 0; y1[3] = 0; }
}
constexpr float LBIG = 1e24f;
constexpr unsigned NEG31 = 0xF1490000u;

DI u32x4 tr_frag(ldsp a, int hi) {
  const s16x4 lo_ = __builtin_amdgcn_ds_read_tr16_b64_v4i16((LAS s16x4*)a), hi_ = __builtin_amdgcn_ds_read_tr16_b64_v4i16((LAS s16x4*)(a + hi));
  return __builtin_bit_cast(u32x4, __builtin_shufflevector(lo_, hi_, 0, 1, 2, 3, 4, 5, 6, 7));
}
DI void part_write(ldsp d, const f32x16& a) {
#pragma unroll
  for (int g = 0; g < 4; ++g) lds_wf4(d + g * KB, f32x4{a[4 * g], a[4 * g + 1], a[4 * g + 2], a[4 * g + 3]});
}
template <bool LN>
DI void kv_finish(ldsp part, int pstr, int isV, int s, ldsp st, ldsp cs, int lane, rsrc_t ws, int kvo, unsigned char* fkv, u64 tag) {
  const int r = lane & 31, h = lane >> 5;
  f32x4 a[2][4], c[2] = {}, m[2] = {};
  float l0 = 0.f, l1 = 0.f;
#pragma unroll
  for (int k = 0; k < 4; ++k) { a[0][k] = lds_rf4(part + k * pstr + 2 * s * KB + lane * 16); a[1][k] = lds_rf4(part + k * pstr + (2 * s + 1) * KB + lane * 16); }
  if (LN) {
    l0 = *(LAS float*)((isV ? cs : st) + r * 4); l1 = *(LAS float*)(st + 128 + r * 4);
#pragma unroll
    for (int g = 0; g < 2; ++g) { c[g] = lds_rf4((isV ? st : cs) + (16 * s + 8 * g + 4 * h) * 4); m[g] = lds_rf4(st + 128 + (16 * s + 8 * g + 4 * h) * 4); }
  }
  SBAR();
  float v[8];
#pragma unroll
  for (int i = 0; i < 8; ++i) v[i] = (a[i >> 2][0][i & 3] + a[i >> 2][1][i & 3]) + (a[i >> 2][2][i & 3] + a[i >> 2][3][i & 3]);
  if (LN) {
#pragma unroll
    for (int i = 0; i < 8; ++i) v[i] = (v[i] - l0 * c[i >> 2][i & 3]) * (isV ? m[i >> 2][i & 3] : l1);
  }
  u32x4 o = {pk(v[0], v[1]), pk(v[2], v[3]), pk(v[4], v[5]), pk(v[6], v[7])};
  if (isV) { if (r >= 28) { const unsigned f = r == 28 ? ONES2 : 0u; o = u32x4{f, f, f, f}; } }
  else if (s == 1 && h == 1) { o[2] = ONES2; o[3] = 0x00003F80u; }
  st16<16>(o, ws, lane * 16, kvo + (2 * isV + s) * KB);
  asm volatile("s_waitcnt vmcnt(0)" ::: "memory");
  if (lane < 8) st_tag(fkv + lane * 64 * 128 + (2 * isV + s) * 8, tag);
}

DI void phase0_h(const Params& p, const ldsp lds, u64 epo, u64& tag) {
  const int tid = threadIdx.x & 255, lane = tid & 63, r = lane & 31, h = lane >> 5;
  const int w = __builtin_amdgcn_readfirstlane(tid >> 6);
  const int b = blockIdx.x, xcd = b & 7, jj = b >> 3, rt = 8 * xcd + (jj >> 2), sub = jj & 3;
  const rsrc_t ws = mkrs(p.ws, WS_BYTES);
  const Ctx cx = {p.ws, lds + M_SIG + 512, lane, w};
  const ldsp priv = lds + P_KV + w * P_KVW;
  if (sub < 2) {
    const unsigned char* src = (const unsigned char*)(sub ? p.wv : p.wk) + w * P_KVW + lane * 16;
#pragma unroll
    for (int i = 0; i < 7; ++i) if (i < 6 || lane < 8) glds16(src + i * KB, (unsigned)(uintptr_t)priv + i * KB);
  }
  const int q8 = tid & 7, row8 = tid >> 3;
  u32x4 xv[7];
  {
    const unsigned short* xr = p.x + (size_t)(rt * 32 + row8) * D_ + q8 * 8;
#pragma unroll
    for (int i = 0; i < 7; ++i) xv[i] = *(const u32x4*)(xr + i * 64);
  }
  SBAR();
  const int tt = b >> 2, tks = (b & 3) * 7 + 2 * w, wog = b * 4 + w;
  u32x2 tv[4];
  unsigned te[8];
  const bool dead = (lane & 7) == 7;
  {
    const int pitch = tt < 16 ? 448 : 1344;
    const unsigned short* src = (tt < 16 ? p.wq + 28 * tt : p.wqkv + ((tt - 16) % 3) * 448 + ((tt - 16) / 3) * 28) + (size_t)(tks * 16 + (lane >> 3)) * pitch + (dead ? 0 : (lane & 7) * 4);
#pragma unroll
    for (int i = 0; i < 4; ++i) tv[i] = *(const u32x2*)(src + (size_t)((w == 3 && i >= 2) ? 0 : i * 8) * pitch);
    int g = wog < 896 ? wog : 0;
    const unsigned short* wo = g >= 448 ? p.wob : p.woa;
    if (g >= 448) g -= 448;
    const int ct = g >> 5, ks = g & 31, head = ks >> 1;
    const int k0 = head * 28 + 16 * (ks & 1) + 4 * h, kmax = head * 28 + 28;
#pragma unroll
    for (int j = 0; j < 8; ++j) {
      const int k = k0 + 8 * (j >> 2) + (j & 3);
      te[j] = wo[(k < kmax ? k : 0) * 448 + 32 * ct + r];
    }
  }
  SBAR();
  {
    float sm[2] = {}, sq[2] = {};
#pragma unroll
    for (int i = 0; i < 7; ++i)
#pragma unroll
      for (int d = 0; d < 4; ++d) { sm[d & 1] = dot2(xv[i][d], ONES2, sm[d & 1]); sq[d & 1] = dot2(xv[i][d], xv[i][d], sq[d & 1]); }
    float a = sm[0] + sm[1], c = sq[0] + sq[1];
#define DPP_ADD(v, ctl) v += __uint_as_float(__builtin_amdgcn_update_dpp(0u, __float_as_uint(v), ctl, 0xF, 0xF, true))
    DPP_ADD(a, 0xB1); DPP_ADD(c, 0xB1); DPP_ADD(a, 0x4E); DPP_ADD(c, 0x4E); DPP_ADD(a, 0x141); DPP_ADD(c, 0x141);
    const float mu = a * (1.f / D_), rs = __builtin_amdgcn_rsqf(__builtin_fmaxf(c * (1.f / D_) - mu * mu, 0.f) + EPS), nb = -mu * rs;
#pragma unroll
    for (int i = 0; i < 7; ++i) {
      unsigned y[4];
#pragma unroll
      for (int d = 0; d < 4; ++d) y[d] = pk(__builtin_fmaf(bflo(xv[i][d]), rs, nb), __builtin_fmaf(bfhi(xv[i][d]), rs, nb));
      const ldsp dst = lds + P_XI + (4 * i + (q8 >> 1)) * XI_KS + row8 * 16 + (q8 & 1) * 8;
      *(LAS u32x2*)dst = u32x2{y[0], y[1]};
      *(LAS u32x2*)(dst + 512) = u32x2{y[2], y[3]};
    }
  }
  lds_sig(cx, 0);
  tag = (((u64)__builtin_amdgcn_readfirstlane((unsigned)(epo >> 32)) << 32) | __builtin_amdgcn_readfirstlane((unsigned)epo)) + 1ull;
  const int i16 = lane & 15, trow = 4 * h + (i16 >> 2), tcol = ((lane >> 4) & 1) * 32 + (i16 & 3) * 8;
  if (sub < 2) {
    u32x4 wf[7];
#pragma unroll
    for (int i = 0; i < 7; ++i) wf[i] = tr_frag(priv + (16 * i + trow) * 56 + tcol, 8 * 56);
    lds_wait<0>(cx, 0, 4u);
    f32x16 acc = {};
#pragma unroll
    for (int i = 0; i < 7; ++i) { const u32x4 xf = lds_r16(lds + P_XI + (7 * w + i) * XI_KS + lane * 16); acc = sub ? MFMA(xf, wf[i], acc) : MFMA(wf[i], xf, acc); }
    part_write(priv + lane * 16, acc);
    lds_sig(cx, 1);
    if (w < 2) {
      lds_wait<0>(cx, 1, 4u);
      kv_finish<false>(lds + P_KV, P_KVW, sub, w, (ldsp)0, (ldsp)0, lane, ws, OFF_PAIR + R_KVA + rt * 4 * KB, p.ws + OFF_PAIR + R_FKV + rt * 128, tag);
    }
    if (rt == 0) {
      float cs = 0.f;
#pragma unroll
      for (int i = 0; i < 7; ++i) {
        st16<16>(wf[i], ws, lane * 16, OFF_WKV + (sub * 28 + 7 * w + i) * KB);
#pragma unroll
        for (int d = 0; d < 4; ++d) cs = dot2(wf[i][d], ONES2, cs);
      }
      cs = xhalf_add(cs);
      st4<16>(h ? 0u : __float_as_uint(cs), ws, lane * 4, OFF_CS + ((64 + sub) * 16 + 4 * w) * 128);
      st4<16>(0u, ws, lane * 4 + 256, OFF_CS + ((64 + sub) * 16 + 4 * w) * 128);
    }
  }
  {
    st4<16>(0u, ws, w == 0 ? h * PAIR_SZ + r * 4 : WS_BYTES, OFF_PAIR + R_FLG + b * 128);
    const ldsp img = priv + 4 * KB;
#pragma unroll
    for (int i = 0; i < 4; ++i) *(LAS u32x2*)(img + i * 512 + lane * 8) = dead ? u32x2{0u, 0u} : tv[i];
    CBAR();
    float cs = 0.f;
#pragma unroll
    for (int ks = 0; ks < 2; ++ks) {
      const u32x4 o = tr_frag(img + ks * KB + trow * 64 + tcol, 512);
      if (ks == 0 || w < 3) {
        st16<16>(o, ws, lane * 16, (tt * 28 + tks + ks) * KB);
#pragma unroll
        for (int i = 0; i < 4; ++i) cs = dot2(o[i], ONES2, cs);
      }
    }
    u32x4 o;
#pragma unroll
    for (int i = 0; i < 4; ++i) { asm volatile("" : "+v"(te[2 * i]), "+v"(te[2 * i + 1])); o[i] = te[2 * i] | (te[2 * i + 1] << 16); }
    if ((wog & 1) && h == 1) { o[2] = 0u; o[3] = 0u; }
    if (wog < 896) st16<16>(o, ws, lane * 16, OFF_WOA + wog * KB);
    cs = xhalf_add(cs);
    if (h == 0) st4<16>(__float_as_uint(cs), ws, r * 4, OFF_CS + wog * 128);
    asm volatile("s_waitcnt vmcnt(0)" ::: "memory");
    unsigned k = 0;
    if (lane == 0) k = __hip_atomic_fetch_add((LAS unsigned*)(cx.sig + 2 * 16), 1u, __ATOMIC_RELAXED, __HIP_MEMORY_SCOPE_WORKGROUP);
    if (__builtin_amdgcn_readfirstlane(k) == 3u && lane < 8) st_tag(p.ws + OFF_INI + (lane * 256 + b) * 8, tag);
  }
}
DI void phase0_c(const Params& p, const ldsp lds, u64 epo, u64& tag, u32x4& q0, u32x4& q1) {
  const int lane = threadIdx.x & 63, h = lane >> 5;
  const int w = __builtin_amdgcn_readfirstlane(threadIdx.x >> 6), sub = (blockIdx.x >> 3) & 3;
  const Ctx cx = {p.ws, lds + M_SIG + 512, lane, w};
  __builtin_amdgcn_s_sleep(PRO_DLY);
  if (lane < 56) {
    const unsigned short* src = p.wq + (size_t)(4 * w + lane / 14) * 448 + sub * 112 + (lane % 14) * 8;
#pragma unroll 4
    for (int i = 0; i < 28; ++i) glds16(src + (size_t)i * 16 * 448, (unsigned)(uintptr_t)(lds + P_WQ) + (i * 16 + 4 * w) * 224);
  }
  asm volatile("s_waitcnt vmcnt(0)" ::: "memory");
  tag = (((u64)__builtin_amdgcn_readfirstlane((unsigned)(epo >> 32)) << 32) | __builtin_amdgcn_readfirstlane((unsigned)epo)) + 1ull;
  lds_sig(cx, 3);
  lds_wait<1>(cx, 3, 4u); lds_wait<1>(cx, 0, 4u);
  const int i16 = lane & 15;
  const ldsp wb0 = opaque(lds + P_WQ + (4 * h + (i16 >> 2)) * 224 + w * 56 + ((lane >> 4) & 1) * 32 + (i16 & 3) * 8), wb1 = opaque(wb0 + 14 * 16 * 224);
  const ldsp xb = lds + P_XI + lane * 16;
  u32x4 wr[4], xr[4];
#define PRE_LD(i) do { wr[(i) & 3] = tr_frag(((i) < 14 ? wb0 : wb1) + ((i) % 14) * 16 * 224, 8 * 224); xr[(i) & 3] = lds_r16(xb + (i) * XI_KS); } while (0)
  PRE_LD(0); PRE_LD(1); PRE_LD(2); SBAR();
  f32x16 acc = {};
#pragma unroll
  for (int i = 0; i < 28; ++i) {
    if (i + 3 < 28) PRE_LD(i + 3);
    SBAR();
    acc = MFMA(wr[i & 3], xr[i & 3], acc);
    SBAR();
  }
  lds_sig(cx, 4);
#pragma unroll
  for (int i = 0; i < 16; ++i) acc[i] *= C2;
  q0 = pack8(acc, 0); q1 = pack8(acc, 1);
  lds_wait<0>(cx, 4, 4u);
}

__global__ void __launch_bounds__(512, 2) k_main(Params p) {
  __shared__ __attribute__((aligned(16))) char smem[L_BYTES];
  const ldsp lds = (ldsp)smem;
  const int tid = threadIdx.x;
  const int w8 = __builtin_amdgcn_readfirstlane(tid >> 6);
  const rsrc_t wt = mkrs(p.ws, OFF_PAIR);
  auto nop = []() {};
  const f32x16 Z = {};
  const u64 epo = ld_tag(p.ws + OFF_EPO);
  u64 tag;
  if (tid < 192) *(LAS unsigned*)(lds + M_SIG + tid * 4) = 0u;
  if (CODE_KB && w8 == 3) {
    const unsigned char* pc = (const unsigned char*)(__builtin_amdgcn_s_getpc() & ~127ull);
    const int ln = (((blockIdx.x >> 3) * 64 + (tid & 63)) % (CODE_KB * 8)) * 128;
    glds16(pc + ln, (unsigned)(uintptr_t)(lds + M_ST));
  }
  BAR();
#define PAIR_SETUP() \
    int lane = tid & 63, b = blockIdx.x, w = w8 & 3; \
    asm volatile("" : "+v"(lane), "+s"(b), "+s"(w)); \
    lane &= 63; b &= 255; w &= 3; \
    const int r = lane & 31, h = lane >> 5, l16 = lane * 16, xcd = b & 7, jj = b >> 3; \
    const int rt = 8 * xcd + (jj >> 2), sub = jj & 3;          \
    const int rb = 2 * xcd + (jj >> 4), hd = jj & 15;          \
    const ldsp scr = lds + L_SCR + l16; \
    unsigned char* const reg = p.ws + OFF_PAIR + pair * PAIR_SZ; \
    const rsrc_t ws = mkrs(reg, 2 * PAIR_SZ - pair * PAIR_SZ); \
    const Ctx cx = {reg + R_FLG, lds + M_SIG + pair * 256, lane, w}

  if (w8 < 4) {
    u32x4 qn0, qn1;
    unsigned kve = 0u;
    phase0_c(p, lds, epo, tag, qn0, qn1);
#pragma unroll 1
   for (int pair = 0; pair < 2; ++pair) {
    PAIR_SETUP();
    const unsigned char* const kpa = reg + R_FKV + (xcd * 64 + 4 * ((2 * xcd + 1 - (lane >> 2)) & 15) + w) * 128 + (lane & 3) * 8;
    u64 miss = 0ull, kvf;
    unsigned chk = pair, pend = 1u;
    int kvi = 0;
    auto kv_mask = [&]() { miss = __builtin_amdgcn_ballot_w64(kvf != tag); };
    auto kv_next = [&]() -> int {
      if (__builtin_expect(((unsigned)(miss >> (4 * kvi)) & 15u) != 0u, 0)) {
#pragma unroll 1
        for (int spin = 0; spin < SPIN_CAP; ++spin) {
          kvf = ld_tag(kpa);
          kv_mask();
          if (((unsigned)(miss >> (4 * kvi)) & 15u) == 0u) break;
          __builtin_amdgcn_s_sleep(2);
        }
        CBAR();
      }
      const int o = R_KVA + (4 * ((2 * xcd + 1 - kvi) & 15) + w) * 4 * KB;
      ++kvi;
      return o;
    };
    {
      u32x4 q[4][2], K[4][2], V[4][2];
      int kb[4];
      if (pair) {
        poll_wait(cx, ((((4 * (lane & 1) + w) << 2) | ((lane >> 1) & 1)) << 3) | xcd, 12, kve);
      } else {
#pragma unroll 1
        for (int spin = 0; spin < SPIN_CAP; ++spin) {
          if (((unsigned)__builtin_amdgcn_ballot_w64(ld_tag(kpa) != tag) & 0xFFu) == 0u) break;
          __builtin_amdgcn_s_sleep(2);
        }
        CBAR();
      }
#pragma unroll
      for (int t = 0; t < 2; ++t) {
        kb[t] = kv_next();
        K[t][0] = ld16<16>(ws, l16, kb[t]); K[t][1] = ld16<16>(ws, l16 + KB, kb[t]);
        V[t][0] = ld16<16>(ws, l16 + 2 * KB, kb[t]); V[t][1] = ld16<16>(ws, l16 + 3 * KB, kb[t]);
        SBAR();
      }
#pragma unroll
      for (int t = 2; t < 4; ++t) { kb[t] = kb[0]; K[t][0] = K[t][1] = V[t][0] = V[t][1] = u32x4{}; }
      kvf = ld_tag(kpa);
      f32x16 o0 = {}, o1 = {}, o2 = {}, o3 = {};
      PIN(o0); PIN(o1); PIN(o2); PIN(o3);
      if (h == 1) { qn1[2] = 0u; qn1[3] = 0u; }
      lds_w16(lds + L_QF + l16 + 2 * w * KB, qn0);
      if (pair) chk_handoff<2>(K[0], ws, l16, kb[0]);
      {
        f32x16 s0 = MFMA(K[0][0], qn0, Z);
        s0 = MFMA(K[0][1], qn1, s0);
        set_shift(qn1, xhalf_max(max16(s0, s0[0])), h, 0u);
        lds_w16(lds + L_QF + l16 + (2 * w + 1) * KB, qn1);
        lds_sig(cx, 7);
      }
      if (pair) chk_handoff<2>(V[0], ws, l16 + 2 * KB, kb[0]);
      lds_wait<0>(cx, 7, 4u);
#pragma unroll
      for (int a = 0; a < 4; ++a) { q[a][0] = lds_r16(lds + L_QF + l16 + 2 * a * KB); q[a][1] = lds_r16(lds + L_QF + l16 + (2 * a + 1) * KB); }
      f32x16 sa = MFMA(K[0][0], q[0][0], Z), sb;
      sa = MFMA(K[0][1], q[0][1], sa);
      u32x4 pp = {};
#define TILE(c, LD, LDV1) { \
        constexpr int n = (c + 1) & 3, pv = (c + 3) & 3; \
        auto ldV1 = [&]() { if (LDV1) V[pv][1] = ld16<16>(ws, l16 + 3 * KB, kb[pv]); }; \
        auto ldK = [&]() { if (LD) { kb[c] = kv_next(); K[c][0] = ld16<16>(ws, l16, kb[c]); K[c][1] = ld16<16>(ws, l16 + KB, kb[c]); } }; \
        auto ldV0 = [&]() { if (LD) V[c][0] = ld16<16>(ws, l16 + 2 * KB, kb[c]); }; \
        unit<true, true>(sa, sb, Z, o0, o3, pp, K[c][0], K[c][1], q[1][0], q[1][1], V[c][0], V[pv][1], ldV1, nop, nop); \
        unit<true, true>(sb, sa, Z, o1, o0, pp, K[c][0], K[c][1], q[2][0], q[2][1], V[c][0], V[c][1], nop, nop, nop); \
        if (LD && c == 0 && __builtin_expect(pend != 0u, 0)) {        \
          pend = 0u; kv_mask(); \
          _Pragma("unroll") for (int t = 2; t < 4; ++t) { kb[t] = kv_next(); K[t][0] = ld16<16>(ws, l16, kb[t]); K[t][1] = ld16<16>(ws, l16 + KB, kb[t]); \
            V[t][0] = ld16<16>(ws, l16 + 2 * KB, kb[t]); V[t][1] = ld16<16>(ws, l16 + 3 * KB, kb[t]); } } \
        unit<true, true>(sa, sb, Z, o2, o1, pp, K[c][0], K[c][1], q[3][0], q[3][1], V[c][0], V[c][1], nop, ldK, nop); \
        if (LD && c == 0 && __builtin_expect(chk != 0u, 0)) { chk = 0u; chk_handoff<2>(K[n], ws, l16, kb[n]); chk_handoff<2>(V[n], ws, l16 + 2 * KB, kb[n]); } \
        unit<true, true>(sb, sa, Z, o3, o2, pp, K[n][0], K[n][1], q[0][0], q[0][1], V[c][0], V[c][1], nop, nop, ldV0); }
#pragma unroll 1
      for (int tt = 0; tt < 12; tt += 4) {
        kv_mask(); if (!FSKIP || miss) kvf = ld_tag(kpa);
        TILE(0, true, true) TILE(1, true, true)
        kv_mask(); if (!FSKIP || miss) kvf = ld_tag(kpa);
        TILE(2, true, true) TILE(3, true, true)
      }
      TILE(0, false, true)
      lds_sig(cx, 11);
      lds_wait<1>(cx, 8, 4u);
      TILE(1, false, false)
      const ldsp red = scr + w * 4 * KB;
#define WR_LO(o, a) [&]() { lds_wf4(red + (a) * 16 * KB, f32x4{o[0], o[1], o[2], o[3]}); lds_wf4(red + (a) * 16 * KB + KB, f32x4{o[4], o[5], o[6], o[7]}); }
#define WR_HI(o, a) [&]() { lds_wf4(red + (a) * 16 * KB + 2 * KB, f32x4{o[8], o[9], o[10], o[11]}); lds_wf4(red + (a) * 16 * KB + 3 * KB, f32x4{o[12], o[13], o[14], o[15]}); lds_sig(cx, a); }
      unit<true, true>(sa, sb, Z, o0, o3, pp, K[3][0], K[3][1], q[0][0], q[0][1], V[2][0], V[1][1], nop, nop, nop);
      unit<true, true>(sb, sa, Z, o0, o0, pp, K[2][0], K[2][1], q[1][0], q[1][1], V[3][0], V[2][1], nop, nop, nop);
      unit<true, true>(sa, sb, Z, o1, o0, pp, K[3][0], K[3][1], q[1][0], q[1][1], V[2][0], V[3][1], nop, WR_LO(o0, 0), WR_HI(o0, 0));
      unit<true, true>(sb, sa, Z, o1, o1, pp, K[2][0], K[2][1], q[2][0], q[2][1], V[3][0], V[2][1], nop, nop, nop);
      unit<true, true>(sa, sb, Z, o2, o1, pp, K[3][0], K[3][1], q[2][0], q[2][1], V[2][0], V[3][1], nop, WR_LO(o1, 1), WR_HI(o1, 1));
      unit<true, true>(sb, sa, Z, o2, o2, pp, K[2][0], K[2][1], q[3][0], q[3][1], V[3][0], V[2][1], nop, nop, nop);
      unit<true, true>(sa, sb, Z, o3, o2, pp, K[3][0], K[3][1], q[3][0], q[3][1], V[2][0], V[3][1], nop, WR_LO(o2, 2), WR_HI(o2, 2));
      unit<false, true>(sb, sa, Z, o3, o3, pp, K[2][0], K[2][1], q[3][0], q[3][1], V[3][0], V[2][1], nop, nop, nop);
      o3 = MFMA(V[3][1], pp, o3);
      WR_LO(o3, 3)(); WR_HI(o3, 3)();
    }
    {
      u32x4 of[8] = {};
      const int src = R_OFA + (rt * 32 + 8 * w) * KB;
      if (w != sub) {
        poll_wait(cx, (((jj & ~3) | w) << 3) | xcd, 0);
        ld_handoff<8>(of, ws, l16, src);
      }
      BAR();
      const unsigned bad = __builtin_amdgcn_readfirstlane(*(LAS unsigned*)(cx.sig + 10 * 16));
      if (__builtin_expect((bad >> w) & 1u, 0)) {
        const u32x4 q0 = lds_r16(lds + L_QF + l16 + 2 * w * KB);
        u32x4 q1 = lds_r16(lds + L_QF + l16 + (2 * w + 1) * KB);
        if (h == 1) { q1[2] = 0; q1[3] = 0; }
        float m = -INFINITY;
#pragma unroll 1
        for (int t = 0; t < 64; ++t) {
          f32x16 s = MFMA(ld16<16>(ws, l16, R_KVA + t * 4 * KB), q0, Z);
          s = MFMA(ld16<16>(ws, l16 + KB, R_KVA + t * 4 * KB), q1, s);
          m = max16(s, m);
        }
        set_shift(q1, xhalf_max(m), h, 0u);
        f32x16 o = {};
#pragma unroll 1
        for (int t = 0; t < 64; ++t) {
          f32x16 s = MFMA(ld16<16>(ws, l16, R_KVA + t * 4 * KB), q0, Z);
          s = MFMA(ld16<16>(ws, l16 + KB, R_KVA + t * 4 * KB), q1, s);
          u32x4 p0, p1;
          exp_pack(s, p0, p1);
          o = MFMA(ld16<16>(ws, l16 + 2 * KB, R_KVA + t * 4 * KB), p0, o);
          o = MFMA(ld16<16>(ws, l16 + 3 * KB, R_KVA + t * 4 * KB), p1, o);
        }
        u32x4 y0, y1;
        norm_pack(o, h, y0, y1);
        st16<16>(y0, ws, l16, R_OFA + (rt * 32 + sub * 8 + 2 * w) * KB);
        st16<16>(y1, ws, l16 + KB, R_OFA + (rt * 32 + sub * 8 + 2 * w) * KB);
        lds_w16(scr + (sub * 8 + 2 * w) * KB, y0); lds_w16(scr + (sub * 8 + 2 * w + 1) * KB, y1);
      }
      if (w != sub) {
        chk_handoff<8>(of, ws, l16, src);
#pragma unroll
        for (int i = 0; i < 8; ++i) lds_w16(scr + (8 * w + i) * KB, of[i]);
      }
      BAR();
    }
    f32x4 ck[4], cq[4];
    const float cv = *(LAS float*)(lds + M_CS + 6 * 128 + r * 4);
#pragma unroll
    for (int g = 0; g < 4; ++g) { cq[g] = lds_rf4(lds + M_CS + 4 * 128 + (8 * g + 4 * h) * 4); ck[g] = lds_rf4(lds + M_CS + 5 * 128 + (8 * g + 4 * h) * 4); }
    BAR();
    u32x4 q0, q1, k0, k1, v0, v1;
    const Clk ck0 = clk_now();
    {
      const int xo = R_XF1 + (rb * 4 + w) * 28 * KB;
      const ldsp wb0 = opaque(lds + L_WB + l16), wb1 = opaque(lds + L_WB + 28 * KB + l16);
      const ldsp st = lds + M_ST + w * 256;
      f32x4 m4[4], r4[4];
      poll_wait(cx, (((jj & ~15) | (w << 2) | (lane & 3)) << 3) | xcd, 1);
      f32x16 aq, ak, av;
      float mu, rs;
      u32x4 xf[28], wk[3], wv[3], wq[4];
#define LDX(i) do { xf[i] = ld16<16>(ws, l16 + ((i) & 3) * KB, xo + ((i) >> 2) * 4 * KB); SBAR(); } while (0)
#define CHKX(i) do { if (__builtin_expect(__builtin_amdgcn_ballot_w64(smax(0u, xf[i]) == SENT) != 0ull, 0)) { \
          _Pragma("unroll 1") for (int spin = 0; spin < SPIN_CAP; ++spin) { CBAR(); LDX(i); if (__builtin_amdgcn_ballot_w64(smax(0u, xf[i]) == SENT) == 0ull) break; } } SBAR(); } while (0)
#define QST(i) do { if ((i) < 28) { SBAR(); aq = MFMA(wq[(i) & 3], xf[(i) < 28 ? (i) : 0], (i) ? aq : Z); SBAR(); if ((i) + 3 < 28) wq[((i) + 3) & 3] = lds_r16(wb0 + ((i) + 3) * KB); SBAR(); } } while (0)
#pragma unroll
      for (int i = 0; i < X1_AHEAD; ++i) LDX(i);
#pragma unroll
      for (int i = 0; i < 2; ++i) { wk[i] = lds_r16(wb1 + i * KB); wv[i] = lds_r16(wb1 + (28 + i) * KB); }
#pragma unroll
      for (int i = 0; i < 3; ++i) wq[i] = lds_r16(wb0 + i * KB);
      SBAR();
      CHKX(0);
#pragma unroll
      for (int ks = 0; ks < 28; ++ks) {
        ak = MFMA(wk[ks % 3], xf[ks], ks ? ak : Z); SBAR();
        if (ks + 2 < 28) { wk[(ks + 2) % 3] = lds_r16(wb1 + (ks + 2) * KB); wv[(ks + 2) % 3] = lds_r16(wb1 + (28 + ks + 2) * KB); }
        if (X1_AHEAD + 2 * ks < 28) LDX(X1_AHEAD + 2 * ks);
        if (ks == 25) {
          lds_wait<0>(cx, 12 + w, 1u);
          mu = *(LAS float*)(st + r * 4); rs = *(LAS float*)(st + 128 + r * 4);
        }
        if (ks == 26) {
#pragma unroll
          for (int g = 0; g < 4; ++g) { m4[g] = lds_rf4(st + (8 * g + 4 * h) * 4); r4[g] = lds_rf4(st + 128 + (8 * g + 4 * h) * 4); }
        }
        SBAR();
        av = MFMA(xf[ks], wv[ks % 3], ks ? av : Z); SBAR();
        if (X1_AHEAD + 2 * ks + 1 < 28) LDX(X1_AHEAD + 2 * ks + 1);
        if (ks < QS) QST(ks);
        if (ks + 1 < 28) CHKX(ks + 1);
        if (ks == POST_KV) post(cx, b, 4 + w, ck0, DT_KV);
      }
      const int ko = R_KVB + (hd * 64 + rb * 4 + w) * 4 * KB;
      QST(QS); QST(QS + 1);
#pragma unroll
      for (int g = 0; g < 4; ++g) {
#pragma unroll
        for (int i = 4 * g; i < 4 * g + 4; ++i) ak[i] = (ak[i] - mu * ck[g][i & 3]) * rs;
        unsigned a0 = pk(ak[4 * g], ak[4 * g + 1]), a1 = pk(ak[4 * g + 2], ak[4 * g + 3]);
        if (g == 3 && h == 1) { a0 = ONES2; a1 = 0x00003F80u; }
        PIN(a0); PIN(a1);
        if (g < 2) { k0[2 * g] = a0; k0[2 * g + 1] = a1; } else { k1[2 * g - 4] = a0; k1[2 * g - 3] = a1; }
        if (g == 1) st16<16>(k0, ws, l16, ko);
        if (g == 3) st16<16>(k1, ws, l16 + KB, ko);
        QST(QS + 2 + g);
      }
      const unsigned vpad = r == 28 ? ONES2 : 0u;
#pragma unroll
      for (int g = 0; g < 4; ++g) {
#pragma unroll
        for (int i = 4 * g; i < 4 * g + 4; ++i) av[i] = (av[i] - m4[g][i & 3] * cv) * r4[g][i & 3];
        unsigned a0 = pk(av[4 * g], av[4 * g + 1]), a1 = pk(av[4 * g + 2], av[4 * g + 3]);
        if (r >= 28) { a0 = vpad; a1 = vpad; }
        PIN(a0); PIN(a1);
        if (g < 2) { v0[2 * g] = a0; v0[2 * g + 1] = a1; } else { v1[2 * g - 4] = a0; v1[2 * g - 3] = a1; }
        if (g == 1) st16<16>(v0, ws, l16 + 2 * KB, ko);
        if (g == 3) st16<16>(v1, ws, l16 + 3 * KB, ko);
        QST(QS + 6 + g);
      }
      lds_w16(scr + (32 + 4 * w) * KB, k0); QST(QS + 10); lds_w16(scr + (33 + 4 * w) * KB, k1); QST(QS + 11);
      lds_w16(scr + (34 + 4 * w) * KB, v0); QST(QS + 12); lds_w16(scr + (35 + 4 * w) * KB, v1);
      lds_sig(cx, 4);
#pragma unroll
      for (int i = QS + 13; i < 28; ++i) QST(i);
      const float rq = rs * C2;
#pragma unroll
      for (int i = 0; i < 16; ++i) aq[i] = (aq[i] - mu * cq[i >> 2][i & 3]) * rq;
      q0 = pack8(aq, 0); q1 = pack8(aq, 1);
      if (h == 1) { q1[2] = 0; q1[3] = NEG31; }
    }
    {
      f32x16 cd, cf;
#pragma unroll
      for (int i = 0; i < 16; ++i) {
        const int kk = (i & 3) + 8 * (i >> 2) + 4 * h;
        cd[i] = kk <= r ? 0.f : -1e30f; cf[i] = kk <= r ? -1e30f : 0.f;
      }
      f32x16 sa = MFMA(k0, q0, cd), sb;
      sa = MFMA(k1, q1, sa);
      set_shift(q1, xhalf_max(max16(sa, sa[0])), h, NEG31);
      const ldsp kp = scr + (8 + w) * 4 * KB;
      auto need = [&](int u) {
        const int ti = 8 + w - u;
        if (u == 1 && w > 0) lds_wait<0>(cx, 4, 4u);
        if (__builtin_expect((ti & 3) == 3, 0)) lds_wait<1>(cx, 5 + (ti >> 2), 2u);
      };
      u32x4 K[2][2], V[2][2], pp;
      f32x16 o = {};
      sa = MFMA(k0, q0, cd); sa = MFMA(k1, q1, sa);
      V[0][0] = v0; V[0][1] = v1;
      need(1);
      K[1][0] = lds_r16(kp - 4 * KB); K[1][1] = lds_r16(kp - 3 * KB); V[1][0] = lds_r16(kp - 2 * KB);
#pragma unroll
      for (int u = 0; u < 9; ++u) {
        const int c = u & 1, n = c ^ 1;
        auto ha = [&]() {
          if (u + 1 < 9) V[n][1] = lds_r16(kp - (u + 1) * 4 * KB + 3 * KB);
          if (u + 2 < 9) { need(u + 2); K[c][0] = lds_r16(kp - (u + 2) * 4 * KB); K[c][1] = lds_r16(kp - (u + 2) * 4 * KB + KB); }
        };
        auto hc = [&]() { if (u + 2 < 9) V[c][0] = lds_r16(kp - (u + 2) * 4 * KB + 2 * KB); if (u >= 4 && u <= 6 && __builtin_expect(u == (w < 2 ? 4 : 3 + w), 0)) post(cx, b, 8 + w, ck0, DT_OB + (8 - u) * DT_UNIT); };
        f32x16& s = c ? sb : sa; f32x16& sn = c ? sa : sb;
        if (u == 0) unit<true, false>(s, sn, Z, o, o, pp, K[n][0], K[n][1], q0, q1, V[c][0], V[n][1], ha, nop, hc);
        else if (u < 7) unit<true, true>(s, sn, Z, o, o, pp, K[n][0], K[n][1], q0, q1, V[c][0], V[n][1], ha, nop, hc);
        else if (u == 7) unit<true, true>(s, sn, cf, o, o, pp, K[n][0], K[n][1], q0, q1, V[c][0], V[n][1], ha, nop, hc);
        else unit<false, true>(s, sn, Z, o, o, pp, K[n][0], K[n][1], q0, q1, V[c][0], V[n][1], ha, nop, hc);
      }
      o = MFMA(V[0][1], pp, o);
      if (__builtin_expect(__builtin_amdgcn_ballot_w64(h == 1 && !(o[12] < LBIG)) != 0ull, 0)) {
        if (h == 1) { q1[2] = 0; q1[3] = NEG31; }
        float m = -INFINITY;
#pragma unroll 1
        for (int u = 0; u < 9; ++u) {
          f32x16 s;
#pragma unroll
          for (int i = 0; i < 16; ++i) s[i] = u == 0 ? cd[i] : u == 8 ? cf[i] : 0.f;
          s = MFMA(lds_r16(kp - u * 4 * KB), q0, s); s = MFMA(lds_r16(kp - u * 4 * KB + KB), q1, s);
          m = max16(s, m);
        }
        set_shift(q1, xhalf_max(m), h, NEG31);
        o = Z;
#pragma unroll 1
        for (int u = 0; u < 9; ++u) {
          f32x16 s;
#pragma unroll
          for (int i = 0; i < 16; ++i) s[i] = u == 0 ? cd[i] : u == 8 ? cf[i] : 0.f;
          s = MFMA(lds_r16(kp - u * 4 * KB), q0, s); s = MFMA(lds_r16(kp - u * 4 * KB + KB), q1, s);
          u32x4 p0, p1;
          exp_pack(s, p0, p1);
          o = MFMA(lds_r16(kp - u * 4 * KB + 2 * KB), p0, o); o = MFMA(lds_r16(kp - u * 4 * KB + 3 * KB), p1, o);
        }
      }
      u32x4 y0, y1;
      norm_pack(o, h, y0, y1);
      const int oo = R_OFB + ((rb * 4 + w) * 16 + hd) * 2 * KB;
      st16<16>(y0, ws, l16, oo); st16<16>(y1, ws, l16 + KB, oo);
      lds_sig(cx, 9);
    }
    u32x4 wa1[WA1];
    {
      u32x4 of[8];
      const int src = R_OFB + (rt * 32 + 8 * w) * KB;
#pragma unroll
      for (int i = 0; i < WA1; ++i) { wa1[i] = ld16<16>(wt, l16 + (i & 3) * KB, pair ? OFF_PAIR : OFF_WAT + ((4 * sub + w) * 28 + (i & ~3)) * KB); SBAR(); }
      poll_wait(cx, (((jj & ~15) | (w << 2) | (lane & 3)) << 3) | xcd, 8 + (rt & 3));
      ld_handoff<8>(of, ws, l16, src);
      chk_handoff<8>(of, ws, l16, src);
      lds_wait<1>(cx, 9, 4u);
#pragma unroll
      for (int i = 0; i < 8; ++i) lds_w16(scr + (8 * w + i) * KB, of[i]);
      BAR();
    }
    if (pair == 0) {
      u32x4 wa[28];
#pragma unroll
      for (int i = 0; i < WA1; ++i) wa[i] = wa1[i];
#pragma unroll
      for (int i = WA1; i < 28; ++i) { wa[i] = ld16<16>(wt, l16 + (i & 3) * KB, OFF_WAT + ((4 * sub + w) * 28 + (i & ~3)) * KB); SBAR(); }
      f32x4 cq[4];
#pragma unroll
      for (int g = 0; g < 4; ++g) cq[g] = lds_rf4(lds + M_CS + w * 128 + (8 * g + 4 * h) * 4);
      if (w >= 2) poll_wait<2>(cx, (((jj & ~3) | (lane & 3)) << 3) | xcd, 2);
      if (w == 3) stats_gather(ws, R_ST2 + rt * 14 * 256, lds + M_ST + 4 * 256, lane);
      if (w == 2 && sub < 2) {
        const unsigned t = eta(ck0, DT_Q);
        if (lane == 0) __hip_atomic_store((GAS unsigned*)(cx.flg + PAIR_SZ + b * 128 + 12 * 4), t, __ATOMIC_RELAXED, __HIP_MEMORY_SCOPE_AGENT);
      }
      BAR();
      if (sub < 2 && w < 2) __builtin_amdgcn_s_sleep(4);
      const float mu = *(LAS float*)(lds + M_ST + 4 * 256 + r * 4), rq = *(LAS float*)(lds + M_ST + 4 * 256 + 128 + r * 4) * C2;
      f32x16 acc = gemm_lds<28, false>(wa, scr + 32 * KB, [&](int i) { if (i == KVE_AT) kve = flag_ld(cx, PAIR_SZ / 128 + (((((4 * (lane & 1) + w) << 2) | ((lane >> 1) & 1)) << 3) | xcd), 12); });
#pragma unroll
      for (int i = 0; i < 16; ++i) acc[i] = (acc[i] - mu * cq[i >> 2][i & 3]) * rq;
      qn0 = pack8(acc, 0); qn1 = pack8(acc, 1);
    }
   }
   return;
  }

  __builtin_amdgcn_s_setprio(2);
  phase0_h(p, lds, epo, tag);
  u32x4 x0 = {}, x1 = {};
#pragma unroll 1
 for (int pair = 0; pair < 2; ++pair) {
  PAIR_SETUP();
  __builtin_amdgcn_s_setprio(HPRIO);
  const int ct0 = sub < 2 ? 4 * sub : 3 * sub + 2, nct = sub < 2 ? 4 : 3;
  const bool ctok = w < nct;
  const int ct = ct0 + (ctok ? w : 0);
  const int xo1 = R_XF1 + (rt * 28 + ct * 2) * KB, xo2 = R_XF2 + (rt * 28 + ct * 2) * KB;
  const int so1 = R_ST1 + (rt * 14 + ct) * 256, so2 = R_ST2 + (rt * 14 + ct) * 256;
  lds_wait<4>(cx, 7, 4u);
  __builtin_amdgcn_s_setprio(HPRIO);
  u32x4 wf[32];
  Clk ck0 = clk_now();
  {
    const u32x4 sv = {SENT, SENT, SENT, SENT};
    auto fill = [&](int i) {
      const int hi = (i & 1) * KB;
      if (i < 2) st16<16>(sv, ws, l16 + hi, R_OFA + (rt * 32 + sub * 8 + 2 * w) * KB);
      else if (i < 4) { if (ctok) { st16<16>(sv, ws, l16 + hi, xo1); if (i == 2) st4<16>(SENT, ws, lane * 4, so1); } }
      else if (i < 8) st16<16>(sv, ws, l16 + (i - 4) * KB, R_KVB + (hd * 64 + rb * 4 + w) * 4 * KB);
      else if (i < 10) st16<16>(sv, ws, l16 + hi, R_OFB + ((rb * 4 + w) * 16 + hd) * 2 * KB);
      else if (i < 12) { if (ctok && pair == 0) { st16<16>(sv, ws, l16 + hi, xo2); if (i == 10) st4<16>(SENT, ws, lane * 4, so2); } }
      else if (i == 12 && pair == 0 && sub < 2 && w < 2) st16<16>(sv, ws, l16, PAIR_SZ + R_KVA + (rt * 4 + 2 * sub + w) * KB);
      SBAR();
    };
#pragma unroll
    for (int i = 0; i < NFILL0; ++i) { fill(i); __builtin_amdgcn_s_sleep(PACEF); }
    if (pair == 0) {
#pragma unroll 1
      for (int spin = 0; spin < SPIN_CAP; ++spin) {
        bool ok = true;
#pragma unroll
        for (int i = 0; i < 4; ++i) ok &= ld_tag(p.ws + OFF_INI + (xcd * 256 + i * 64 + lane) * 8) == tag;
        if (__builtin_amdgcn_ballot_w64(!ok) == 0ull) break;
        __builtin_amdgcn_s_sleep(8);
      }
      CBAR();
    }
    unsigned c4[16] = {};
    if (pair == 0) {
      const unsigned short* xr = p.x + (size_t)(rt * 32 + r) * D_ + ct * 32 + 4 * h;
      const u32x2 a0 = *(const u32x2*)xr, a1 = *(const u32x2*)(xr + 8), a2 = *(const u32x2*)(xr + 16), a3 = *(const u32x2*)(xr + 24);
      x0 = u32x4{a0[0], a0[1], a1[0], a1[1]}; x1 = u32x4{a2[0], a2[1], a3[0], a3[1]};
      const int co = (h ? (w < 3 ? 16 + hd * 3 + w : 64 + (sub & 1)) : 4 * sub + w) * 2048 + r * 4;
#pragma unroll
      for (int i = 0; i < 16; ++i) c4[i] = ld4<16>(wt, co + i * 128, OFF_CS);
      SBAR();
    }
    const unsigned char* wbsrc = p.ws + OFF_WBT + (hd * 84 + w * 21) * KB + l16;
    const unsigned wbdst = (unsigned)(uintptr_t)(lds + L_WB) + w * 21 * KB;
#pragma unroll
    for (int i = 0; i < 32; ++i) {
      wf[i] = ld16<16>(wt, l16 + (i & 3) * KB, OFF_WOA + (ct * 32 + (i & ~3)) * KB); SBAR();
      if (i % 3 != 2 || i > 29) { const int g = i - (i + 1) / 3; if (g < 21 && pair == 0) glds16_sc1(wbsrc + g * KB, wbdst + g * KB); }
      if ((i & 1) == 0 && NFILL0 + (i >> 1) < 13) fill(NFILL0 + (i >> 1));
      if (i == 10) { asm volatile("s_waitcnt vmcnt(5)" ::: "memory"); lds_sig(cx, 8); }
      if (i % PGRP == PGRP - 1) { SBAR(); if (pair) __builtin_amdgcn_s_sleep(PACE3); else __builtin_amdgcn_s_sleep(PACE); SBAR(); }
    }
    if (pair == 0) {
      float t = 0.f;
#pragma unroll
      for (int i = 0; i < 16; ++i) t += __uint_as_float(c4[i]);
      *(LAS float*)(lds + M_CS + (4 * h + w) * 128 + r * 4) = t;
    }
    asm volatile("s_waitcnt vmcnt(0)" ::: "memory");
  }
  {
    u32x4 keep[2];
    const int s = w & 1;
    __builtin_amdgcn_s_setprio(2);
    lds_wait<2>(cx, 8, 4u);
#pragma unroll 1
    for (int spin = 0; spin < SPIN_CAP; ++spin) {
      if (__builtin_amdgcn_readfirstlane(*(volatile LAS unsigned*)(cx.sig + 11 * 16 + w * 4))) break;
      __builtin_amdgcn_s_sleep(DOZE);
    }
#pragma unroll
    for (int it = 0; it < 2; ++it) {
      const int T = 2 * it + (w >> 1);
      if (it == 0) lds_wait<2>(cx, T, 4u); else lds_wait<1>(cx, T, 4u);
      if (it == 0) { if (w == POST_OA) post(cx, b, 0, ck0, DT_OA); ck0 = clk_now(); }

      const ldsp red = scr + T * 16 * KB;
      f32x4 a0[4], a1[4]; float l4[4];
#pragma unroll
      for (int k = 0; k < 4; ++k) {
        l4[k] = *(LAS float*)(lds + L_SCR + T * 16 * KB + k * 4 * KB + 3 * KB + (32 + r) * 16);
        a0[k] = lds_rf4(red + k * 4 * KB + (2 * s) * KB);
        a1[k] = lds_rf4(red + k * 4 * KB + (2 * s + 1) * KB);
      }
      const float l = (l4[0] + l4[1]) + (l4[2] + l4[3]);
      const f32x4 b0 = (a0[0] + a0[1]) + (a0[2] + a0[3]), b1 = (a1[0] + a1[1]) + (a1[2] + a1[3]);
      const float inv = __builtin_amdgcn_rcpf(l);
      u32x4 o = {pk(b0[0] * inv, b0[1] * inv), pk(b0[2] * inv, b0[3] * inv), pk(b1[0] * inv, b1[1] * inv), pk(b1[2] * inv, b1[3] * inv)};
      if (s == 1 && h == 1) { o[2] = 0; o[3] = 0; }
      if (__builtin_expect(__builtin_amdgcn_ballot_w64(!(l < LBIG)) == 0ull, 1)) st16<16>(o, ws, l16, R_OFA + (rt * 32 + sub * 8 + 2 * T + s) * KB);
      else if (lane == 0) __hip_atomic_fetch_or((LAS unsigned*)(cx.sig + 10 * 16), 1u << T, __ATOMIC_RELAXED, __HIP_MEMORY_SCOPE_WORKGROUP);
      keep[it] = o;
    }
    BAR();
    const unsigned bad = __builtin_amdgcn_readfirstlane(*(LAS unsigned*)(cx.sig + 10 * 16));
#pragma unroll
    for (int it = 0; it < 2; ++it) {
      const int T = 2 * it + (w >> 1);
      if (!((bad >> T) & 1u)) lds_w16(scr + (sub * 8 + 2 * T + s) * KB, keep[it]);
    }
    BAR();
  }
  {
    const f32x16 acc = gemm_lds<32, false>(wf, scr, [&](int i) { if (i == 0 && w == 0) post(cx, b, 1, ck0, DT_X); if (i == 24) BAR();   });
    add_res(acc, x0, x1, x0, x1);
    if (ctok) { st16<16>(x0, ws, l16, xo1); st16<16>(x1, ws, l16 + KB, xo1); SBAR(); st4<16>(__float_as_uint(row_stats(x0, x1, h)), ws, lane * 4, so1); }
  }
  u32x4 wkv[7];
  {
    __builtin_amdgcn_s_setprio(0);
    poll_wait<2>(cx, (((jj & ~15) | (w << 2) | (lane & 3)) << 3) | xcd, 1);
    __builtin_amdgcn_s_sleep(ST_DLY);
    stats_gather(ws, R_ST1 + (rb * 4 + w) * 14 * 256, lds + M_ST + w * 256, lane);
    lds_sig(cx, 12 + w);
    const int kt = 4 * rb - 8 + 2 * w;
    u32x4 hv[8];
    if (kt >= 0) {
      const int prb = kt >> 2;
      __builtin_amdgcn_s_setprio(0);
      poll_wait<HALO_SLP>(cx, (((prb & 1) * 16 + hd) << 3) | (prb >> 1), 4 + (kt & 3) + (lane & 1));
      __builtin_amdgcn_s_setprio(2);
      ld_handoff<8>(hv, ws, l16, R_KVB + (hd * 64 + kt) * 4 * KB);
      chk_handoff<8>(hv, ws, l16, R_KVB + (hd * 64 + kt) * 4 * KB);
    } else {
#pragma unroll
      for (int i = 0; i < 8; ++i) hv[i] = u32x4{0u, 0u, 0u, ((i & 3) == 1 && h == 1) ? 0x3F800000u : 0u};
    }
#pragma unroll
    for (int i = 0; i < 8; ++i) lds_w16(scr + (8 * w + i) * KB, hv[i]);
    lds_sig(cx, 5 + (w >> 1));
    __builtin_amdgcn_s_setprio(0);
#pragma unroll
    for (int i = 0; i < 32; ++i) { wf[i] = ld16<16>(wt, l16 + (i & 3) * KB, OFF_WOB + (ct * 32 + (i & ~3)) * KB); SBAR(); }
#pragma unroll
    for (int i = 0; i < 7; ++i) wkv[i] = ld16<16>(wt, l16 + (i & 3) * KB, OFF_WKV + ((sub & 1) * 28 + 7 * w + (i & 4)) * KB);
    asm volatile("s_waitcnt vmcnt(0)" ::: "memory");
    BAR();
  }
  __builtin_amdgcn_s_setprio(2);
  {
    const f32x16 acc = gemm_lds<32, false>(wf, scr, [&](int i) { if (i == 0 && w == 0 && pair == 0) post(cx, b, 2, ck0, DT_X); if (i == 31 && pair == 0) lds_sig(cx, 32 + 5); });
    add_res(acc, x0, x1, x0, x1);
    if (pair == 1) {
      if (ctok) {
        unsigned short* orow = p.out + (size_t)(rt * 32 + r) * D_ + ct * 32 + 4 * h;
        *(u32x2*)orow = u32x2{x0[0], x0[1]}; *(u32x2*)(orow + 8) = u32x2{x0[2], x0[3]};
        *(u32x2*)(orow + 16) = u32x2{x1[0], x1[1]}; *(u32x2*)(orow + 24) = u32x2{x1[2], x1[3]};
      }
      if (b == 0 && w == 0 && lane == 0) st_tag(p.ws + OFF_EPO, tag);
      return;
    }
    if (ctok) { st16<16>(x0, ws, l16, xo2); st16<16>(x1, ws, l16 + KB, xo2); SBAR(); st4<16>(__float_as_uint(row_stats(x0, x1, h)), ws, lane * 4, so2); }
  }
  {
    u32x4 xf[7];
    const int src = R_XF2 + (rt * 28 + 7 * w) * KB;
    lds_wait<0>(cx, 32 + 5, 4u);
    poll_wait(cx, (((jj & ~3) | (lane & 3)) << 3) | xcd, 2);
    ld_handoff<7>(xf, ws, l16, src);
    chk_handoff<7>(xf, ws, l16, src);
    if (sub < 2) {
      f32x16 acc = {};
      if (sub) {
#pragma unroll
        for (int i = 0; i < 7; ++i) { acc = MFMA(xf[i], wkv[i], acc); SBAR(); lds_w16(scr + (32 + 7 * w + i) * KB, xf[i]); SBAR(); }
      } else {
#pragma unroll
        for (int i = 0; i < 7; ++i) { acc = MFMA(wkv[i], xf[i], acc); SBAR(); lds_w16(scr + (32 + 7 * w + i) * KB, xf[i]); SBAR(); }
      }
      part_write(scr + w * 4 * KB, acc);
    } else {
#pragma unroll
      for (int i = 0; i < 7; ++i) lds_w16(scr + (32 + 7 * w + i) * KB, xf[i]);
    }
    BAR();
    if (sub < 2 && w < 2) kv_finish<true>(lds + L_SCR, 4 * KB, sub, w, lds + M_ST + 4 * 256, lds + M_CS + 7 * 128, lane, ws, PAIR_SZ + R_KVA + rt * 4 * KB, reg + PAIR_SZ + R_FKV + rt * 128, tag);
  }
 }
}

extern "C" void kernel_launch(void* const* d_in, const int* in_sizes, int n_in, void* d_out, int out_size, void* d_ws, size_t ws_size,
                              hipStream_t stream) {
  Params p;
  p.x = (const unsigned short*)d_in[0];
  p.wq = (const unsigned short*)d_in[1];
  p.wk = (const unsigned short*)d_in[2];
  p.wv = (const unsigned short*)d_in[3];
  p.woa = (const unsigned short*)d_in[4];
  p.wqkv = (const unsigned short*)d_in[6];
  p.wob = (const unsigned short*)d_in[7];
  p.out = (unsigned short*)d_out;
  p.ws = (unsigned char*)d_ws;
  k_main<<<256, 512, 0, stream>>>(p);
}
```
